```python
import torch

def _softmax(x, axis=-1):
    return torch.softmax(x, dim=axis)
def _gelu(x):
    return torch.nn.functional.gelu(x, approximate="tanh")
def _silu(x):
    return torch.nn.functional.silu(x)
def _sigmoid(x):
    return torch.sigmoid(x)
def _layernorm(x, axis=-1, eps=1e-5):
    m = x.mean(dim=axis, keepdim=True)
    v = x.var(dim=axis, keepdim=True, unbiased=False)
    return (x - m) / torch.sqrt(v + eps)
def _scatter_add(tgt, idx, src, axis=0):
    return tgt.clone().index_add_(axis, idx, src)
def _segment_sum(src, seg, n):
    out = torch.zeros((n,) + src.shape[1:], dtype=src.dtype)
    return out.index_add_(0, seg, src)
def _pad(x, pw):
    flat = tuple(v for pair in reversed(pw) for v in pair)
    return torch.nn.functional.pad(x, flat)

kernel_name = 'gated_mix__mha_block__glu_gated_bilinear__prompt'

def setup_inputs():

    _w = (lambda a: (lambda t: t.to(torch.bfloat16) if t.is_floating_point() else t)(torch.as_tensor(a)))
    def _A_setup():
        B, S, D, H = 1, 2048, 576, 32
        return {
            "x": _w((lambda *s: torch.randn(*s, dtype=torch.bfloat16) * 0.05)(B, S, D)),
            "wqkv": _w((lambda *s: torch.randn(*s, dtype=torch.bfloat16) * 0.8)(D, 3 * D)),
            "wo": _w((lambda *s: torch.randn(*s, dtype=torch.bfloat16) * 0.05)(D, D)),
        }
    _A_in = _A_setup()

    def _B_setup():
        B, S, D, F = 1, 2048, 576, 4608
        return {
            "x": _w((lambda *s: torch.randn(*s, dtype=torch.bfloat16) * 0.05)(B, S, D)),
            "w_gate": _w((lambda *s: torch.randn(*s, dtype=torch.bfloat16) * 3.2)(D, F) * (D ** -0.5)),
            "w_up": _w((lambda *s: torch.randn(*s, dtype=torch.bfloat16) * 3.2)(D, F) * (D ** -0.5)),
            "w_down": _w((lambda *s: torch.randn(*s, dtype=torch.bfloat16) * 3.2)(F, D) * (F ** -0.5)),
        }
    _B_in = _B_setup()

    return {
        "A_x": _A_in["x"],
        "A_wqkv": _A_in["wqkv"],
        "A_wo": _A_in["wo"],
        "B_x": _B_in["x"],
        "B_w_gate": _B_in["w_gate"],
        "B_w_up": _B_in["w_up"],
        "B_w_down": _B_in["w_down"],
        "gate_w": _w((lambda *s: torch.randn(*s, dtype=torch.bfloat16) * 0.05)(576) * 16.0),
    }

def reference(A_x, A_wqkv, A_wo, B_x, B_w_gate, B_w_up, B_w_down, gate_w):
    def _A_ref(x, wqkv, wo):
        B, S, D = x.shape
        H = 32
        Dh = D // H
        qkv = (lambda x, *d: x.permute(*d))((x @ wqkv).reshape(B, S, 3, H, Dh), 2, 0, 3, 1, 4)
        q, k, v = qkv[0], qkv[1], qkv[2]
        scores = torch.einsum("bhqd,bhkd->bhqk", q, k) * (Dh ** -0.5)
        p = _softmax(scores, axis=-1)
        o = torch.einsum("bhqk,bhkd->bhqd", p, v)
        return (lambda x, *d: x.permute(*d))(o, 0, 2, 1, 3).reshape(B, S, D) @ wo

    def _B_ref(x, w_gate, w_up, w_down):
        g = torch.einsum("bsd,df->bsf", x, w_gate)
        u = torch.einsum("bsd,df->bsf", x, w_up)
        h = g * u
        return torch.einsum("bsf,fd->bsd", h, w_down)

    x = A_x
    g = _sigmoid((x * gate_w).sum(-1, keepdims=True))
    a = _A_ref(x, A_wqkv, A_wo)
    b = _B_ref(x, B_w_gate, B_w_up, B_w_down)
    return g * a + (1 - g) * b
```

```cpp
#include <hip/hip_runtime.h>
#include <hip/hip_bf16.h>

typedef __attribute__((ext_vector_type(8))) short bf16x8;
typedef __attribute__((ext_vector_type(4))) short s16x4;
typedef __attribute__((ext_vector_type(4))) float f32x4;
typedef __attribute__((ext_vector_type(2))) float f32x2;
typedef __attribute__((ext_vector_type(16))) float f32x16;
typedef __attribute__((ext_vector_type(4))) unsigned u32x4;
typedef __attribute__((ext_vector_type(2))) unsigned u32x2;
typedef __attribute__((ext_vector_type(2))) __bf16 bf16x2_t;
typedef unsigned short u16;

#define DI __device__ __forceinline__
#define LDSP(T) __attribute__((address_space(3))) T

constexpr int S_ = 2048, D_ = 576, H_ = 32, DH_ = 18, F_ = 4608, KA_ = D_ + F_;

DI unsigned pk2(float lo, float hi) {
  f32x2 v = {lo, hi};
  bf16x2_t b = __builtin_convertvector(v, bf16x2_t);
  return __builtin_bit_cast(unsigned, b);
}
DI float bf2f(u16 v) { return __uint_as_float(((unsigned)v) << 16); }
DI float rbf(float x) { return __uint_as_float(pk2(x, 0.f) << 16); }

DI void glds16(const void* g, char* lds_wave_base) {
  __builtin_amdgcn_global_load_lds((const __attribute__((address_space(1))) void*)g,
                                   (LDSP(void)*)lds_wave_base, 16, 0, 0);
}
DI s16x4 trread(const char* p) {
  return __builtin_amdgcn_ds_read_tr16_b64_v4i16((LDSP(s16x4)*)p);
}
#define WAITV(n) asm volatile("s_waitcnt vmcnt(" #n ")" ::: "memory")
#define BARRIER() do { asm volatile("s_waitcnt lgkmcnt(0)" ::: "memory"); __builtin_amdgcn_s_barrier(); asm volatile("" ::: "memory"); } while (0)

template <int BM, int BN, int WM, int WN>
struct GC {
  static constexpr int NTH = WM * WN * 64;
  static constexpr int TM = BM / WM, TN = BN / WN, MT = TM / 16, NT = TN / 16, NB = BN / 16;
  static constexpr int A_BYTES = BM * 128, B_BYTES = 64 * BN * 2, STAGE = A_BYTES + B_BYTES;
  static constexpr int A_UNITS = BM * 8, B_UNITS = 8 * BN;
  static constexpr int A_IT = A_UNITS / NTH, B_IT = (B_UNITS + NTH - 1) / NTH;
  static_assert(A_UNITS % NTH == 0, "A units");
};

template <int BM, int BN, int WM, int WN, class BPtr>
DI void gemm_stage(const u16* __restrict__ A, int lda, int m0, int k0, BPtr bptr, char* buf, int tid) {
  using G = GC<BM, BN, WM, WN>;
  const int wv = __builtin_amdgcn_readfirstlane(tid >> 6);
#pragma unroll
  for (int i = 0; i < G::A_IT; ++i) {
    int u = i * G::NTH + tid;
    int row = u >> 3, ch = (u & 7) ^ (row & 7);
    glds16(A + (size_t)(m0 + row) * lda + k0 + ch * 8, buf + (i * G::NTH + wv * 64) * 16);
  }
#pragma unroll
  for (int i = 0; i < G::B_IT; ++i) {
    int ub = i * G::NTH + wv * 64;
    if ((i + 1) * G::NTH <= G::B_UNITS || ub < G::B_UNITS) {
      int u = i * G::NTH + tid;
      int st = u >> 4, kblk = st / G::NB, nblk = st - kblk * G::NB;
      int rr = (u >> 1) & 7, hf = u & 1;
      int k = kblk * 8 + (rr ^ ((kblk & 1) << 2));
      int n = nblk * 16 + hf * 8;
      glds16(bptr(k0 + k, n), buf + G::A_BYTES + ub * 16);
    }
  }
}

template <int BM, int BN, int WM, int WN>
DI void gemm_compute(const char* buf, f32x4 (&acc)[GC<BM, BN, WM, WN>::NT][GC<BM, BN, WM, WN>::MT], int wm, int wn, int lane) {
  using G = GC<BM, BN, WM, WN>;
  const int g = lane >> 4, q = (lane & 15) >> 2, p = lane & 3;
  const char* abase = buf + (wm * G::TM + (lane & 15)) * 128;
  const char* bbase = buf + G::A_BYTES + (g * G::NB + wn * G::NT) * 256 + q * 32 + p * 8;
#pragma unroll
  for (int ks = 0; ks < 2; ++ks) {
    bf16x8 af[G::MT];
#pragma unroll
    for (int mt = 0; mt < G::MT; ++mt)
      af[mt] = *(const bf16x8*)(abase + mt * 16 * 128 + (((ks * 4 + g) ^ (lane & 7)) << 4));
#pragma unroll
    for (int nt = 0; nt < G::NT; ++nt) {
      const char* bp = bbase + (ks * 4 * G::NB + nt) * 256;
      s16x4 lo = trread(bp + ((g & 1) << 7));
      s16x4 hi = trread(bp + (((g & 1) ^ 1) << 7));
      bf16x8 bfr = __builtin_shufflevector(lo, hi, 0, 1, 2, 3, 4, 5, 6, 7);
#pragma unroll
      for (int mt = 0; mt < G::MT; ++mt)
        acc[nt][mt] = __builtin_amdgcn_mfma_f32_16x16x32_bf16(bfr, af[mt], acc[nt][mt], 0, 0, 0);
    }
  }
}

template <int BM, int BN, int WM, int WN, class BPtr>
DI void gemm_main(const u16* __restrict__ A, int lda, int m0, int kt0, int nk, BPtr bptr, char* lds,
                  f32x4 (&acc)[GC<BM, BN, WM, WN>::NT][GC<BM, BN, WM, WN>::MT], int tid) {
  using G = GC<BM, BN, WM, WN>;
  const int wv = tid >> 6, lane = tid & 63, wm = wv / WN, wn = wv % WN;
  gemm_stage<BM, BN, WM, WN>(A, lda, m0, kt0 * 64, bptr, lds, tid);
  for (int kt = 0; kt < nk; ++kt) {
    WAITV(0);
    BARRIER();
    if (kt + 1 < nk) gemm_stage<BM, BN, WM, WN>(A, lda, m0, (kt0 + kt + 1) * 64, bptr, lds + ((kt + 1) & 1) * G::STAGE, tid);
    gemm_compute<BM, BN, WM, WN>(lds + (kt & 1) * G::STAGE, acc, wm, wn, lane);
  }
  BARRIER();
}

constexpr float QSCALE = 0.23570226039551584f * 1.4426950408889634f;

__global__ __launch_bounds__(256) void k_qkv(const u16* __restrict__ x, const u16* __restrict__ wqkv,
                                             const u16* __restrict__ gate_w, u16* __restrict__ qkvp,
                                             float* __restrict__ gate) {
  using G = GC<128, 144, 4, 1>;
  __shared__ __attribute__((aligned(16))) char lds[2 * G::STAGE > 65536 ? 2 * G::STAGE : 65536];
  const int tid = threadIdx.x, lane = tid & 63, wv = tid >> 6;
  const int nt_blk = blockIdx.x % 12, mt_blk = blockIdx.x / 12;
  const int m0 = mt_blk * 128, n0 = nt_blk * 144;

  if (nt_blk < 8) {
    int row = m0 + nt_blk * 16 + (tid >> 4), part = tid & 15;
    const u16* xr = x + (size_t)row * D_ + part * 36;
    const u16* gw = gate_w + part * 36;
    float s = 0.f;
#pragma unroll
    for (int i = 0; i < 9; ++i) {
      u32x2 xv = *(const u32x2*)(xr + i * 4);
      u32x2 wv2 = *(const u32x2*)(gw + i * 4);
#pragma unroll
      for (int j = 0; j < 2; ++j) {
        s += rbf(__uint_as_float(xv[j] << 16) * __uint_as_float(wv2[j] << 16));
        s += rbf(__uint_as_float(xv[j] & 0xffff0000u) * __uint_as_float(wv2[j] & 0xffff0000u));
      }
    }
    s += __shfl_xor(s, 1); s += __shfl_xor(s, 2); s += __shfl_xor(s, 4); s += __shfl_xor(s, 8);
    if (part == 0) {
      float z = rbf(s);
      float gv = rbf(1.f / (1.f + __expf(-z)));
      gate[row] = gv;
    }
  }

  f32x4 acc[G::NT][G::MT] = {};
  auto bptr = [&](int k, int n) { return wqkv + (size_t)k * (3 * D_) + n0 + n; };
  gemm_main<128, 144, 4, 1>(x, D_, m0, 0, 9, bptr, lds, acc, tid);

  const int which = nt_blk >> 2;
  {
    u32x4 z = {0u, 0u, 0u, 0u};
#pragma unroll
    for (int i = 0; i < 16; ++i) *(u32x4*)(lds + (i * 256 + tid) * 16) = z;
  }
  __syncthreads();
  const float sc = which == 0 ? QSCALE : 1.f;
#pragma unroll
  for (int nt = 0; nt < G::NT; ++nt)
#pragma unroll
    for (int mt = 0; mt < G::MT; ++mt) {
      int tok = wv * 32 + mt * 16 + (lane & 15);
#pragma unroll
      for (int r = 0; r < 4; ++r) {
        int n = nt * 16 + 4 * (lane >> 4) + r;
        int hl = n / 18, d = n - hl * 18;
        int ch = d >> 3;
        if (which == 1) ch ^= (tok >> 2) & 3;
        unsigned v = pk2(acc[nt][mt][r] * sc, 0.f);
        *(u16*)(lds + (hl * 128 + tok) * 64 + ch * 16 + (d & 7) * 2) = (u16)v;
      }
    }
  if (which == 2) {
#pragma unroll
    for (int i = 0; i < 4; ++i) {
      int r = i * 256 + tid;
      *(u16*)(lds + r * 64 + 36) = (u16)0x3f80;
    }
  }
  __syncthreads();
  u16* dst = qkvp + (size_t)which * (H_ * S_ * 32);
  const int head0 = (nt_blk & 3) * 8;
#pragma unroll
  for (int i = 0; i < 16; ++i) {
    int u = i * 256 + tid;
    int hl = u >> 9, rem = u & 511;
    u32x4 v = *(const u32x4*)(lds + u * 16);
    *(u32x4*)(dst + ((size_t)(head0 + hl) * S_ + m0) * 32 + rem * 8) = v;
  }
}

__global__ __launch_bounds__(512) void k_gateup(const u16* __restrict__ x, const u16* __restrict__ wg,
                                                const u16* __restrict__ wu, const float* __restrict__ gate,
                                                u16* __restrict__ A2) {
  using G = GC<256, 288, 4, 2>;
  __shared__ __attribute__((aligned(16))) char lds[2 * G::STAGE];
  const int tid = threadIdx.x, lane = tid & 63, wv = tid >> 6, wm = wv >> 1, wn = wv & 1;
  const int bid = blockIdx.x;
  const int nt_blk = (bid & 7) * 4 + ((bid >> 3) & 3), mt_blk = bid >> 5;
  const int m0 = mt_blk * 256, f0 = nt_blk * 144;
  f32x4 acc[G::NT][G::MT] = {};
  auto bptr = [&](int k, int n) {
    return (n < 144 ? wg + n : wu + (n - 144)) + (size_t)k * F_ + f0;
  };
  gemm_main<256, 288, 4, 2>(x, D_, m0, 0, 9, bptr, lds, acc, tid);

  char* xch = lds + wm * (36 * 64 * 8);
  if (wn == 1) {
#pragma unroll
    for (int nt = 0; nt < G::NT; ++nt)
#pragma unroll
      for (int mt = 0; mt < G::MT; ++mt) {
        u32x2 v = {pk2(acc[nt][mt][0], acc[nt][mt][1]), pk2(acc[nt][mt][2], acc[nt][mt][3])};
        *(u32x2*)(xch + ((nt * G::MT + mt) * 64 + lane) * 8) = v;
      }
  }
  __syncthreads();
  if (wn == 0) {
    float og[G::MT];
#pragma unroll
    for (int mt = 0; mt < G::MT; ++mt) og[mt] = rbf(1.f - gate[m0 + wm * 64 + mt * 16 + (lane & 15)]);
#pragma unroll
    for (int nt = 0; nt < G::NT; ++nt)
#pragma unroll
      for (int mt = 0; mt < G::MT; ++mt) {
        u32x2 uv = *(const u32x2*)(xch + ((nt * G::MT + mt) * 64 + lane) * 8);
        float h0 = rbf(acc[nt][mt][0]) * __uint_as_float(uv[0] << 16) * og[mt];
        float h1 = rbf(acc[nt][mt][1]) * __uint_as_float(uv[0] & 0xffff0000u) * og[mt];
        float h2 = rbf(acc[nt][mt][2]) * __uint_as_float(uv[1] << 16) * og[mt];
        float h3 = rbf(acc[nt][mt][3]) * __uint_as_float(uv[1] & 0xffff0000u) * og[mt];
        u32x2 o = {pk2(h0, h1), pk2(h2, h3)};
        int tok = m0 + wm * 64 + mt * 16 + (lane & 15);
        int n = f0 + nt * 16 + 4 * (lane >> 4);
        *(u32x2*)(A2 + (size_t)tok * KA_ + D_ + n) = o;
      }
  }
}

DI void attn_stage(const u16* __restrict__ Kh, const u16* __restrict__ Vh, int c, char* buf, int tid) {
  const int wv = __builtin_amdgcn_readfirstlane(tid >> 6);
#pragma unroll
  for (int i = 0; i < 2; ++i) {
    int u = i * 512 + tid;
    glds16(Kh + (size_t)c * 8192 + u * 8, buf + (i * 512 + wv * 64) * 16);
  }
#pragma unroll
  for (int i = 0; i < 2; ++i) {
    int u = i * 512 + tid;
    glds16(Vh + (size_t)c * 8192 + u * 8, buf + 16384 + (i * 512 + wv * 64) * 16);
  }
}

DI float halfswap_max(float m) {
  auto rr = __builtin_amdgcn_permlane32_swap(__float_as_uint(m), __float_as_uint(m), false, false);
  return fmaxf(__uint_as_float(rr[0]), __uint_as_float(rr[1]));
}

template <bool SAFE>
DI void attn_run(const u16* __restrict__ Kh, const u16* __restrict__ Vh, bf16x8 q0, bf16x8 q1, char* lds, f32x16& o,
                 int tid) {
  const int lane = tid & 63, r32 = lane & 31, h = lane >> 5;
  const int koff0 = r32 * 64 + ((h ^ ((r32 >> 2) & 3)) << 4);
  const int koff1 = r32 * 64 + (((2 + h) ^ ((r32 >> 2) & 3)) << 4);
  const int voff = 16384 + (4 * h + ((lane & 15) >> 2)) * 64 + ((lane >> 4) & 1) * 32 + (lane & 3) * 8;
  f32x16 negm;
  float m = -INFINITY;
#pragma unroll
  for (int r = 0; r < 16; ++r) { o[r] = 0.f; negm[r] = 0.f; }
  attn_stage(Kh, Vh, 0, lds, tid);
  for (int c = 0; c < 8; ++c) {
    WAITV(0);
    BARRIER();
    if (c + 1 < 8) attn_stage(Kh, Vh, c + 1, lds + ((c + 1) & 1) * 32768, tid);
    const char* buf = lds + (c & 1) * 32768;
    if (!SAFE && c == 0) {
      bf16x8 k0 = *(const bf16x8*)(buf + koff0), k1 = *(const bf16x8*)(buf + koff1);
      f32x16 s = __builtin_amdgcn_mfma_f32_32x32x16_bf16(k0, q0, negm, 0, 0, 0);
      s = __builtin_amdgcn_mfma_f32_32x32x16_bf16(k1, q1, s, 0, 0, 0);
      float mx = s[0];
#pragma unroll
      for (int r = 1; r < 16; ++r) mx = fmaxf(mx, s[r]);
      mx = halfswap_max(mx);
#pragma unroll
      for (int r = 0; r < 16; ++r) negm[r] = -mx;
    }
#pragma unroll 2
    for (int t = 0; t < 8; ++t) {
      const char* kb = buf + t * 2048;
      bf16x8 k0 = *(const bf16x8*)(kb + koff0), k1 = *(const bf16x8*)(kb + koff1);
      s16x4 v0 = trread(kb + voff), v1 = trread(kb + voff + 512), v2 = trread(kb + voff + 1024), v3 = trread(kb + voff + 1536);
      f32x16 s = __builtin_amdgcn_mfma_f32_32x32x16_bf16(k0, q0, negm, 0, 0, 0);
      s = __builtin_amdgcn_mfma_f32_32x32x16_bf16(k1, q1, s, 0, 0, 0);
      if (SAFE) {
        float mx = s[0];
#pragma unroll
        for (int r = 1; r < 16; ++r) mx = fmaxf(mx, s[r]);
        mx = halfswap_max(mx);
        float mn = fmaxf(m, mx);
        float al = __builtin_amdgcn_exp2f(m - mn);
        m = mn;
#pragma unroll
        for (int r = 0; r < 16; ++r) { o[r] *= al; s[r] -= mn; }
      }
      u32x4 p0, p1;
#pragma unroll
      for (int r = 0; r < 4; ++r) {
        p0[r] = pk2(__builtin_amdgcn_exp2f(s[2 * r]), __builtin_amdgcn_exp2f(s[2 * r + 1]));
        p1[r] = pk2(__builtin_amdgcn_exp2f(s[8 + 2 * r]), __builtin_amdgcn_exp2f(s[8 + 2 * r + 1]));
      }
      bf16x8 va = __builtin_shufflevector(v0, v1, 0, 1, 2, 3, 4, 5, 6, 7);
      bf16x8 vb = __builtin_shufflevector(v2, v3, 0, 1, 2, 3, 4, 5, 6, 7);
      o = __builtin_amdgcn_mfma_f32_32x32x16_bf16(va, __builtin_bit_cast(bf16x8, p0), o, 0, 0, 0);
      o = __builtin_amdgcn_mfma_f32_32x32x16_bf16(vb, __builtin_bit_cast(bf16x8, p1), o, 0, 0, 0);
    }
  }
}

__global__ __launch_bounds__(512) void k_attn(const u16* __restrict__ qkvp, const float* __restrict__ gate,
                                              u16* __restrict__ A2) {
  __shared__ __attribute__((aligned(16))) char lds[65536];
  const int tid = threadIdx.x, lane = tid & 63, wv = tid >> 6, r32 = lane & 31, h = lane >> 5;
  const int bid = blockIdx.x;
  const int head = (bid & 7) * 4 + ((bid >> 3) & 3), qb = bid >> 5;
  const u16* Qh = qkvp + (size_t)head * (S_ * 32);
  const u16* Kh = qkvp + (size_t)(H_ + head) * (S_ * 32);
  const u16* Vh = qkvp + (size_t)(2 * H_ + head) * (S_ * 32);
  const int qrow = qb * 256 + wv * 32 + r32;
  bf16x8 q0 = *(const bf16x8*)(Qh + (size_t)qrow * 32 + h * 8);
  bf16x8 q1 = *(const bf16x8*)(Qh + (size_t)qrow * 32 + 16 + h * 8);
  f32x16 o;
  attn_run<false>(Kh, Vh, q0, q1, lds, o, tid);
  bool bad = false;
#pragma unroll
  for (int r = 0; r < 11; ++r) bad |= !(fabsf(o[r]) < 1e30f);
  if (__syncthreads_or(bad)) {
    attn_run<true>(Kh, Vh, q0, q1, lds, o, tid);
  }
  float l = __shfl(o[10], r32);
  float sc = gate[qrow] / l;
  u16* dst = A2 + (size_t)qrow * KA_ + head * DH_;
  if (h == 0) {
    *(unsigned*)(dst + 0) = pk2(o[0] * sc, o[1] * sc);
    *(unsigned*)(dst + 2) = pk2(o[2] * sc, o[3] * sc);
    *(unsigned*)(dst + 8) = pk2(o[4] * sc, o[5] * sc);
    *(unsigned*)(dst + 10) = pk2(o[6] * sc, o[7] * sc);
    *(unsigned*)(dst + 16) = pk2(o[8] * sc, o[9] * sc);
  } else {
    *(unsigned*)(dst + 4) = pk2(o[0] * sc, o[1] * sc);
    *(unsigned*)(dst + 6) = pk2(o[2] * sc, o[3] * sc);
    *(unsigned*)(dst + 12) = pk2(o[4] * sc, o[5] * sc);
    *(unsigned*)(dst + 14) = pk2(o[6] * sc, o[7] * sc);
  }
}

__global__ __launch_bounds__(256) void k_down(const u16* __restrict__ A2, const u16* __restrict__ wo,
                                              const u16* __restrict__ wd, float* __restrict__ part) {
  using G = GC<128, 144, 4, 1>;
  __shared__ __attribute__((aligned(16))) char lds[2 * G::STAGE];
  const int tid = threadIdx.x, lane = tid & 63, wv = tid >> 6;
  const int bid = blockIdx.x;
  const int mt_blk = (bid & 7) * 2 + ((bid >> 3) & 1), rest = bid >> 4;
  const int nt_blk = rest & 3, sp = rest >> 2;
  const int m0 = mt_blk * 128, n0 = nt_blk * 144;
  const int kt0 = sp == 0 ? 0 : 21 + (sp - 1) * 20, nk = sp == 0 ? 21 : 20;
  f32x4 acc[G::NT][G::MT] = {};
  auto bptr = [&](int k, int n) {
    return (k < D_ ? wo + (size_t)k * D_ : wd + (size_t)(k - D_) * D_) + n0 + n;
  };
  gemm_main<128, 144, 4, 1>(A2, KA_, m0, kt0, nk, bptr, lds, acc, tid);
  float* dst = part + (size_t)sp * (S_ * D_);
#pragma unroll
  for (int nt = 0; nt < G::NT; ++nt)
#pragma unroll
    for (int mt = 0; mt < G::MT; ++mt) {
      int tok = m0 + wv * 32 + mt * 16 + (lane & 15);
      int n = n0 + nt * 16 + 4 * (lane >> 4);
      *(f32x4*)(dst + (size_t)tok * D_ + n) = acc[nt][mt];
    }
}

__global__ __launch_bounds__(256) void k_reduce(const float* __restrict__ part, u16* __restrict__ out) {
  int i = (blockIdx.x * 256 + threadIdx.x) * 8;
  f32x4 a = *(const f32x4*)(part + i), b = *(const f32x4*)(part + i + 4);
#pragma unroll
  for (int s = 1; s < 4; ++s) {
    a += *(const f32x4*)(part + (size_t)s * (S_ * D_) + i);
    b += *(const f32x4*)(part + (size_t)s * (S_ * D_) + i + 4);
  }
  u32x4 o = {pk2(a[0], a[1]), pk2(a[2], a[3]), pk2(b[0], b[1]), pk2(b[2], b[3])};
  *(u32x4*)(out + i) = o;
}

extern "C" void kernel_launch(void* const* d_in, const int* in_sizes, int n_in, void* d_out, int out_size,
                              void* d_ws, size_t ws_size, hipStream_t stream) {
  const u16* x = (const u16*)d_in[0];
  const u16* wqkv = (const u16*)d_in[1];
  const u16* wo = (const u16*)d_in[2];
  const u16* wg = (const u16*)d_in[4];
  const u16* wu = (const u16*)d_in[5];
  const u16* wd = (const u16*)d_in[6];
  const u16* gw = (const u16*)d_in[7];
  char* ws = (char*)d_ws;
  u16* qkvp = (u16*)ws;
  float* gate = (float*)(ws + (size_t)16 * 1024 * 1024);
  u16* A2 = (u16*)(ws + (size_t)17 * 1024 * 1024);
  float* part = (float*)(ws + (size_t)48 * 1024 * 1024);
  k_qkv<<<192, 256, 0, stream>>>(x, wqkv, gw, qkvp, gate);
  k_gateup<<<256, 512, 0, stream>>>(x, wg, wu, gate, A2);
  k_attn<<<256, 512, 0, stream>>>(qkvp, gate, A2);
  k_down<<<256, 256, 0, stream>>>(A2, wo, wd, part);
  k_reduce<<<576, 256, 0, stream>>>(part, (u16*)d_out);
}
```

```cpp
#include <hip/hip_runtime.h>
#include <hip/hip_bf16.h>

typedef __attribute__((ext_vector_type(8))) short bf16x8;
typedef __attribute__((ext_vector_type(4))) short s16x4;
typedef __attribute__((ext_vector_type(4))) float f32x4;
typedef __attribute__((ext_vector_type(2))) float f32x2;
typedef __attribute__((ext_vector_type(16))) float f32x16;
typedef __attribute__((ext_vector_type(4))) unsigned u32x4;
typedef __attribute__((ext_vector_type(2))) unsigned u32x2;
typedef __attribute__((ext_vector_type(2))) __bf16 bf16x2_t;
typedef unsigned short u16;

#define DI __device__ __forceinline__
#define LDSP(T) __attribute__((address_space(3))) T

constexpr int S_ = 2048, D_ = 576, H_ = 32, DH_ = 18, F_ = 4608;

DI unsigned pk2(float lo, float hi) {
  f32x2 v = {lo, hi};
  bf16x2_t b = __builtin_convertvector(v, bf16x2_t);
  return __builtin_bit_cast(unsigned, b);
}
DI float rbf(float x) { return __uint_as_float(pk2(x, 0.f) << 16); }
DI float dot2bf(unsigned a, unsigned b, float c) {
  return __builtin_amdgcn_fdot2_f32_bf16(__builtin_bit_cast(bf16x2_t, a), __builtin_bit_cast(bf16x2_t, b), c, false);
}

DI void glds16(const void* sbase, unsigned voff, unsigned lds_base) {
  unsigned sv;
  asm volatile("s_mov_b32 %0, m0\n\ts_mov_b32 m0, %3\n\ts_nop 0\n\tglobal_load_lds_dwordx4 %1, %2\n\ts_mov_b32 m0, %0"
               : "=&s"(sv) : "v"(voff), "s"(sbase), "s"(lds_base) : "memory");
}
DI unsigned lds_addr(const char* p) { return (unsigned)(size_t)(LDSP(const char)*)p; }
DI s16x4 trread(const char* p) { return __builtin_amdgcn_ds_read_tr16_b64_v4i16((LDSP(s16x4)*)p); }
template <int N> DI void waitv() { asm volatile("s_waitcnt vmcnt(%0)" ::"n"(N) : "memory"); }
#define BARRIER() do { asm volatile("s_waitcnt lgkmcnt(0)" ::: "memory"); __builtin_amdgcn_s_barrier(); asm volatile("" ::: "memory"); } while (0)

template <int BM_, int BN_, int WM_, int WN_, int BK_, int NST_>
struct GC {
  static constexpr int BM = BM_, BN = BN_, WM = WM_, WN = WN_, BK = BK_, NST = NST_;
  static constexpr int NW = WM * WN, NTH = NW * 64;
  static constexpr int TM = BM / WM, MT = TM / 16, NB = BN / 16;
  static constexpr int ROWB = BK * 2, CH = BK / 8;
  static constexpr int A_BYTES = BM * ROWB, B_BYTES = BK * BN * 2, STAGE = A_BYTES + B_BYTES;
  static constexpr int A_W = A_BYTES / 1024, B_W = B_BYTES / 1024;
  static constexpr int A_IT = A_W / NW, B_IT = (B_W + NW - 1) / NW, B_REM = B_W % NW;
  static_assert(A_W % NW == 0, "A units");
  static constexpr int L_HI = A_IT + B_IT, L_LO = A_IT + (B_REM ? B_IT - 1 : B_IT);
  DI static int aswz(int row) { return BK == 64 ? (row & 7) : ((row >> 2) & 3); }
};

template <int NBs>
DI unsigned bseg_off(int u, int ld) {
  int st = u >> 4, kblk = st / NBs, nblk = st - kblk * NBs;
  int rr = (u >> 1) & 7, hf = u & 1;
  int k = kblk * 8 + (rr ^ ((kblk & 1) << 2));
  return (unsigned)(k * ld + nblk * 16 + hf * 8) * 2u;
}
template <class G>
DI void stage_a(const u16* __restrict__ A, int lda, int m0, int k0, unsigned buf, int wv, int lane) {
  const u16* base = A + (size_t)m0 * lda + k0;
#pragma unroll
  for (int i = 0; i < G::A_IT; ++i) {
    int u = (i * G::NW + wv) * 64 + lane;
    int row = u / G::CH, ch = (u % G::CH) ^ G::aswz(row);
    glds16(base, (unsigned)(row * lda + ch * 8) * 2u, buf + (i * G::NW + wv) * 1024);
  }
}
template <class G>
DI void stage_b(const u16* __restrict__ B, int ldb, int k0, unsigned buf, int wv, int lane) {
  const u16* base = B + (size_t)k0 * ldb;
#pragma unroll
  for (int i = 0; i < G::B_IT; ++i) {
    if (G::B_REM == 0 || i + 1 < G::B_IT || wv < G::B_REM)
      glds16(base, bseg_off<G::NB>((i * G::NW + wv) * 64 + lane, ldb), buf + G::A_BYTES + (i * G::NW + wv) * 1024);
  }
}

template <class G, int NFLY>
DI void gemm_wait(int wv) {
  if (G::B_REM == 0 || wv < G::B_REM) waitv<G::L_HI * NFLY>();
  else waitv<G::L_LO * NFLY>();
}

DI bf16x8 bfrag(const char* bp, int g) {
  s16x4 lo = trread(bp + ((g & 1) << 7));
  s16x4 hi = trread(bp + (((g & 1) ^ 1) << 7));
  return __builtin_shufflevector(lo, hi, 0, 1, 2, 3, 4, 5, 6, 7);
}

template <class G, int NT>
DI void gemm_compute(const char* buf, f32x4 (&acc)[NT][G::MT], int wm, int nb0, int lane) {
  const int g = lane >> 4, q = (lane & 15) >> 2, p = lane & 3;
  const int row = wm * G::TM + (lane & 15);
  const char* abase = buf + row * G::ROWB;
  const int sw = G::aswz(row);
  const char* bbase = buf + G::A_BYTES + (g * G::NB + nb0) * 256 + q * 32 + p * 8;
#pragma unroll
  for (int ks = 0; ks < G::BK / 32; ++ks) {
    bf16x8 af[G::MT];
#pragma unroll
    for (int mt = 0; mt < G::MT; ++mt) af[mt] = *(const bf16x8*)(abase + mt * 16 * G::ROWB + (((ks * 4 + g) ^ sw) << 4));
#pragma unroll
    for (int nt = 0; nt < NT; ++nt) {
      bf16x8 bfr = bfrag(bbase + (ks * 4 * G::NB + nt) * 256, g);
#pragma unroll
      for (int mt = 0; mt < G::MT; ++mt)
        acc[nt][mt] = __builtin_amdgcn_mfma_f32_16x16x32_bf16(bfr, af[mt], acc[nt][mt], 0, 0, 0);
    }
  }
}

template <class G, class Stage, class Comp>
DI void gemm_main(int nk, Stage stage, char* lds, Comp comp, int wv) {
  const unsigned l0 = lds_addr(lds);
#pragma unroll
  for (int s = 0; s < G::NST - 1; ++s)
    if (s < nk) stage(s, l0 + s * G::STAGE);
  int cur = 0, nxt = G::NST - 1;
  for (int kt = 0; kt < nk; ++kt) {
    if (kt + G::NST - 1 <= nk) gemm_wait<G, G::NST - 2>(wv);
    else waitv<0>();
    BARRIER();
    if (kt + G::NST - 1 < nk) stage(kt + G::NST - 1, l0 + nxt * G::STAGE);
    comp(lds + cur * G::STAGE);
    cur = cur + 1 == G::NST ? 0 : cur + 1;
    nxt = nxt + 1 == G::NST ? 0 : nxt + 1;
  }
  BARRIER();
}

constexpr float QSCALE = 0.23570226039551584f * 1.4426950408889634f;
using G1 = GC<256, 352, 4, 2, 64, 2>;

__global__ __launch_bounds__(512) void k_proj(const u16* __restrict__ x, const u16* __restrict__ wqkv,
                                              const u16* __restrict__ wg, const u16* __restrict__ wu,
                                              const u16* __restrict__ gate_w, u16* __restrict__ qkvp,
                                              float* __restrict__ gate, u16* __restrict__ Ah) {
  using G = G1;
  __shared__ __attribute__((aligned(16))) char lds[2 * G::STAGE + 1152];
  const int tid = threadIdx.x, lane = tid & 63;
  const int wv = __builtin_amdgcn_readfirstlane(tid >> 6), wm = wv >> 1, wn = wv & 1;
  const int bid = blockIdx.x;
  const int nt_blk = (bid & 7) * 4 + ((bid >> 3) & 3), mt_blk = bid >> 5;
  const int m0 = mt_blk * 256, f0 = nt_blk * 144;
  const int coff = nt_blk == 31 ? 2 : 0;
  const int c0 = nt_blk * 54 - coff;
  char* gwl = lds + 2 * G::STAGE;
  if (tid < 72) *(u32x4*)(gwl + tid * 16) = *(const u32x4*)(gate_w + tid * 8);

  f32x4 acc[11][4] = {};
  float z[4] = {0.f, 0.f, 0.f, 0.f};
  auto stage = [&](int kt, unsigned buf) {
    stage_a<G>(x, D_, m0, kt * 64, buf, wv, lane);
    const u16* bg = wg + (size_t)kt * 64 * F_ + f0;
    const u16* bu = wu + (size_t)kt * 64 * F_ + f0;
    const u16* bq = wqkv + (size_t)kt * 64 * (3 * D_) + c0;
    const unsigned bb = buf + G::A_BYTES;
#pragma unroll
    for (int i = 0; i < 3; ++i) {
      int j = i * 8 + wv;
      if (i < 2 || wv < 2) {
        unsigned off = bseg_off<9>(j * 64 + lane, F_);
        glds16(bg, off, bb + j * 1024);
        glds16(bu, off, bb + 18432 + j * 1024);
      }
    }
    {
      int u = wv * 64 + lane;
      if ((u & 63) >= 49 && (u & 1) && ((u >> 4) & 3) == 3) u -= 49;
      glds16(bq, bseg_off<4>(u, 3 * D_), bb + 36864 + wv * 1024);
    }
  };
  const int g = lane >> 4, q = (lane & 15) >> 2, p = lane & 3;
  int ktc = 0;
  auto comp = [&](const char* buf) {
    const int row = wm * 64 + (lane & 15);
    const char* abase = buf + row * 128;
    const int sw = row & 7;
    const char* bb = buf + G::A_BYTES + q * 32 + p * 8;
    const char* bgu = bb + wn * 18432 + g * 9 * 256;
    const char* bqq = bb + 36864 + (g * 4 + wn * 2) * 256;
#pragma unroll
    for (int ks = 0; ks < 2; ++ks) {
      bf16x8 af[4];
#pragma unroll
      for (int mt = 0; mt < 4; ++mt) af[mt] = *(const bf16x8*)(abase + mt * 16 * 128 + (((ks * 4 + g) ^ sw) << 4));
      u32x4 gw = *(const u32x4*)(gwl + (ktc * 64 + ks * 32 + g * 8) * 2);
#pragma unroll
      for (int nt = 0; nt < 11; ++nt) {
        bf16x8 bfr = nt < 9 ? bfrag(bgu + (ks * 36 + nt) * 256, g) : bfrag(bqq + (ks * 16 + nt - 9) * 256, g);
#pragma unroll
        for (int mt = 0; mt < 4; ++mt)
          acc[nt][mt] = __builtin_amdgcn_mfma_f32_16x16x32_bf16(bfr, af[mt], acc[nt][mt], 0, 0, 0);
      }
#pragma unroll
      for (int mt = 0; mt < 4; ++mt) {
        u32x4 a4 = __builtin_bit_cast(u32x4, af[mt]);
#pragma unroll
        for (int j = 0; j < 4; ++j) z[mt] = dot2bf(a4[j], gw[j], z[mt]);
      }
      __builtin_amdgcn_sched_barrier(0);
    }
    ++ktc;
  };
  gemm_main<G>(9, stage, lds, comp, wv);
  int tz = threadIdx.x;
  asm volatile("" : "+v"(tz));
  const int lz = tz & 63, gz = lz >> 4;

  float gv[4];
#pragma unroll
  for (int mt = 0; mt < 4; ++mt) {
    float s = z[mt];
    s += __shfl_xor(s, 16);
    s += __shfl_xor(s, 32);
    s = rbf(s);
    gv[mt] = rbf(1.f / (1.f + __expf(-s)));
  }
  if (nt_blk == 0 && wn == 0 && lz < 16) {
#pragma unroll
    for (int mt = 0; mt < 4; ++mt) gate[m0 + wm * 64 + mt * 16 + lz] = gv[mt];
  }

  char* xch = lds + wm * 18720;
  char* img = lds + 4 * 18720;
  {
    u32x4 zz = {0u, 0u, 0u, 0u};
#pragma unroll
    for (int i = 0; i < 6; ++i) *(u32x4*)(img + (i * 512 + tz) * 16) = zz;
  }
  if (wn == 1) {
#pragma unroll
    for (int nt = 0; nt < 9; ++nt)
#pragma unroll
      for (int mt = 0; mt < 4; ++mt) {
        u32x2 v = {pk2(acc[nt][mt][0], acc[nt][mt][1]), pk2(acc[nt][mt][2], acc[nt][mt][3])};
        *(u32x2*)(xch + (nt * 4 + mt) * 520 + lz * 8) = v;
      }
  }
  __syncthreads();
  {
    const int hg0 = nt_blk * 3;
#pragma unroll
    for (int t2 = 0; t2 < 2; ++t2)
#pragma unroll
      for (int r = 0; r < 4; ++r) {
        int c = wn * 32 + t2 * 16 + 4 * gz + r - coff;
        if (c >= 0 && c < 54) {
          int hl = c / 18, d = c - hl * 18;
          int which = (hg0 + hl) >> 5;
          float sc = which == 0 ? QSCALE : 1.f;
#pragma unroll
          for (int mt = 0; mt < 4; ++mt) {
            int tok = wm * 64 + mt * 16 + (lz & 15);
            int ch = d >> 3;
            if (which == 1) ch ^= (tok >> 2) & 3;
            *(u16*)(img + (hl * 256 + tok) * 64 + ch * 16 + (d & 7) * 2) = (u16)pk2(acc[9 + t2][mt][r] * sc, 0.f);
          }
        }
      }
    for (int i = tz; i < 768; i += 512) {
      int hl = i >> 8;
      if (((hg0 + hl) >> 5) == 2) *(u16*)(img + i * 64 + 36) = (u16)0x3f80;
    }
  }
  if (wn == 0) {
#pragma unroll
    for (int nt = 0; nt < 9; ++nt)
#pragma unroll
      for (int mt = 0; mt < 4; ++mt) {
        float og = rbf(1.f - gv[mt]);
        u32x2* slot = (u32x2*)(xch + (nt * 4 + mt) * 520 + lz * 8);
        u32x2 uv = *slot;
        float h0 = rbf(acc[nt][mt][0]) * __uint_as_float(uv[0] << 16) * og;
        float h1 = rbf(acc[nt][mt][1]) * __uint_as_float(uv[0] & 0xffff0000u) * og;
        float h2 = rbf(acc[nt][mt][2]) * __uint_as_float(uv[1] << 16) * og;
        float h3 = rbf(acc[nt][mt][3]) * __uint_as_float(uv[1] & 0xffff0000u) * og;
        *slot = (u32x2){pk2(h0, h1), pk2(h2, h3)};
      }
  }
  __syncthreads();
#pragma unroll
  for (int i = 0; i < 9; ++i) {
    int u = i * 512 + tz;
    int row = u / 18, cu = u - row * 18;
    const char* src = lds + (row >> 6) * 18720 + ((cu >> 1) * 4 + ((row >> 4) & 3)) * 520 + ((cu & 1) * 32 + (row & 15)) * 8;
    u32x2 v0 = *(const u32x2*)src, v1 = *(const u32x2*)(src + 128);
    u32x4 v = {v0[0], v0[1], v1[0], v1[1]};
    *(u32x4*)(Ah + (size_t)(m0 + row) * F_ + f0 + cu * 8) = v;
  }
#pragma unroll
  for (int i = 0; i < 6; ++i) {
    int u = i * 512 + tz;
    int hl = u >> 10, rem = u & 1023;
    u32x4 v = *(const u32x4*)(img + u * 16);
    *(u32x4*)(qkvp + ((size_t)(nt_blk * 3 + hl) * S_ + m0) * 32 + rem * 8) = v;
  }
}

DI void attn_stage(const u16* __restrict__ Kh, const u16* __restrict__ Vh, int c, unsigned buf, int wv, int lane) {
#pragma unroll
  for (int i = 0; i < 4; ++i) glds16(Kh + (size_t)c * 8192, ((i * 4 + wv) * 64 + lane) * 16u, buf + (i * 4 + wv) * 1024);
#pragma unroll
  for (int i = 0; i < 4; ++i)
    glds16(Vh + (size_t)c * 8192, ((i * 4 + wv) * 64 + lane) * 16u, buf + 16384 + (i * 4 + wv) * 1024);
}

DI float halfswap_max(float m) {
  auto rr = __builtin_amdgcn_permlane32_swap(__float_as_uint(m), __float_as_uint(m), false, false);
  return fmaxf(__uint_as_float(rr[0]), __uint_as_float(rr[1]));
}
DI float max16(const f32x16& s) {
  float mx = s[0];
#pragma unroll
  for (int r = 1; r < 16; ++r) mx = fmaxf(mx, s[r]);
  return halfswap_max(mx);
}
#define MFMA32(a, b, c) __builtin_amdgcn_mfma_f32_32x32x16_bf16((a), (b), (c), 0, 0, 0)

template <bool SAFE>
DI void attn_run(const u16* __restrict__ Kh, const u16* __restrict__ Vh, const bf16x8 (&qf)[2][2], char* lds,
                 f32x16 (&o)[2], int wv, int lane) {
  const int r32 = lane & 31, h = lane >> 5;
  const int koff0 = r32 * 64 + ((h ^ ((r32 >> 2) & 3)) << 4);
  const int koff1 = r32 * 64 + (((2 + h) ^ ((r32 >> 2) & 3)) << 4);
  const int voff = 16384 + (4 * h + ((lane & 15) >> 2)) * 64 + ((lane >> 4) & 1) * 32 + (lane & 3) * 8;
  const unsigned l0 = lds_addr(lds);
  f32x16 negm[2];
  float m[2] = {-INFINITY, -INFINITY};
#pragma unroll
  for (int a = 0; a < 2; ++a)
#pragma unroll
    for (int r = 0; r < 16; ++r) { o[a][r] = 0.f; negm[a][r] = 0.f; }
  attn_stage(Kh, Vh, 0, l0, wv, lane);
  for (int c = 0; c < 8; ++c) {
    waitv<0>();
    BARRIER();
    if (c + 1 < 8) attn_stage(Kh, Vh, c + 1, l0 + ((c + 1) & 1) * 32768, wv, lane);
    const char* buf = lds + (c & 1) * 32768;
    if (!SAFE && c == 0) {
      bf16x8 k0 = *(const bf16x8*)(buf + koff0), k1 = *(const bf16x8*)(buf + koff1);
#pragma unroll
      for (int a = 0; a < 2; ++a) {
        f32x16 s = MFMA32(k0, qf[a][0], negm[a]);
        s = MFMA32(k1, qf[a][1], s);
        float mx = max16(s);
#pragma unroll
        for (int r = 0; r < 16; ++r) negm[a][r] = -mx;
      }
    }
#pragma unroll 2
    for (int t = 0; t < 8; ++t) {
      const char* kb = buf + t * 2048;
      bf16x8 k0 = *(const bf16x8*)(kb + koff0), k1 = *(const bf16x8*)(kb + koff1);
      s16x4 v0 = trread(kb + voff), v1 = trread(kb + voff + 512), v2 = trread(kb + voff + 1024), v3 = trread(kb + voff + 1536);
      bf16x8 va = __builtin_shufflevector(v0, v1, 0, 1, 2, 3, 4, 5, 6, 7);
      bf16x8 vb = __builtin_shufflevector(v2, v3, 0, 1, 2, 3, 4, 5, 6, 7);
#pragma unroll
      for (int a = 0; a < 2; ++a) {
        f32x16 s = MFMA32(k0, qf[a][0], negm[a]);
        s = MFMA32(k1, qf[a][1], s);
        if (SAFE) {
          float mn = fmaxf(m[a], max16(s));
          float al = __builtin_amdgcn_exp2f(m[a] - mn);
          m[a] = mn;
#pragma unroll
          for (int r = 0; r < 16; ++r) { o[a][r] *= al; s[r] -= mn; }
        }
        u32x4 p0, p1;
#pragma unroll
        for (int r = 0; r < 4; ++r) {
          p0[r] = pk2(__builtin_amdgcn_exp2f(s[2 * r]), __builtin_amdgcn_exp2f(s[2 * r + 1]));
          p1[r] = pk2(__builtin_amdgcn_exp2f(s[8 + 2 * r]), __builtin_amdgcn_exp2f(s[8 + 2 * r + 1]));
        }
        o[a] = MFMA32(va, __builtin_bit_cast(bf16x8, p0), o[a]);
        o[a] = MFMA32(vb, __builtin_bit_cast(bf16x8, p1), o[a]);
      }
    }
  }
}

DI void attn_block(int bid, const u16* __restrict__ qkvp, const float* __restrict__ gate, u16* __restrict__ Ao,
                   char* lds) {
  const int tid = threadIdx.x, lane = tid & 63, r32 = lane & 31, h = lane >> 5;
  const int wv = __builtin_amdgcn_readfirstlane(tid >> 6);
  const int head = (bid & 7) * 4 + ((bid >> 3) & 3), qb = bid >> 5;
  const u16* Qh = qkvp + (size_t)head * (S_ * 32);
  const u16* Kh = qkvp + (size_t)(H_ + head) * (S_ * 32);
  const u16* Vh = qkvp + (size_t)(2 * H_ + head) * (S_ * 32);
  const int qrow0 = qb * 256 + wv * 64 + r32;
  bf16x8 qf[2][2];
#pragma unroll
  for (int a = 0; a < 2; ++a) {
    qf[a][0] = *(const bf16x8*)(Qh + (size_t)(qrow0 + a * 32) * 32 + h * 8);
    qf[a][1] = *(const bf16x8*)(Qh + (size_t)(qrow0 + a * 32) * 32 + 16 + h * 8);
  }
  float gq[2] = {gate[qrow0], gate[qrow0 + 32]};
  asm volatile("" ::"v"(qf[0][0]), "v"(qf[0][1]), "v"(qf[1][0]), "v"(qf[1][1]), "v"(gq[0]), "v"(gq[1]));
  f32x16 o[2];
  attn_run<false>(Kh, Vh, qf, lds, o, wv, lane);
  bool bad = false;
#pragma unroll
  for (int a = 0; a < 2; ++a)
#pragma unroll
    for (int r = 0; r < 11; ++r) bad |= !(fabsf(o[a][r]) < 1e30f);
  if (__syncthreads_or(bad)) attn_run<true>(Kh, Vh, qf, lds, o, wv, lane);
#pragma unroll
  for (int a = 0; a < 2; ++a) {
    float l = __shfl(o[a][10], r32);
    float sc = gq[a] / l;
    u16* dst = Ao + (size_t)(qrow0 + a * 32) * D_ + head * DH_ + h * 4;
    *(unsigned*)(dst + 0) = pk2(o[a][0] * sc, o[a][1] * sc);
    *(unsigned*)(dst + 2) = pk2(o[a][2] * sc, o[a][3] * sc);
    *(unsigned*)(dst + 8) = pk2(o[a][4] * sc, o[a][5] * sc);
    *(unsigned*)(dst + 10) = pk2(o[a][6] * sc, o[a][7] * sc);
    if (h == 0) *(unsigned*)(dst + 16) = pk2(o[a][8] * sc, o[a][9] * sc);
  }
}

using G2 = GC<128, 144, 4, 1, 32, 4>;
DI void down_block(int bid, const u16* __restrict__ Ah, const u16* __restrict__ wd, float* __restrict__ part,
                   char* lds) {
  using G = G2;
  const int tid = threadIdx.x, lane = tid & 63;
  const int wv = __builtin_amdgcn_readfirstlane(tid >> 6);
  const int mt_blk = (bid & 7) * 2 + ((bid >> 3) & 1), rest = bid >> 4;
  const int nt_blk = rest & 3, sp = rest >> 2;
  const int m0 = mt_blk * 128, n0 = nt_blk * 144;
  f32x4 acc[9][G::MT] = {};
  auto stage = [&](int kt, unsigned buf) {
    stage_a<G>(Ah, F_, m0, (sp * 36 + kt) * 32, buf, wv, lane);
    stage_b<G>(wd + n0, D_, (sp * 36 + kt) * 32, buf, wv, lane);
  };
  auto comp = [&](const char* buf) { gemm_compute<G, 9>(buf, acc, wv, 0, lane); };
  gemm_main<G>(36, stage, lds, comp, wv);
  float* dst = part + (size_t)sp * (S_ * D_);
#pragma unroll
  for (int nt = 0; nt < 9; ++nt)
#pragma unroll
    for (int mt = 0; mt < G::MT; ++mt) {
      int tok = m0 + wv * 32 + mt * 16 + (lane & 15);
      int n = n0 + nt * 16 + 4 * (lane >> 4);
      *(f32x4*)(dst + (size_t)tok * D_ + n) = acc[nt][mt];
    }
}

__global__ __launch_bounds__(256, 2) void k_mid(const u16* __restrict__ qkvp, const float* __restrict__ gate,
                                                u16* __restrict__ Ao, const u16* __restrict__ Ah,
                                                const u16* __restrict__ wd, float* __restrict__ part) {
  __shared__ __attribute__((aligned(16))) char lds[G2::NST * G2::STAGE > 65536 ? G2::NST * G2::STAGE : 65536];
  const int bid = blockIdx.x;
  if (bid < 256) attn_block(bid, qkvp, gate, Ao, lds);
  else down_block(bid - 256, Ah, wd, part, lds);
}

using G3 = GC<64, 96, 2, 2, 64, 4>;
__global__ __launch_bounds__(256) void k_out(const u16* __restrict__ Ao, const u16* __restrict__ wo,
                                             const float* __restrict__ part, u16* __restrict__ out) {
  using G = G3;
  __shared__ __attribute__((aligned(16))) char lds[G::NST * G::STAGE];
  const int tid = threadIdx.x, lane = tid & 63;
  const int wv = __builtin_amdgcn_readfirstlane(tid >> 6), wm = wv >> 1, wn = wv & 1;
  const int nt_blk = blockIdx.x % 6, mt_blk = blockIdx.x / 6;
  const int m0 = mt_blk * 64, n0 = nt_blk * 96;
  f32x4 acc[3][2];
#pragma unroll
  for (int nt = 0; nt < 3; ++nt)
#pragma unroll
    for (int mt = 0; mt < 2; ++mt) {
      size_t off = (size_t)(m0 + wm * 32 + mt * 16 + (lane & 15)) * D_ + n0 + wn * 48 + nt * 16 + 4 * (lane >> 4);
      f32x4 s = *(const f32x4*)(part + off);
#pragma unroll
      for (int sp = 1; sp < 4; ++sp) s += *(const f32x4*)(part + (size_t)sp * (S_ * D_) + off);
      acc[nt][mt] = s;
    }
  auto stage = [&](int kt, unsigned buf) {
    stage_a<G>(Ao, D_, m0, kt * 64, buf, wv, lane);
    stage_b<G>(wo + n0, D_, kt * 64, buf, wv, lane);
  };
  auto comp = [&](const char* buf) { gemm_compute<G, 3>(buf, acc, wm, wn * 3, lane); };
  gemm_main<G>(9, stage, lds, comp, wv);
#pragma unroll
  for (int nt = 0; nt < 3; ++nt)
#pragma unroll
    for (int mt = 0; mt < 2; ++mt) {
      size_t off = (size_t)(m0 + wm * 32 + mt * 16 + (lane & 15)) * D_ + n0 + wn * 48 + nt * 16 + 4 * (lane >> 4);
      u32x2 v = {pk2(acc[nt][mt][0], acc[nt][mt][1]), pk2(acc[nt][mt][2], acc[nt][mt][3])};
      *(u32x2*)(out + off) = v;
    }
}

extern "C" void kernel_launch(void* const* d_in, const int* in_sizes, int n_in, void* d_out, int out_size,
                              void* d_ws, size_t ws_size, hipStream_t stream) {
  const u16* x = (const u16*)d_in[0];
  const u16* wqkv = (const u16*)d_in[1];
  const u16* wo = (const u16*)d_in[2];
  const u16* wg = (const u16*)d_in[4];
  const u16* wu = (const u16*)d_in[5];
  const u16* wd = (const u16*)d_in[6];
  const u16* gw = (const u16*)d_in[7];
  char* ws = (char*)d_ws;
  u16* qkvp = (u16*)ws;
  float* gate = (float*)(ws + (size_t)16 * 1024 * 1024);
  u16* Ao = (u16*)(ws + (size_t)17 * 1024 * 1024);
  u16* Ah = (u16*)(ws + (size_t)20 * 1024 * 1024);
  float* part = (float*)(ws + (size_t)48 * 1024 * 1024);
  k_proj<<<256, 512, 0, stream>>>(x, wqkv, wg, wu, gw, qkvp, gate, Ah);
  k_mid<<<512, 256, 0, stream>>>(qkvp, gate, Ao, Ah, wd, part);
  k_out<<<192, 256, 0, stream>>>(Ao, wo, part, (u16*)d_out);
}
```

```cpp
#include <hip/hip_runtime.h>
#include <hip/hip_bf16.h>

typedef __attribute__((ext_vector_type(8))) short bf16x8;
typedef __attribute__((ext_vector_type(4))) short s16x4;
typedef __attribute__((ext_vector_type(4))) float f32x4;
typedef __attribute__((ext_vector_type(2))) float f32x2;
typedef __attribute__((ext_vector_type(16))) float f32x16;
typedef __attribute__((ext_vector_type(4))) unsigned u32x4;
typedef __attribute__((ext_vector_type(2))) unsigned u32x2;
typedef __attribute__((ext_vector_type(2))) __bf16 bf16x2_t;
typedef unsigned short u16;

#define DI __device__ __forceinline__
#define LDSP(T) __attribute__((address_space(3))) T

constexpr int S_ = 2048, D_ = 576, H_ = 32, DH_ = 18, F_ = 4608;

DI unsigned pk2(float lo, float hi) {
  f32x2 v = {lo, hi};
  bf16x2_t b = __builtin_convertvector(v, bf16x2_t);
  return __builtin_bit_cast(unsigned, b);
}
DI float rbf(float x) { return __uint_as_float(pk2(x, 0.f) << 16); }
DI float dot2bf(unsigned a, unsigned b, float c) {
  return __builtin_amdgcn_fdot2_f32_bf16(__builtin_bit_cast(bf16x2_t, a), __builtin_bit_cast(bf16x2_t, b), c, false);
}

DI void glds16(const void* sbase, unsigned voff, unsigned lds_base) {
  unsigned sv;
  asm volatile("s_mov_b32 %0, m0\n\ts_mov_b32 m0, %3\n\ts_nop 0\n\tglobal_load_lds_dwordx4 %1, %2\n\ts_mov_b32 m0, %0"
               : "=&s"(sv) : "v"(voff), "s"(sbase), "s"(lds_base) : "memory");
}
DI unsigned lds_addr(const char* p) { return (unsigned)(size_t)(LDSP(const char)*)p; }
DI s16x4 trread(const char* p) { return __builtin_amdgcn_ds_read_tr16_b64_v4i16((LDSP(s16x4)*)p); }
template <int N> DI void waitv() { asm volatile("s_waitcnt vmcnt(%0)" ::"n"(N) : "memory"); }
#define BARRIER() do { asm volatile("s_waitcnt lgkmcnt(0)" ::: "memory"); __builtin_amdgcn_s_barrier(); asm volatile("" ::: "memory"); } while (0)

#define SBAR() __builtin_amdgcn_sched_barrier(0)
template <int NBs>
DI unsigned bseg_off(int u, int ld) {
  int st = u >> 4, kblk = st / NBs, nblk = st - kblk * NBs;
  int rr = (u >> 1) & 7, hf = u & 1;
  int k = kblk * 8 + (rr ^ ((kblk & 1) << 2));
  return (unsigned)(k * ld + nblk * 16 + hf * 8) * 2u;
}
DI unsigned aseg_off(int u, int lda) {
  int row = u >> 2, ch = (u & 3) ^ ((row >> 2) & 3);
  return (unsigned)(row * lda + ch * 8) * 2u;
}
DI bf16x8 bfrag2(const char* lo, const char* hi) {
  s16x4 a = trread(lo), b = trread(hi);
  return __builtin_shufflevector(a, b, 0, 1, 2, 3, 4, 5, 6, 7);
}

template <int MT, int NT, int LMAX, int STAGE, class LdA, class LdB, class Piece, class WaitL, class Extra>
DI void gemm_loop(int nk, char* lds, f32x4 (&acc)[NT][MT], LdA ldA, LdB ldB, Piece piece, WaitL waitL, Extra extra) {
  static_assert(NT >= MT + 1 && NT >= LMAX, "schedule");
  const unsigned l0 = lds_addr(lds);
#pragma unroll
  for (int s = 0; s < 3; ++s)
#pragma unroll
    for (int i = 0; i < LMAX; ++i) piece(i, s, l0 + s * STAGE);
  waitL(2);
  BARRIER();
  bf16x8 af[MT], bw0, bw1;
#pragma unroll
  for (int mt = 0; mt < MT; ++mt) af[mt] = ldA(lds, mt);
  bw0 = ldB(lds, 0);
  bw1 = ldB(lds, 1);
  for (int k = 0; k < nk; ++k) {
    if (k + 2 < nk) waitL(1); else waitv<0>();
    BARRIER();
    const char* cur = lds + (k & 3) * STAGE;
    const char* nx = lds + ((k + 1) & 3) * STAGE;
    const unsigned dst = l0 + ((k + 3) & 3) * STAGE;
    const bool more = k + 3 < nk;
    bf16x8 an[MT];
#pragma unroll
    for (int nt = 0; nt < NT; ++nt) {
      bf16x8 b = (nt & 1) ? bw1 : bw0;
      bf16x8 nb = nt + 2 < NT ? ldB(cur, nt + 2) : ldB(nx, nt + 2 - NT);
      if (nt & 1) bw1 = nb; else bw0 = nb;
      if (nt >= NT - 1 - MT && nt < NT - 1) an[nt - (NT - 1 - MT)] = ldA(nx, nt - (NT - 1 - MT));
      if (nt < LMAX && more) piece(nt, k + 3, dst);
#pragma unroll
      for (int mt = 0; mt < MT; ++mt) acc[nt][mt] = __builtin_amdgcn_mfma_f32_16x16x32_bf16(b, af[mt], acc[nt][mt], 0, 0, 0);
      extra(nt, k, af);
      SBAR();
    }
#pragma unroll
    for (int mt = 0; mt < MT; ++mt) af[mt] = an[mt];
    if (NT & 1) { bf16x8 t = bw0; bw0 = bw1; bw1 = t; }
  }
  BARRIER();
}

constexpr float QSCALE = 0.23570226039551584f * 1.4426950408889634f;
constexpr int K1_STAGE = 16384 + 9216 + 9216 + 4096;

__global__ __launch_bounds__(512) void k_proj(const u16* __restrict__ x, const u16* __restrict__ wqkv,
                                              const u16* __restrict__ wg, const u16* __restrict__ wu,
                                              const u16* __restrict__ gate_w, u16* __restrict__ qkvp,
                                              float* __restrict__ gate, u16* __restrict__ Ah) {
  __shared__ __attribute__((aligned(16))) char lds[4 * K1_STAGE + 1152];
  const int tid = threadIdx.x, lane = tid & 63;
  const int wv = __builtin_amdgcn_readfirstlane(tid >> 6), wm = wv >> 1, wn = wv & 1;
  const int bid = blockIdx.x;
  const int nt_blk = (bid & 7) * 4 + ((bid >> 3) & 3), mt_blk = bid >> 5;
  const int m0 = mt_blk * 256, f0 = nt_blk * 144;
  const int coff = nt_blk == 31 ? 2 : 0;
  const int c0 = nt_blk * 54 - coff;
  char* gwl = lds + 4 * K1_STAGE;
  if (tid < 72) *(u32x4*)(gwl + tid * 16) = *(const u32x4*)(gate_w + tid * 8);

  f32x4 acc[11][4] = {};
  float z[4] = {0.f, 0.f, 0.f, 0.f};
  const u16* xa = x + (size_t)m0 * D_;
  const unsigned aof0 = aseg_off(wv * 64 + lane, D_), aof1 = aseg_off((8 + wv) * 64 + lane, D_);
  unsigned bof[3];
  const u16* bsrc[3];
  int bld[3];
#pragma unroll
  for (int i = 0; i < 3; ++i) {
    int jj = i * 8 + wv;
    if (jj < 18) {
      int j = jj < 9 ? jj : jj - 9;
      bof[i] = bseg_off<9>(j * 64 + lane, F_);
      bsrc[i] = (jj < 9 ? wg : wu) + f0;
      bld[i] = F_;
    } else {
      int u = ((jj - 18) & 3) * 64 + lane;
      if ((u & 1) && ((u >> 4) & 3) == 3) u -= 49;
      bof[i] = bseg_off<4>(u, 3 * D_);
      bsrc[i] = wqkv + c0;
      bld[i] = 3 * D_;
    }
  }
  auto piece = [&](int i, int kt, unsigned buf) {
    if (i == 0) glds16(xa + kt * 32, aof0, buf + wv * 1024);
    else if (i == 1) glds16(xa + kt * 32, aof1, buf + (8 + wv) * 1024);
    else if (i < 4 || wv < 6) glds16(bsrc[i - 2] + (size_t)kt * 32 * bld[i - 2], bof[i - 2], buf + 16384 + ((i - 2) * 8 + wv) * 1024);
  };
  auto waitL = [&](int n) {
    if (wv < 6) { if (n == 2) waitv<10>(); else waitv<5>(); }
    else { if (n == 2) waitv<8>(); else waitv<4>(); }
  };
  const int g = lane >> 4;
  const int bl = ((lane & 15) >> 2) * 32 + (lane & 3) * 8;
  const int aoff = (wm * 64 + (lane & 15)) * 64 + ((g ^ ((lane >> 2) & 3)) << 4);
  const int bgu = 16384 + wn * 9216 + g * 9 * 256 + bl;
  const int bqq = 16384 + 18432 + (g * 4 + wn * 2) * 256 + bl;
  const int hlo = (g & 1) << 7, hhi = hlo ^ 128;
  auto ldA = [&](const char* st, int mt) { return *(const bf16x8*)(st + aoff + mt * 1024); };
  auto ldB = [&](const char* st, int nt) {
    const char* bp = st + (nt < 9 ? bgu + nt * 256 : bqq + (nt - 9) * 256);
    return bfrag2(bp + hlo, bp + hhi);
  };
  u32x4 gw;
  auto extra = [&](int nt, int kt, const bf16x8 (&af)[4]) {
    if (nt == 0) gw = *(const u32x4*)(gwl + kt * 64 + g * 16);
    if (nt >= 2 && nt < 6) {
      u32x4 a4 = __builtin_bit_cast(u32x4, af[nt - 2]);
#pragma unroll
      for (int j = 0; j < 4; ++j) z[nt - 2] = dot2bf(a4[j], gw[j], z[nt - 2]);
    }
  };
  gemm_loop<4, 11, 5, K1_STAGE>(18, lds, acc, ldA, ldB, piece, waitL, extra);
  int tz = threadIdx.x;
  asm volatile("" : "+v"(tz));
  const int lz = tz & 63, gz = lz >> 4;

  float gv[4];
#pragma unroll
  for (int mt = 0; mt < 4; ++mt) {
    float s = z[mt];
    s += __shfl_xor(s, 16);
    s += __shfl_xor(s, 32);
    s = rbf(s);
    gv[mt] = rbf(1.f / (1.f + __expf(-s)));
  }
  if (nt_blk == 0 && wn == 0 && lz < 16) {
#pragma unroll
    for (int mt = 0; mt < 4; ++mt) gate[m0 + wm * 64 + mt * 16 + lz] = gv[mt];
  }

  char* xch = lds + wm * 18720;
  char* img = lds + 4 * 18720;
  {
    u32x4 zz = {0u, 0u, 0u, 0u};
#pragma unroll
    for (int i = 0; i < 6; ++i) *(u32x4*)(img + (i * 512 + tz) * 16) = zz;
  }
  if (wn == 1) {
#pragma unroll
    for (int nt = 0; nt < 9; ++nt)
#pragma unroll
      for (int mt = 0; mt < 4; ++mt) {
        u32x2 v = {pk2(acc[nt][mt][0], acc[nt][mt][1]), pk2(acc[nt][mt][2], acc[nt][mt][3])};
        *(u32x2*)(xch + (nt * 4 + mt) * 520 + lz * 8) = v;
      }
  }
  __syncthreads();
  {
    const int hg0 = nt_blk * 3;
#pragma unroll
    for (int t2 = 0; t2 < 2; ++t2)
#pragma unroll
      for (int r = 0; r < 4; ++r) {
        int c = wn * 32 + t2 * 16 + 4 * gz + r - coff;
        if (c >= 0 && c < 54) {
          int hl = c / 18, d = c - hl * 18;
          int which = (hg0 + hl) >> 5;
          float sc = which == 0 ? QSCALE : 1.f;
#pragma unroll
          for (int mt = 0; mt < 4; ++mt) {
            int tok = wm * 64 + mt * 16 + (lz & 15);
            int ch = d >> 3;
            if (which == 1) ch ^= (tok >> 2) & 3;
            *(u16*)(img + (hl * 256 + tok) * 64 + ch * 16 + (d & 7) * 2) = (u16)pk2(acc[9 + t2][mt][r] * sc, 0.f);
          }
        }
      }
    for (int i = tz; i < 768; i += 512) {
      int hl = i >> 8;
      if (((hg0 + hl) >> 5) == 2) *(u16*)(img + i * 64 + 36) = (u16)0x3f80;
    }
  }
  if (wn == 0) {
#pragma unroll
    for (int nt = 0; nt < 9; ++nt)
#pragma unroll
      for (int mt = 0; mt < 4; ++mt) {
        float og = rbf(1.f - gv[mt]);
        u32x2* slot = (u32x2*)(xch + (nt * 4 + mt) * 520 + lz * 8);
        u32x2 uv = *slot;
        float h0 = rbf(acc[nt][mt][0]) * __uint_as_float(uv[0] << 16) * og;
        float h1 = rbf(acc[nt][mt][1]) * __uint_as_float(uv[0] & 0xffff0000u) * og;
        float h2 = rbf(acc[nt][mt][2]) * __uint_as_float(uv[1] << 16) * og;
        float h3 = rbf(acc[nt][mt][3]) * __uint_as_float(uv[1] & 0xffff0000u) * og;
        *slot = (u32x2){pk2(h0, h1), pk2(h2, h3)};
      }
  }
  __syncthreads();
#pragma unroll
  for (int i = 0; i < 9; ++i) {
    int u = i * 512 + tz;
    int row = u / 18, cu = u - row * 18;
    const char* src = lds + (row >> 6) * 18720 + ((cu >> 1) * 4 + ((row >> 4) & 3)) * 520 + ((cu & 1) * 32 + (row & 15)) * 8;
    u32x2 v0 = *(const u32x2*)src, v1 = *(const u32x2*)(src + 128);
    u32x4 v = {v0[0], v0[1], v1[0], v1[1]};
    *(u32x4*)(Ah + (size_t)(m0 + row) * F_ + f0 + cu * 8) = v;
  }
#pragma unroll
  for (int i = 0; i < 6; ++i) {
    int u = i * 512 + tz;
    int hl = u >> 10, rem = u & 1023;
    u32x4 v = *(const u32x4*)(img + u * 16);
    *(u32x4*)(qkvp + ((size_t)(nt_blk * 3 + hl) * S_ + m0) * 32 + rem * 8) = v;
  }
}

DI void attn_stage(const u16* __restrict__ Kh, const u16* __restrict__ Vh, int c, unsigned buf, int wv, int lane) {
#pragma unroll
  for (int i = 0; i < 2; ++i) glds16(Kh + (size_t)c * 4096, ((i * 4 + wv) * 64 + lane) * 16u, buf + (i * 4 + wv) * 1024);
#pragma unroll
  for (int i = 0; i < 2; ++i)
    glds16(Vh + (size_t)c * 4096, ((i * 4 + wv) * 64 + lane) * 16u, buf + 8192 + (i * 4 + wv) * 1024);
}

DI float halfswap_max(float m) {
  auto rr = __builtin_amdgcn_permlane32_swap(__float_as_uint(m), __float_as_uint(m), false, false);
  return fmaxf(__uint_as_float(rr[0]), __uint_as_float(rr[1]));
}
DI float max16(const f32x16& s) {
  float mx = s[0];
#pragma unroll
  for (int r = 1; r < 16; ++r) mx = fmaxf(mx, s[r]);
  return halfswap_max(mx);
}
#define MFMA32(a, b, c) __builtin_amdgcn_mfma_f32_32x32x16_bf16((a), (b), (c), 0, 0, 0)

DI void exp_pack(const f32x16& s, bf16x8& p0, bf16x8& p1) {
  u32x4 a, b;
#pragma unroll
  for (int r = 0; r < 4; ++r) a[r] = pk2(__builtin_amdgcn_exp2f(s[2 * r]), __builtin_amdgcn_exp2f(s[2 * r + 1]));
#pragma unroll
  for (int r = 0; r < 4; ++r) b[r] = pk2(__builtin_amdgcn_exp2f(s[8 + 2 * r]), __builtin_amdgcn_exp2f(s[9 + 2 * r]));
  p0 = __builtin_bit_cast(bf16x8, a);
  p1 = __builtin_bit_cast(bf16x8, b);
}

#define EX2(x) __builtin_amdgcn_exp2f(x)
#define PHASE(S, PA, PB, M1, M2, M3, M4)                                              \
  do {                                                                                \
    M1; SBAR(); PA[0] = pk2(EX2(S[0]), EX2(S[1])); PA[1] = pk2(EX2(S[2]), EX2(S[3])); SBAR();  \
    M2; SBAR(); PA[2] = pk2(EX2(S[4]), EX2(S[5])); PA[3] = pk2(EX2(S[6]), EX2(S[7])); SBAR();  \
    M3; SBAR(); PB[0] = pk2(EX2(S[8]), EX2(S[9])); PB[1] = pk2(EX2(S[10]), EX2(S[11])); SBAR(); \
    M4; SBAR(); PB[2] = pk2(EX2(S[12]), EX2(S[13])); PB[3] = pk2(EX2(S[14]), EX2(S[15])); SBAR(); \
  } while (0)
#define BC8(x) __builtin_bit_cast(bf16x8, x)
DI void attn_fast(const u16* __restrict__ Kh, const u16* __restrict__ Vh, const bf16x8 (&qf)[2][2], char* lds,
                  f32x16 (&o)[2], int wv, int lane) {
  const int r32 = lane & 31, h = lane >> 5;
  const int koff0 = r32 * 64 + ((h ^ ((r32 >> 2) & 3)) << 4);
  const int koff1 = r32 * 64 + (((2 + h) ^ ((r32 >> 2) & 3)) << 4);
  const int voff = 8192 + (4 * h + ((lane & 15) >> 2)) * 64 + ((lane >> 4) & 1) * 32 + (lane & 3) * 8;
  const unsigned l0 = lds_addr(lds);
  f32x16 negm[2], sa, sb;
#pragma unroll
  for (int a = 0; a < 2; ++a)
#pragma unroll
    for (int r = 0; r < 16; ++r) { o[a][r] = 0.f; negm[a][r] = 0.f; }
  attn_stage(Kh, Vh, 0, l0, wv, lane);
  attn_stage(Kh, Vh, 1, l0 + 16384, wv, lane);
  attn_stage(Kh, Vh, 2, l0 + 32768, wv, lane);
  waitv<8>();
  BARRIER();
  bf16x8 kc0 = *(const bf16x8*)(lds + koff0), kc1 = *(const bf16x8*)(lds + koff1);
  {
#pragma unroll
    for (int a = 0; a < 2; ++a) {
      f32x16 s = MFMA32(kc0, qf[a][0], negm[a]);
      s = MFMA32(kc1, qf[a][1], s);
      float mx = max16(s);
#pragma unroll
      for (int r = 0; r < 16; ++r) { negm[a][r] = -mx; if (a == 0) sa[r] = s[r] - mx; }
    }
  }
  u32x4 pa0, pa1, pb0 = {0u, 0u, 0u, 0u}, pb1 = {0u, 0u, 0u, 0u};
  bf16x8 vp0 = {0, 0, 0, 0, 0, 0, 0, 0}, vp1 = {0, 0, 0, 0, 0, 0, 0, 0};
  for (int c = 0; c < 16; ++c) {
    if (c + 2 < 16) waitv<4>(); else waitv<0>();
    BARRIER();
    if (c + 3 < 16) attn_stage(Kh, Vh, c + 3, l0 + ((c + 3) & 3) * 16384, wv, lane);
    const char* buf = lds + (c & 3) * 16384;
    const char* bufn = lds + ((c + 1) & 3) * 16384;
#pragma unroll
    for (int t = 0; t < 4; ++t) {
      const char* kb = t < 3 ? buf + (t + 1) * 2048 : bufn;
      const char* vb = buf + t * 2048 + voff;
      bf16x8 kn0 = *(const bf16x8*)(kb + koff0), kn1 = *(const bf16x8*)(kb + koff1);
      s16x4 v0 = trread(vb), v1 = trread(vb + 512), v2 = trread(vb + 1024), v3 = trread(vb + 1536);
      bf16x8 vc0 = __builtin_shufflevector(v0, v1, 0, 1, 2, 3, 4, 5, 6, 7);
      bf16x8 vc1 = __builtin_shufflevector(v2, v3, 0, 1, 2, 3, 4, 5, 6, 7);
      SBAR();
      PHASE(sa, pa0, pa1, sb = MFMA32(kc0, qf[1][0], negm[1]), o[1] = MFMA32(vp0, BC8(pb0), o[1]),
            sb = MFMA32(kc1, qf[1][1], sb), o[1] = MFMA32(vp1, BC8(pb1), o[1]));
      PHASE(sb, pb0, pb1, sa = MFMA32(kn0, qf[0][0], negm[0]), o[0] = MFMA32(vc0, BC8(pa0), o[0]),
            sa = MFMA32(kn1, qf[0][1], sa), o[0] = MFMA32(vc1, BC8(pa1), o[0]));
      kc0 = kn0; kc1 = kn1; vp0 = vc0; vp1 = vc1;
    }
  }
  o[1] = MFMA32(vp0, BC8(pb0), o[1]);
  o[1] = MFMA32(vp1, BC8(pb1), o[1]);
}

DI void attn_safe(const u16* __restrict__ Kh, const u16* __restrict__ Vh, const bf16x8 (&qf)[2][2], char* lds,
                  f32x16 (&o)[2], int wv, int lane) {
  const int r32 = lane & 31, h = lane >> 5;
  const int koff0 = r32 * 64 + ((h ^ ((r32 >> 2) & 3)) << 4);
  const int koff1 = r32 * 64 + (((2 + h) ^ ((r32 >> 2) & 3)) << 4);
  const int voff = 8192 + (4 * h + ((lane & 15) >> 2)) * 64 + ((lane >> 4) & 1) * 32 + (lane & 3) * 8;
  const unsigned l0 = lds_addr(lds);
  f32x16 zero;
  float m[2] = {-INFINITY, -INFINITY};
#pragma unroll
  for (int r = 0; r < 16; ++r) { o[0][r] = 0.f; o[1][r] = 0.f; zero[r] = 0.f; }
  for (int c = 0; c < 16; ++c) {
    BARRIER();
    attn_stage(Kh, Vh, c, l0, wv, lane);
    waitv<0>();
    BARRIER();
    for (int t = 0; t < 4; ++t) {
      const char* kb = lds + t * 2048;
      bf16x8 k0 = *(const bf16x8*)(kb + koff0), k1 = *(const bf16x8*)(kb + koff1);
      s16x4 v0 = trread(kb + voff), v1 = trread(kb + voff + 512), v2 = trread(kb + voff + 1024), v3 = trread(kb + voff + 1536);
      bf16x8 va = __builtin_shufflevector(v0, v1, 0, 1, 2, 3, 4, 5, 6, 7);
      bf16x8 vb = __builtin_shufflevector(v2, v3, 0, 1, 2, 3, 4, 5, 6, 7);
#pragma unroll
      for (int a = 0; a < 2; ++a) {
        f32x16 s = MFMA32(k0, qf[a][0], zero);
        s = MFMA32(k1, qf[a][1], s);
        float mn = fmaxf(m[a], max16(s));
        float al = __builtin_amdgcn_exp2f(m[a] - mn);
        m[a] = mn;
#pragma unroll
        for (int r = 0; r < 16; ++r) { o[a][r] *= al; s[r] -= mn; }
        bf16x8 p0, p1;
        exp_pack(s, p0, p1);
        o[a] = MFMA32(va, p0, o[a]);
        o[a] = MFMA32(vb, p1, o[a]);
      }
    }
  }
}

DI void attn_block(int bid, const u16* __restrict__ qkvp, const float* __restrict__ gate, u16* __restrict__ Ao,
                   char* lds) {
  const int tid = threadIdx.x, lane = tid & 63, r32 = lane & 31, h = lane >> 5;
  const int wv = __builtin_amdgcn_readfirstlane(tid >> 6);
  const int head = (bid & 7) * 4 + ((bid >> 3) & 3), qb = bid >> 5;
  const u16* Qh = qkvp + (size_t)head * (S_ * 32);
  const u16* Kh = qkvp + (size_t)(H_ + head) * (S_ * 32);
  const u16* Vh = qkvp + (size_t)(2 * H_ + head) * (S_ * 32);
  const int qrow0 = qb * 256 + wv * 64 + r32;
  bf16x8 qf[2][2];
#pragma unroll
  for (int a = 0; a < 2; ++a) {
    qf[a][0] = *(const bf16x8*)(Qh + (size_t)(qrow0 + a * 32) * 32 + h * 8);
    qf[a][1] = *(const bf16x8*)(Qh + (size_t)(qrow0 + a * 32) * 32 + 16 + h * 8);
  }
  float gq[2] = {gate[qrow0], gate[qrow0 + 32]};
  asm volatile("" ::"v"(qf[0][0]), "v"(qf[0][1]), "v"(qf[1][0]), "v"(qf[1][1]), "v"(gq[0]), "v"(gq[1]));
  f32x16 o[2];
  attn_fast(Kh, Vh, qf, lds, o, wv, lane);
  bool bad = false;
#pragma unroll
  for (int a = 0; a < 2; ++a)
#pragma unroll
    for (int r = 0; r < 11; ++r) bad |= !(fabsf(o[a][r]) < 1e30f);
  if (__syncthreads_or(bad)) attn_safe(Kh, Vh, qf, lds, o, wv, lane);
#pragma unroll
  for (int a = 0; a < 2; ++a) {
    float l = __shfl(o[a][10], r32);
    float sc = gq[a] / l;
    u16* dst = Ao + (size_t)(qrow0 + a * 32) * D_ + head * DH_ + h * 4;
    *(unsigned*)(dst + 0) = pk2(o[a][0] * sc, o[a][1] * sc);
    *(unsigned*)(dst + 2) = pk2(o[a][2] * sc, o[a][3] * sc);
    *(unsigned*)(dst + 8) = pk2(o[a][4] * sc, o[a][5] * sc);
    *(unsigned*)(dst + 10) = pk2(o[a][6] * sc, o[a][7] * sc);
    if (h == 0) *(unsigned*)(dst + 16) = pk2(o[a][8] * sc, o[a][9] * sc);
  }
}

constexpr int K2_STAGE = 8192 + 9216;
DI void down_block(int bid, const u16* __restrict__ Ah, const u16* __restrict__ wd, float* __restrict__ part,
                   char* lds) {
  const int tid = threadIdx.x, lane = tid & 63;
  const int wv = __builtin_amdgcn_readfirstlane(tid >> 6);
  const int mt_blk = (bid & 7) * 2 + ((bid >> 3) & 1), rest = bid >> 4;
  const int nt_blk = rest & 3, sp = rest >> 2;
  const int m0 = mt_blk * 128, n0 = nt_blk * 144;
  f32x4 acc[9][2] = {};
  const u16* pa = Ah + (size_t)m0 * F_ + sp * 1152;
  const u16* pb = wd + (size_t)sp * 1152 * D_ + n0;
  const unsigned aof0 = aseg_off(wv * 64 + lane, F_), aof1 = aseg_off((4 + wv) * 64 + lane, F_);
  const unsigned bof0 = bseg_off<9>(wv * 64 + lane, D_), bof1 = bseg_off<9>((4 + wv) * 64 + lane, D_),
                 bof2 = bseg_off<9>(8 * 64 + lane, D_);
  auto piece = [&](int i, int kt, unsigned buf) {
    if (i == 0) glds16(pa + kt * 32, aof0, buf + wv * 1024);
    else if (i == 1) glds16(pa + kt * 32, aof1, buf + (4 + wv) * 1024);
    else if (i == 2) glds16(pb + (size_t)kt * 32 * D_, bof0, buf + 8192 + wv * 1024);
    else if (i == 3) glds16(pb + (size_t)kt * 32 * D_, bof1, buf + 8192 + (4 + wv) * 1024);
    else if (wv == 0) glds16(pb + (size_t)kt * 32 * D_, bof2, buf + 8192 + 8 * 1024);
  };
  auto waitL = [&](int n) {
    if (wv == 0) { if (n == 2) waitv<10>(); else waitv<5>(); }
    else { if (n == 2) waitv<8>(); else waitv<4>(); }
  };
  const int g = lane >> 4;
  const int aoff = (wv * 32 + (lane & 15)) * 64 + ((g ^ ((lane >> 2) & 3)) << 4);
  const int boff = 8192 + g * 9 * 256 + ((lane & 15) >> 2) * 32 + (lane & 3) * 8;
  const int hlo = (g & 1) << 7, hhi = hlo ^ 128;
  auto ldA = [&](const char* st, int mt) { return *(const bf16x8*)(st + aoff + mt * 1024); };
  auto ldB = [&](const char* st, int nt) { return bfrag2(st + boff + nt * 256 + hlo, st + boff + nt * 256 + hhi); };
  auto extra = [&](int, int, const bf16x8 (&)[2]) {};
  gemm_loop<2, 9, 5, K2_STAGE>(36, lds, acc, ldA, ldB, piece, waitL, extra);
  float* dst = part + (size_t)sp * (S_ * D_);
#pragma unroll
  for (int nt = 0; nt < 9; ++nt)
#pragma unroll
    for (int mt = 0; mt < 2; ++mt) {
      int tok = m0 + wv * 32 + mt * 16 + (lane & 15);
      int n = n0 + nt * 16 + 4 * (lane >> 4);
      *(f32x4*)(dst + (size_t)tok * D_ + n) = acc[nt][mt];
    }
}

__global__ __launch_bounds__(256, 2) void k_mid(const u16* __restrict__ qkvp, const float* __restrict__ gate,
                                                u16* __restrict__ Ao, const u16* __restrict__ Ah,
                                                const u16* __restrict__ wd, float* __restrict__ part) {
  __shared__ __attribute__((aligned(16))) char lds[4 * K2_STAGE > 65536 ? 4 * K2_STAGE : 65536];
  const int bid = blockIdx.x;
  if (bid < 256) attn_block(bid, qkvp, gate, Ao, lds);
  else down_block(bid - 256, Ah, wd, part, lds);
}

constexpr int K3_STAGE = 4096 + 6144;
__global__ __launch_bounds__(256) void k_out(const u16* __restrict__ Ao, const u16* __restrict__ wo,
                                             const float* __restrict__ part, u16* __restrict__ out) {
  __shared__ __attribute__((aligned(16))) char lds[4 * K3_STAGE];
  const int tid = threadIdx.x, lane = tid & 63;
  const int wv = __builtin_amdgcn_readfirstlane(tid >> 6), wm = wv >> 1, wn = wv & 1;
  const int nt_blk = blockIdx.x % 6, mt_blk = blockIdx.x / 6;
  const int m0 = mt_blk * 64, n0 = nt_blk * 96;
  f32x4 acc[3][2];
#pragma unroll
  for (int nt = 0; nt < 3; ++nt)
#pragma unroll
    for (int mt = 0; mt < 2; ++mt) {
      size_t off = (size_t)(m0 + wm * 32 + mt * 16 + (lane & 15)) * D_ + n0 + wn * 48 + nt * 16 + 4 * (lane >> 4);
      f32x4 s = *(const f32x4*)(part + off);
#pragma unroll
      for (int sp = 1; sp < 4; ++sp) s += *(const f32x4*)(part + (size_t)sp * (S_ * D_) + off);
      acc[nt][mt] = s;
    }
  const u16* pa = Ao + (size_t)m0 * D_;
  const u16* pb = wo + n0;
  const unsigned aof0 = aseg_off(wv * 64 + lane, D_);
  const unsigned bof0 = bseg_off<6>(wv * 64 + lane, D_), bof1 = bseg_off<6>((4 + (wv & 1)) * 64 + lane, D_);
  auto piece = [&](int i, int kt, unsigned buf) {
    if (i == 0) glds16(pa + kt * 32, aof0, buf + wv * 1024);
    else if (i == 1) glds16(pb + (size_t)kt * 32 * D_, bof0, buf + 4096 + wv * 1024);
    else if (wv < 2) glds16(pb + (size_t)kt * 32 * D_, bof1, buf + 4096 + (4 + wv) * 1024);
  };
  auto waitL = [&](int n) {
    if (wv < 2) { if (n == 2) waitv<6>(); else waitv<3>(); }
    else { if (n == 2) waitv<4>(); else waitv<2>(); }
  };
  const int g = lane >> 4;
  const int aoff = (wm * 32 + (lane & 15)) * 64 + ((g ^ ((lane >> 2) & 3)) << 4);
  const int boff = 4096 + (g * 6 + wn * 3) * 256 + ((lane & 15) >> 2) * 32 + (lane & 3) * 8;
  const int hlo = (g & 1) << 7, hhi = hlo ^ 128;
  auto ldA = [&](const char* st, int mt) { return *(const bf16x8*)(st + aoff + mt * 1024); };
  auto ldB = [&](const char* st, int nt) { return bfrag2(st + boff + nt * 256 + hlo, st + boff + nt * 256 + hhi); };
  auto extra = [&](int, int, const bf16x8 (&)[2]) {};
  gemm_loop<2, 3, 3, K3_STAGE>(18, lds, acc, ldA, ldB, piece, waitL, extra);
#pragma unroll
  for (int nt = 0; nt < 3; ++nt)
#pragma unroll
    for (int mt = 0; mt < 2; ++mt) {
      size_t off = (size_t)(m0 + wm * 32 + mt * 16 + (lane & 15)) * D_ + n0 + wn * 48 + nt * 16 + 4 * (lane >> 4);
      u32x2 v = {pk2(acc[nt][mt][0], acc[nt][mt][1]), pk2(acc[nt][mt][2], acc[nt][mt][3])};
      *(u32x2*)(out + off) = v;
    }
}

extern "C" void kernel_launch(void* const* d_in, const int* in_sizes, int n_in, void* d_out, int out_size,
                              void* d_ws, size_t ws_size, hipStream_t stream) {
  const u16* x = (const u16*)d_in[0];
  const u16* wqkv = (const u16*)d_in[1];
  const u16* wo = (const u16*)d_in[2];
  const u16* wg = (const u16*)d_in[4];
  const u16* wu = (const u16*)d_in[5];
  const u16* wd = (const u16*)d_in[6];
  const u16* gw = (const u16*)d_in[7];
  char* ws = (char*)d_ws;
  u16* qkvp = (u16*)ws;
  float* gate = (float*)(ws + (size_t)16 * 1024 * 1024);
  u16* Ao = (u16*)(ws + (size_t)17 * 1024 * 1024);
  u16* Ah = (u16*)(ws + (size_t)20 * 1024 * 1024);
  float* part = (float*)(ws + (size_t)48 * 1024 * 1024);
  k_proj<<<256, 512, 0, stream>>>(x, wqkv, wg, wu, gw, qkvp, gate, Ah);
  k_mid<<<512, 256, 0, stream>>>(qkvp, gate, Ao, Ah, wd, part);
  k_out<<<192, 256, 0, stream>>>(Ao, wo, part, (u16*)d_out);
}
```

```cpp
#include <hip/hip_runtime.h>
#include <hip/hip_bf16.h>

typedef __attribute__((ext_vector_type(8))) short bf16x8;
typedef __attribute__((ext_vector_type(4))) short s16x4;
typedef __attribute__((ext_vector_type(4))) float f32x4;
typedef __attribute__((ext_vector_type(2))) float f32x2;
typedef __attribute__((ext_vector_type(16))) float f32x16;
typedef __attribute__((ext_vector_type(4))) unsigned u32x4;
typedef __attribute__((ext_vector_type(2))) unsigned u32x2;
typedef __attribute__((ext_vector_type(2))) __bf16 bf16x2_t;
typedef unsigned short u16;

#define DI __device__ __forceinline__
#define LDSP(T) __attribute__((address_space(3))) T

constexpr int S_ = 2048, D_ = 576, H_ = 32, DH_ = 18, F_ = 4608;

DI unsigned pk2(float lo, float hi) {
  f32x2 v = {lo, hi};
  bf16x2_t b = __builtin_convertvector(v, bf16x2_t);
  return __builtin_bit_cast(unsigned, b);
}
DI float rbf(float x) { return __uint_as_float(pk2(x, 0.f) << 16); }
DI float dot2bf(unsigned a, unsigned b, float c) {
  return __builtin_amdgcn_fdot2_f32_bf16(__builtin_bit_cast(bf16x2_t, a), __builtin_bit_cast(bf16x2_t, b), c, false);
}

DI void glds16(const void* sbase, unsigned voff, unsigned lds_base) {
  unsigned sv;
  asm volatile("s_mov_b32 %0, m0\n\ts_mov_b32 m0, %3\n\ts_nop 0\n\tglobal_load_lds_dwordx4 %1, %2\n\ts_mov_b32 m0, %0"
               : "=&s"(sv) : "v"(voff), "s"(sbase), "s"(lds_base) : "memory");
}
DI unsigned lds_addr(const char* p) { return (unsigned)(size_t)(LDSP(const char)*)p; }
DI s16x4 trread(const char* p) { return __builtin_amdgcn_ds_read_tr16_b64_v4i16((LDSP(s16x4)*)p); }
template <int N> DI void waitv() { asm volatile("s_waitcnt vmcnt(%0)" ::"n"(N) : "memory"); }
#define BARRIER() do { asm volatile("s_waitcnt lgkmcnt(0)" ::: "memory"); __builtin_amdgcn_s_barrier(); asm volatile("" ::: "memory"); } while (0)

#define SBAR() __builtin_amdgcn_sched_barrier(0)
template <int NBs>
DI unsigned bseg_off(int u, int ld) {
  int st = u >> 4, kblk = st / NBs, nblk = st - kblk * NBs;
  int rr = (u >> 1) & 7, hf = u & 1;
  int k = kblk * 8 + (rr ^ ((kblk & 1) << 2));
  return (unsigned)(k * ld + nblk * 16 + hf * 8) * 2u;
}
DI unsigned aseg_off(int u, int lda) {
  int row = u >> 2, ch = (u & 3) ^ ((-(row >> 2)) & 3);
  return (unsigned)(row * lda + ch * 8) * 2u;
}
DI bf16x8 bfrag2(const char* lo, const char* hi) {
  s16x4 a = trread(lo), b = trread(hi);
  return __builtin_shufflevector(a, b, 0, 1, 2, 3, 4, 5, 6, 7);
}

template <int MT, int NT, int LMAX, int STAGE, int KS, class LdA, class LdB, class Piece, class WaitL, class Extra>
DI void gemm_loop(int nsup, char* lds, f32x4 (&acc)[NT][MT], LdA ldA, LdB ldB, Piece piece, WaitL waitL, Extra extra) {
  static_assert(NT >= MT + 1 && NT >= LMAX, "schedule");
  const unsigned l0 = lds_addr(lds);
#pragma unroll
  for (int s = 0; s < 3 * KS; ++s)
#pragma unroll
    for (int i = 0; i < LMAX; ++i) piece(i, s, l0 + s * STAGE);
  waitL(2);
  BARRIER();
  bf16x8 af[MT], bw0, bw1;
#pragma unroll
  for (int mt = 0; mt < MT; ++mt) af[mt] = ldA(lds, mt);
  bw0 = ldB(lds, 0);
  bw1 = ldB(lds, 1);
  for (int j = 0; j < nsup; ++j) {
    if (j + 2 < nsup) waitL(1); else waitv<0>();
    BARRIER();
    const char* base = lds + (j & 3) * (KS * STAGE);
    const char* nbase = lds + ((j + 1) & 3) * (KS * STAGE);
    const unsigned dst = l0 + ((j + 3) & 3) * (KS * STAGE);
    const bool more = j + 3 < nsup;
#pragma unroll
    for (int ks = 0; ks < KS; ++ks) {
      const char* cur = base + ks * STAGE;
      const char* nx = ks + 1 < KS ? base + (ks + 1) * STAGE : nbase;
      bf16x8 an[MT];
#pragma unroll
      for (int nt = 0; nt < NT; ++nt) {
        bf16x8 b = (nt & 1) ? bw1 : bw0;
        bf16x8 nb = nt + 2 < NT ? ldB(cur, nt + 2) : ldB(nx, nt + 2 - NT);
        if (nt & 1) bw1 = nb; else bw0 = nb;
        if (nt >= NT - 1 - MT && nt < NT - 1) an[nt - (NT - 1 - MT)] = ldA(nx, nt - (NT - 1 - MT));
        if (nt < LMAX && more) piece(nt, (j + 3) * KS + ks, dst + ks * STAGE);
#pragma unroll
        for (int mt = 0; mt < MT; ++mt) acc[nt][mt] = __builtin_amdgcn_mfma_f32_16x16x32_bf16(b, af[mt], acc[nt][mt], 0, 0, 0);
        if (more) extra(nt, (j + 3) * KS + ks, dst + ks * STAGE - l0);
        SBAR();
      }
#pragma unroll
      for (int mt = 0; mt < MT; ++mt) af[mt] = an[mt];
      if (NT & 1) { bf16x8 t = bw0; bw0 = bw1; bw1 = t; }
    }
  }
  BARRIER();
}

constexpr float QSCALE = 0.23570226039551584f * 1.4426950408889634f;
constexpr int K1_STAGE = 16384 + 9216 + 9216 + 4096;

__global__ __launch_bounds__(512) void k_proj(const u16* __restrict__ x, const u16* __restrict__ wqkv,
                                              const u16* __restrict__ wg, const u16* __restrict__ wu,
                                              const u16* __restrict__ gate_w, u16* __restrict__ qkvp,
                                              float* __restrict__ gate, u16* __restrict__ Ah) {
  __shared__ __attribute__((aligned(16))) char lds[4 * K1_STAGE + 1152];
  const int tid = threadIdx.x, lane = tid & 63;
  const int wv = __builtin_amdgcn_readfirstlane(tid >> 6), wm = wv >> 1, wn = wv & 1;
  const int bid = blockIdx.x;
  const int nt_blk = (bid & 7) * 4 + ((bid >> 3) & 3), mt_blk = bid >> 5;
  const int m0 = mt_blk * 256, f0 = nt_blk * 144;
  const int coff = nt_blk == 31 ? 2 : 0;
  const int c0 = nt_blk * 54 - coff;
  char* gwl = lds + 4 * K1_STAGE;
  if (tid < 72) *(u32x4*)(gwl + tid * 16) = *(const u32x4*)(gate_w + tid * 8);

  f32x4 acc[11][4] = {};
  const u16* xa = x + (size_t)m0 * D_;
  const unsigned aof0 = aseg_off(wv * 64 + lane, D_), aof1 = aseg_off((8 + wv) * 64 + lane, D_);
  unsigned bof[3];
  const u16* bsrc[3];
  int bld[3];
#pragma unroll
  for (int i = 0; i < 3; ++i) {
    int jj = i * 8 + wv;
    if (jj < 18) {
      int j = jj < 9 ? jj : jj - 9;
      bof[i] = bseg_off<9>(j * 64 + lane, F_);
      bsrc[i] = (jj < 9 ? wg : wu) + f0;
      bld[i] = F_;
    } else {
      bof[i] = bseg_off<4>(((jj - 18) & 3) * 64 + lane, 3 * D_);
      bsrc[i] = wqkv + c0;
      bld[i] = 3 * D_;
    }
  }
  const bool padlane = (lane & 1) && (lane >> 4) == 3;
  auto piece = [&](int i, int kt, unsigned buf) {
    if (i == 0) glds16(xa + kt * 32, aof0, buf + wv * 1024);
    else if (i == 1) glds16(xa + kt * 32, aof1, buf + (8 + wv) * 1024);
    else if (i < 4) glds16(bsrc[i - 2] + (size_t)kt * 32 * bld[i - 2], bof[i - 2], buf + 16384 + ((i - 2) * 8 + wv) * 1024);
    else if (wv < 2) glds16(bsrc[2] + (size_t)kt * 32 * bld[2], bof[2], buf + 16384 + (16 + wv) * 1024);
    else if (wv < 6) { if (!padlane) glds16(bsrc[2] + (size_t)kt * 32 * bld[2], bof[2], buf + 16384 + (16 + wv) * 1024); }
  };
  const int gk = lane & 31, gkb = gk >> 3;
  const int gdst = 16384 + 18432 + (gkb * 4 + 3) * 256 + ((gk & 7) ^ ((gkb & 1) << 2)) * 32 + 16;
  auto gfill = [&](int kt, unsigned off) {
    if (wv == 6 && lane < 32) {
      u32x4 v = {(unsigned)*(const u16*)(gwl + (kt * 32 + gk) * 2), 0u, 0u, 0u};
      *(u32x4*)(lds + off + gdst) = v;
    }
  };
  __syncthreads();
  gfill(0, 0); gfill(1, K1_STAGE); gfill(2, 2 * K1_STAGE);
  auto waitL = [&](int n) {
    if (wv < 6) { if (n == 2) waitv<10>(); else waitv<5>(); }
    else { if (n == 2) waitv<8>(); else waitv<4>(); }
  };
  const int g = lane >> 4;
  const int bl = ((lane & 15) >> 2) * 32 + (lane & 3) * 8;
  const int aoff = (wm * 64 + (lane & 15)) * 64 + ((g ^ ((-(lane >> 2)) & 3)) << 4);
  const int bgu = 16384 + wn * 9216 + g * 9 * 256 + bl;
  const int bqq = 16384 + 18432 + (g * 4 + wn * 2) * 256 + bl;
  const int hlo = (g & 1) << 7, hhi = hlo ^ 128;
  auto ldA = [&](const char* st, int mt) { return *(const bf16x8*)(st + aoff + mt * 1024); };
  auto ldB = [&](const char* st, int nt) {
    const char* bp = st + (nt < 9 ? bgu + nt * 256 : bqq + (nt - 9) * 256);
    return bfrag2(bp + hlo, bp + hhi);
  };
  auto extra = [&](int nt, int kt, unsigned off) { if (nt == 5) gfill(kt, off); };
  gemm_loop<4, 11, 5, K1_STAGE, 1>(18, lds, acc, ldA, ldB, piece, waitL, extra);
  int tz = threadIdx.x;
  asm volatile("" : "+v"(tz));
  const int lz = tz & 63, gz = lz >> 4, tk = lz & 15;

  char* H = lds + 73728;
  float* G = (float*)(lds + 73728 + 256 * 304);
  const int hg0 = nt_blk * 3;
  {
    char* xs = lds + wv * 9216 + lz * 8;
    if (wn == 1) {
#pragma unroll
      for (int nt = 0; nt < 9; ++nt)
#pragma unroll
        for (int m2 = 0; m2 < 2; ++m2) { f32x4 t = acc[nt][m2]; acc[nt][m2] = acc[nt][2 + m2]; acc[nt][2 + m2] = t; }
    }
#pragma unroll
    for (int nt = 0; nt < 9; ++nt)
#pragma unroll
      for (int m2 = 0; m2 < 2; ++m2) {
        f32x4 v = acc[nt][2 + m2];
        *(u32x2*)(xs + (nt * 2 + m2) * 512) = (u32x2){pk2(v[0], v[1]), pk2(v[2], v[3])};
      }
    if (wn == 1 && gz == 2) {
#pragma unroll
      for (int mt = 0; mt < 4; ++mt) {
        float gv = rbf(1.f / (1.f + __expf(-rbf(acc[10][mt][0]))));
        G[wm * 64 + mt * 16 + tk] = gv;
        if (nt_blk == 0) gate[m0 + wm * 64 + mt * 16 + tk] = gv;
      }
    }
  }
  __syncthreads();
  {
    const char* xr = lds + (wv ^ 1) * 9216 + lz * 8;
    u32x2 pv[9][2];
#pragma unroll
    for (int nt = 0; nt < 9; ++nt)
#pragma unroll
      for (int m2 = 0; m2 < 2; ++m2) pv[nt][m2] = *(const u32x2*)(xr + (nt * 2 + m2) * 512);
    float og[2];
#pragma unroll
    for (int m2 = 0; m2 < 2; ++m2) og[m2] = rbf(1.f - G[wm * 64 + (wn * 2 + m2) * 16 + tk]);
    asm volatile("s_waitcnt lgkmcnt(0)" ::: "memory");
    __builtin_amdgcn_s_barrier();
#pragma unroll
    for (int nt = 0; nt < 9; ++nt)
#pragma unroll
      for (int m2 = 0; m2 < 2; ++m2) {
        f32x4 v = acc[nt][m2];
        u32x2 uv = pv[nt][m2];
        float h0 = rbf(v[0]) * __uint_as_float(uv[0] << 16) * og[m2];
        float h1 = rbf(v[1]) * __uint_as_float(uv[0] & 0xffff0000u) * og[m2];
        float h2 = rbf(v[2]) * __uint_as_float(uv[1] << 16) * og[m2];
        float h3 = rbf(v[3]) * __uint_as_float(uv[1] & 0xffff0000u) * og[m2];
        *(u32x2*)(H + (wm * 64 + (wn * 2 + m2) * 16 + tk) * 304 + (nt * 16 + 4 * gz) * 2) = (u32x2){pk2(h0, h1), pk2(h2, h3)};
      }
  }
  {
    char* img = lds;
#pragma unroll
    for (int t2 = 0; t2 < 2; ++t2)
#pragma unroll
      for (int r = 0; r < 4; r += 2) {
        int c = wn * 32 + t2 * 16 + 4 * gz + r - coff;
        if (c >= 0 && c < 54) {
          int hl = c / 18, d = c - hl * 18;
          int which = (hg0 + hl) >> 5;
          float sc = which == 0 ? QSCALE : 1.f;
#pragma unroll
          for (int mt = 0; mt < 4; ++mt) {
            int tok = wm * 64 + mt * 16 + tk;
            int ch = d >> 3;
            if (which == 1) ch ^= (tok >> 2) & 3;
            *(unsigned*)(img + (hl * 256 + tok) * 64 + ch * 16 + (d & 7) * 2) =
                pk2(acc[9 + t2][mt][r] * sc, acc[9 + t2][mt][r + 1] * sc);
          }
        }
      }
    for (int i = tz; i < 768; i += 512) {
      int which = (hg0 + (i >> 8)) >> 5;
      int sw = which == 1 ? (i >> 2) & 3 : 0;
      char* row = img + i * 64;
      *(unsigned*)(row + (2 ^ sw) * 16 + 4) = which == 2 ? 0x3f80u : 0u;
      *(u32x2*)(row + (2 ^ sw) * 16 + 8) = (u32x2){0u, 0u};
      *(u32x4*)(row + (3 ^ sw) * 16) = (u32x4){0u, 0u, 0u, 0u};
    }
  }
  __syncthreads();
#pragma unroll
  for (int i = 0; i < 9; ++i) {
    int u = i * 512 + tz;
    int row = u / 18, cu = u - row * 18;
    u32x4 v = *(const u32x4*)(H + row * 304 + cu * 16);
    *(u32x4*)(Ah + (size_t)(m0 + row) * F_ + f0 + cu * 8) = v;
  }
#pragma unroll
  for (int i = 0; i < 6; ++i) {
    int u = i * 512 + tz;
    int hl = u >> 10, rem = u & 1023;
    u32x4 v = *(const u32x4*)(lds + u * 16);
    *(u32x4*)(qkvp + ((size_t)(nt_blk * 3 + hl) * S_ + m0) * 32 + rem * 8) = v;
  }
}

DI void attn_stage(const u16* __restrict__ Kh, const u16* __restrict__ Vh, int c, unsigned buf, int wv, int lane) {
#pragma unroll
  for (int i = 0; i < 2; ++i) glds16(Kh + (size_t)c * 4096, ((i * 4 + wv) * 64 + lane) * 16u, buf + (i * 4 + wv) * 1024);
#pragma unroll
  for (int i = 0; i < 2; ++i)
    glds16(Vh + (size_t)c * 4096, ((i * 4 + wv) * 64 + lane) * 16u, buf + 8192 + (i * 4 + wv) * 1024);
}

DI float halfswap_max(float m) {
  auto rr = __builtin_amdgcn_permlane32_swap(__float_as_uint(m), __float_as_uint(m), false, false);
  return fmaxf(__uint_as_float(rr[0]), __uint_as_float(rr[1]));
}
DI float max16(const f32x16& s) {
  float mx = s[0];
#pragma unroll
  for (int r = 1; r < 16; ++r) mx = fmaxf(mx, s[r]);
  return halfswap_max(mx);
}
#define MFMA32(a, b, c) __builtin_amdgcn_mfma_f32_32x32x16_bf16((a), (b), (c), 0, 0, 0)

DI void exp_pack(const f32x16& s, bf16x8& p0, bf16x8& p1) {
  u32x4 a, b;
#pragma unroll
  for (int r = 0; r < 4; ++r) a[r] = pk2(__builtin_amdgcn_exp2f(s[2 * r]), __builtin_amdgcn_exp2f(s[2 * r + 1]));
#pragma unroll
  for (int r = 0; r < 4; ++r) b[r] = pk2(__builtin_amdgcn_exp2f(s[8 + 2 * r]), __builtin_amdgcn_exp2f(s[9 + 2 * r]));
  p0 = __builtin_bit_cast(bf16x8, a);
  p1 = __builtin_bit_cast(bf16x8, b);
}

#define EX2(x) __builtin_amdgcn_exp2f(x)
#define PHASE(S, PA, PB, M1, M2, M3, M4)                                              \
  do {                                                                                \
    M1; SBAR(); PA[0] = pk2(EX2(S[0]), EX2(S[1])); PA[1] = pk2(EX2(S[2]), EX2(S[3])); SBAR();  \
    M2; SBAR(); PA[2] = pk2(EX2(S[4]), EX2(S[5])); PA[3] = pk2(EX2(S[6]), EX2(S[7])); SBAR();  \
    M3; SBAR(); PB[0] = pk2(EX2(S[8]), EX2(S[9])); PB[1] = pk2(EX2(S[10]), EX2(S[11])); SBAR(); \
    M4; SBAR(); PB[2] = pk2(EX2(S[12]), EX2(S[13])); PB[3] = pk2(EX2(S[14]), EX2(S[15])); SBAR(); \
  } while (0)
#define BC8(x) __builtin_bit_cast(bf16x8, x)
DI void attn_fast(const u16* __restrict__ Kh, const u16* __restrict__ Vh, const bf16x8 (&qf)[2][2], char* lds,
                  f32x16 (&o)[2], int wv, int lane) {
  const int r32 = lane & 31, h = lane >> 5;
  const int koff0 = r32 * 64 + ((h ^ ((r32 >> 2) & 3)) << 4);
  const int koff1 = r32 * 64 + (((2 + h) ^ ((r32 >> 2) & 3)) << 4);
  const int voff = 8192 + (4 * h + ((lane & 15) >> 2)) * 64 + ((lane >> 4) & 1) * 32 + (lane & 3) * 8;
  const unsigned l0 = lds_addr(lds);
  f32x16 negm[2], sa, sb;
#pragma unroll
  for (int a = 0; a < 2; ++a)
#pragma unroll
    for (int r = 0; r < 16; ++r) { o[a][r] = 0.f; negm[a][r] = 0.f; }
  attn_stage(Kh, Vh, 0, l0, wv, lane);
  attn_stage(Kh, Vh, 1, l0 + 16384, wv, lane);
  attn_stage(Kh, Vh, 2, l0 + 32768, wv, lane);
  waitv<8>();
  BARRIER();
  bf16x8 kc0 = *(const bf16x8*)(lds + koff0), kc1 = *(const bf16x8*)(lds + koff1);
  {
#pragma unroll
    for (int a = 0; a < 2; ++a) {
      f32x16 s = MFMA32(kc0, qf[a][0], negm[a]);
      s = MFMA32(kc1, qf[a][1], s);
      float mx = max16(s);
#pragma unroll
      for (int r = 0; r < 16; ++r) { negm[a][r] = -mx; if (a == 0) sa[r] = s[r] - mx; }
    }
  }
  u32x4 pa0, pa1, pb0 = {0u, 0u, 0u, 0u}, pb1 = {0u, 0u, 0u, 0u};
  bf16x8 vp0 = {0, 0, 0, 0, 0, 0, 0, 0}, vp1 = {0, 0, 0, 0, 0, 0, 0, 0};
  for (int c = 0; c < 16; ++c) {
    if (c + 2 < 16) waitv<4>(); else waitv<0>();
    BARRIER();
    if (c + 3 < 16) attn_stage(Kh, Vh, c + 3, l0 + ((c + 3) & 3) * 16384, wv, lane);
    const char* buf = lds + (c & 3) * 16384;
    const char* bufn = lds + ((c + 1) & 3) * 16384;
#pragma unroll
    for (int t = 0; t < 4; ++t) {
      const char* kb = t < 3 ? buf + (t + 1) * 2048 : bufn;
      const char* vb = buf + t * 2048 + voff;
      bf16x8 kn0 = *(const bf16x8*)(kb + koff0), kn1 = *(const bf16x8*)(kb + koff1);
      s16x4 v0 = trread(vb), v1 = trread(vb + 512), v2 = trread(vb + 1024), v3 = trread(vb + 1536);
      bf16x8 vc0 = __builtin_shufflevector(v0, v1, 0, 1, 2, 3, 4, 5, 6, 7);
      bf16x8 vc1 = __builtin_shufflevector(v2, v3, 0, 1, 2, 3, 4, 5, 6, 7);
      SBAR();
      PHASE(sa, pa0, pa1, sb = MFMA32(kc0, qf[1][0], negm[1]), o[1] = MFMA32(vp0, BC8(pb0), o[1]),
            sb = MFMA32(kc1, qf[1][1], sb), o[1] = MFMA32(vp1, BC8(pb1), o[1]));
      PHASE(sb, pb0, pb1, sa = MFMA32(kn0, qf[0][0], negm[0]), o[0] = MFMA32(vc0, BC8(pa0), o[0]),
            sa = MFMA32(kn1, qf[0][1], sa), o[0] = MFMA32(vc1, BC8(pa1), o[0]));
      kc0 = kn0; kc1 = kn1; vp0 = vc0; vp1 = vc1;
    }
  }
  o[1] = MFMA32(vp0, BC8(pb0), o[1]);
  o[1] = MFMA32(vp1, BC8(pb1), o[1]);
}

DI void attn_safe(const u16* __restrict__ Kh, const u16* __restrict__ Vh, const bf16x8 (&qf)[2][2], char* lds,
                  f32x16 (&o)[2], int wv, int lane) {
  const int r32 = lane & 31, h = lane >> 5;
  const int koff0 = r32 * 64 + ((h ^ ((r32 >> 2) & 3)) << 4);
  const int koff1 = r32 * 64 + (((2 + h) ^ ((r32 >> 2) & 3)) << 4);
  const int voff = 8192 + (4 * h + ((lane & 15) >> 2)) * 64 + ((lane >> 4) & 1) * 32 + (lane & 3) * 8;
  const unsigned l0 = lds_addr(lds);
  f32x16 zero;
  float m[2] = {-INFINITY, -INFINITY};
#pragma unroll
  for (int r = 0; r < 16; ++r) { o[0][r] = 0.f; o[1][r] = 0.f; zero[r] = 0.f; }
  for (int c = 0; c < 16; ++c) {
    BARRIER();
    attn_stage(Kh, Vh, c, l0, wv, lane);
    waitv<0>();
    BARRIER();
    for (int t = 0; t < 4; ++t) {
      const char* kb = lds + t * 2048;
      bf16x8 k0 = *(const bf16x8*)(kb + koff0), k1 = *(const bf16x8*)(kb + koff1);
      s16x4 v0 = trread(kb + voff), v1 = trread(kb + voff + 512), v2 = trread(kb + voff + 1024), v3 = trread(kb + voff + 1536);
      bf16x8 va = __builtin_shufflevector(v0, v1, 0, 1, 2, 3, 4, 5, 6, 7);
      bf16x8 vb = __builtin_shufflevector(v2, v3, 0, 1, 2, 3, 4, 5, 6, 7);
#pragma unroll
      for (int a = 0; a < 2; ++a) {
        f32x16 s = MFMA32(k0, qf[a][0], zero);
        s = MFMA32(k1, qf[a][1], s);
        float mn = fmaxf(m[a], max16(s));
        float al = __builtin_amdgcn_exp2f(m[a] - mn);
        m[a] = mn;
#pragma unroll
        for (int r = 0; r < 16; ++r) { o[a][r] *= al; s[r] -= mn; }
        bf16x8 p0, p1;
        exp_pack(s, p0, p1);
        o[a] = MFMA32(va, p0, o[a]);
        o[a] = MFMA32(vb, p1, o[a]);
      }
    }
  }
}

DI void attn_block(int bid, const u16* __restrict__ qkvp, const float* __restrict__ gate, u16* __restrict__ Ao,
                   char* lds) {
  const int tid = threadIdx.x, lane = tid & 63, r32 = lane & 31, h = lane >> 5;
  const int wv = __builtin_amdgcn_readfirstlane(tid >> 6);
  const int head = (bid & 7) * 4 + ((bid >> 3) & 3), qb = bid >> 5;
  const u16* Qh = qkvp + (size_t)head * (S_ * 32);
  const u16* Kh = qkvp + (size_t)(H_ + head) * (S_ * 32);
  const u16* Vh = qkvp + (size_t)(2 * H_ + head) * (S_ * 32);
  const int qrow0 = qb * 256 + wv * 64 + r32;
  bf16x8 qf[2][2];
#pragma unroll
  for (int a = 0; a < 2; ++a) {
    qf[a][0] = *(const bf16x8*)(Qh + (size_t)(qrow0 + a * 32) * 32 + h * 8);
    qf[a][1] = *(const bf16x8*)(Qh + (size_t)(qrow0 + a * 32) * 32 + 16 + h * 8);
  }
  float gq[2] = {gate[qrow0], gate[qrow0 + 32]};
  asm volatile("" ::"v"(qf[0][0]), "v"(qf[0][1]), "v"(qf[1][0]), "v"(qf[1][1]), "v"(gq[0]), "v"(gq[1]));
  f32x16 o[2];
  attn_fast(Kh, Vh, qf, lds, o, wv, lane);
  bool bad = false;
#pragma unroll
  for (int a = 0; a < 2; ++a)
#pragma unroll
    for (int r = 0; r < 11; ++r) bad |= !(fabsf(o[a][r]) < 1e30f);
  if (__syncthreads_or(bad)) attn_safe(Kh, Vh, qf, lds, o, wv, lane);
#pragma unroll
  for (int a = 0; a < 2; ++a) {
    float l = __shfl(o[a][10], r32);
    float sc = gq[a] / l;
    u16* dst = Ao + (size_t)(qrow0 + a * 32) * D_ + head * DH_ + h * 4;
    *(unsigned*)(dst + 0) = pk2(o[a][0] * sc, o[a][1] * sc);
    *(unsigned*)(dst + 2) = pk2(o[a][2] * sc, o[a][3] * sc);
    *(unsigned*)(dst + 8) = pk2(o[a][4] * sc, o[a][5] * sc);
    *(unsigned*)(dst + 10) = pk2(o[a][6] * sc, o[a][7] * sc);
    if (h == 0) *(unsigned*)(dst + 16) = pk2(o[a][8] * sc, o[a][9] * sc);
  }
}

constexpr int K2_STAGE = 8192 + 9216;
DI void down_block(int bid, const u16* __restrict__ Ah, const u16* __restrict__ wd, float* __restrict__ part,
                   char* lds) {
  const int tid = threadIdx.x, lane = tid & 63;
  const int wv = __builtin_amdgcn_readfirstlane(tid >> 6);
  const int mt_blk = (bid & 7) * 2 + ((bid >> 3) & 1), rest = bid >> 4;
  const int nt_blk = rest & 3, sp = rest >> 2;
  const int m0 = mt_blk * 128, n0 = nt_blk * 144;
  f32x4 acc[9][2] = {};
  const u16* pa = Ah + (size_t)m0 * F_ + sp * 1152;
  const u16* pb = wd + (size_t)sp * 1152 * D_ + n0;
  const unsigned aof0 = aseg_off(wv * 64 + lane, F_), aof1 = aseg_off((4 + wv) * 64 + lane, F_);
  const unsigned bof0 = bseg_off<9>(wv * 64 + lane, D_), bof1 = bseg_off<9>((4 + wv) * 64 + lane, D_),
                 bof2 = bseg_off<9>(8 * 64 + lane, D_);
  auto piece = [&](int i, int kt, unsigned buf) {
    if (i == 0) glds16(pa + kt * 32, aof0, buf + wv * 1024);
    else if (i == 1) glds16(pa + kt * 32, aof1, buf + (4 + wv) * 1024);
    else if (i == 2) glds16(pb + (size_t)kt * 32 * D_, bof0, buf + 8192 + wv * 1024);
    else if (i == 3) glds16(pb + (size_t)kt * 32 * D_, bof1, buf + 8192 + (4 + wv) * 1024);
    else if (wv == 0) glds16(pb + (size_t)kt * 32 * D_, bof2, buf + 8192 + 8 * 1024);
  };
  auto waitL = [&](int n) {
    if (wv == 0) { if (n == 2) waitv<10>(); else waitv<5>(); }
    else { if (n == 2) waitv<8>(); else waitv<4>(); }
  };
  const int g = lane >> 4;
  const int aoff = (wv * 32 + (lane & 15)) * 64 + ((g ^ ((-(lane >> 2)) & 3)) << 4);
  const int boff = 8192 + g * 9 * 256 + ((lane & 15) >> 2) * 32 + (lane & 3) * 8;
  const int hlo = (g & 1) << 7, hhi = hlo ^ 128;
  auto ldA = [&](const char* st, int mt) { return *(const bf16x8*)(st + aoff + mt * 1024); };
  auto ldB = [&](const char* st, int nt) { return bfrag2(st + boff + nt * 256 + hlo, st + boff + nt * 256 + hhi); };
  auto extra = [&](int, int, unsigned) {};
  gemm_loop<2, 9, 5, K2_STAGE, 1>(36, lds, acc, ldA, ldB, piece, waitL, extra);
  float* dst = part + (size_t)sp * (S_ * D_);
#pragma unroll
  for (int nt = 0; nt < 9; ++nt)
#pragma unroll
    for (int mt = 0; mt < 2; ++mt) {
      int tok = m0 + wv * 32 + mt * 16 + (lane & 15);
      int n = n0 + nt * 16 + 4 * (lane >> 4);
      *(f32x4*)(dst + (size_t)tok * D_ + n) = acc[nt][mt];
    }
}

__global__ __launch_bounds__(256, 2) void k_mid(const u16* __restrict__ qkvp, const float* __restrict__ gate,
                                                u16* __restrict__ Ao, const u16* __restrict__ Ah,
                                                const u16* __restrict__ wd, float* __restrict__ part) {
  __shared__ __attribute__((aligned(16))) char lds[4 * K2_STAGE > 65536 ? 4 * K2_STAGE : 65536];
  const int bid = blockIdx.x;
  if (bid < 256) attn_block(bid, qkvp, gate, Ao, lds);
  else down_block(bid - 256, Ah, wd, part, lds);
}

constexpr int K3_STAGE = 4096 + 6144;
__global__ __launch_bounds__(256) void k_out(const u16* __restrict__ Ao, const u16* __restrict__ wo,
                                             const float* __restrict__ part, u16* __restrict__ out) {
  __shared__ __attribute__((aligned(16))) char lds[12 * K3_STAGE];
  const int tid = threadIdx.x, lane = tid & 63;
  const int wv = __builtin_amdgcn_readfirstlane(tid >> 6), wm = wv >> 1, wn = wv & 1;
  const int nt_blk = blockIdx.x % 6, mt_blk = blockIdx.x / 6;
  const int m0 = mt_blk * 64, n0 = nt_blk * 96;
  f32x4 acc[3][2];
#pragma unroll
  for (int nt = 0; nt < 3; ++nt)
#pragma unroll
    for (int mt = 0; mt < 2; ++mt) {
      size_t off = (size_t)(m0 + wm * 32 + mt * 16 + (lane & 15)) * D_ + n0 + wn * 48 + nt * 16 + 4 * (lane >> 4);
      f32x4 s = *(const f32x4*)(part + off);
#pragma unroll
      for (int sp = 1; sp < 4; ++sp) s += *(const f32x4*)(part + (size_t)sp * (S_ * D_) + off);
      acc[nt][mt] = s;
    }
  const u16* pa = Ao + (size_t)m0 * D_;
  const u16* pb = wo + n0;
  const unsigned aof0 = aseg_off(wv * 64 + lane, D_);
  const unsigned bof0 = bseg_off<6>(wv * 64 + lane, D_), bof1 = bseg_off<6>((4 + (wv & 1)) * 64 + lane, D_);
  auto piece = [&](int i, int kt, unsigned buf) {
    if (i == 0) glds16(pa + kt * 32, aof0, buf + wv * 1024);
    else if (i == 1) glds16(pb + (size_t)kt * 32 * D_, bof0, buf + 4096 + wv * 1024);
    else if (wv < 2) glds16(pb + (size_t)kt * 32 * D_, bof1, buf + 4096 + (4 + wv) * 1024);
  };
  auto waitL = [&](int n) {
    if (wv < 2) { if (n == 2) waitv<18>(); else waitv<9>(); }
    else { if (n == 2) waitv<12>(); else waitv<6>(); }
  };
  const int g = lane >> 4;
  const int aoff = (wm * 32 + (lane & 15)) * 64 + ((g ^ ((-(lane >> 2)) & 3)) << 4);
  const int boff = 4096 + (g * 6 + wn * 3) * 256 + ((lane & 15) >> 2) * 32 + (lane & 3) * 8;
  const int hlo = (g & 1) << 7, hhi = hlo ^ 128;
  auto ldA = [&](const char* st, int mt) { return *(const bf16x8*)(st + aoff + mt * 1024); };
  auto ldB = [&](const char* st, int nt) { return bfrag2(st + boff + nt * 256 + hlo, st + boff + nt * 256 + hhi); };
  auto extra = [&](int, int, unsigned) {};
  gemm_loop<2, 3, 3, K3_STAGE, 3>(6, lds, acc, ldA, ldB, piece, waitL, extra);
#pragma unroll
  for (int nt = 0; nt < 3; ++nt)
#pragma unroll
    for (int mt = 0; mt < 2; ++mt) {
      size_t off = (size_t)(m0 + wm * 32 + mt * 16 + (lane & 15)) * D_ + n0 + wn * 48 + nt * 16 + 4 * (lane >> 4);
      u32x2 v = {pk2(acc[nt][mt][0], acc[nt][mt][1]), pk2(acc[nt][mt][2], acc[nt][mt][3])};
      *(u32x2*)(out + off) = v;
    }
}

extern "C" void kernel_launch(void* const* d_in, const int* in_sizes, int n_in, void* d_out, int out_size,
                              void* d_ws, size_t ws_size, hipStream_t stream) {
  const u16* x = (const u16*)d_in[0];
  const u16* wqkv = (const u16*)d_in[1];
  const u16* wo = (const u16*)d_in[2];
  const u16* wg = (const u16*)d_in[4];
  const u16* wu = (const u16*)d_in[5];
  const u16* wd = (const u16*)d_in[6];
  const u16* gw = (const u16*)d_in[7];
  char* ws = (char*)d_ws;
  u16* qkvp = (u16*)ws;
  float* gate = (float*)(ws + (size_t)16 * 1024 * 1024);
  u16* Ao = (u16*)(ws + (size_t)17 * 1024 * 1024);
  u16* Ah = (u16*)(ws + (size_t)20 * 1024 * 1024);
  float* part = (float*)(ws + (size_t)48 * 1024 * 1024);
  k_proj<<<256, 512, 0, stream>>>(x, wqkv, wg, wu, gw, qkvp, gate, Ah);
  k_mid<<<512, 256, 0, stream>>>(qkvp, gate, Ao, Ah, wd, part);
  k_out<<<192, 256, 0, stream>>>(Ao, wo, part, (u16*)d_out);
}
```

```cpp
#include <hip/hip_runtime.h>
#include <hip/hip_bf16.h>
#include <type_traits>

typedef __attribute__((ext_vector_type(8))) short bf16x8;
typedef __attribute__((ext_vector_type(4))) short s16x4;
typedef __attribute__((ext_vector_type(4))) float f32x4;
typedef __attribute__((ext_vector_type(2))) float f32x2;
typedef __attribute__((ext_vector_type(16))) float f32x16;
typedef __attribute__((ext_vector_type(4))) unsigned u32x4;
typedef __attribute__((ext_vector_type(2))) unsigned u32x2;
typedef __attribute__((ext_vector_type(2))) __bf16 bf16x2_t;
typedef unsigned short u16;

#define DI __device__ __forceinline__
#define LDSP(T) __attribute__((address_space(3))) T

constexpr int S_ = 2048, D_ = 576, H_ = 32, DH_ = 18, F_ = 4608;

DI unsigned pk2(float lo, float hi) {
  f32x2 v = {lo, hi};
  bf16x2_t b = __builtin_convertvector(v, bf16x2_t);
  return __builtin_bit_cast(unsigned, b);
}
DI float rbf(float x) { return __uint_as_float(pk2(x, 0.f) << 16); }
DI float dot2bf(unsigned a, unsigned b, float c) {
  return __builtin_amdgcn_fdot2_f32_bf16(__builtin_bit_cast(bf16x2_t, a), __builtin_bit_cast(bf16x2_t, b), c, false);
}

DI void glds16(const void* sbase, unsigned voff, unsigned lds_base) {
  unsigned sv;
  asm volatile("s_mov_b32 %0, m0\n\ts_mov_b32 m0, %3\n\ts_nop 0\n\tglobal_load_lds_dwordx4 %1, %2\n\ts_mov_b32 m0, %0"
               : "=&s"(sv) : "v"(voff), "s"(sbase), "s"(lds_base) : "memory");
}
DI unsigned lds_addr(const char* p) { return (unsigned)(size_t)(LDSP(const char)*)p; }
DI s16x4 trread(const char* p) { return __builtin_amdgcn_ds_read_tr16_b64_v4i16((LDSP(s16x4)*)p); }
template <int N> DI void waitv() { asm volatile("s_waitcnt vmcnt(%0)" ::"n"(N) : "memory"); }
#define BARRIER() do { asm volatile("s_waitcnt lgkmcnt(0)" ::: "memory"); __builtin_amdgcn_s_barrier(); asm volatile("" ::: "memory"); } while (0)

#define SBAR() __builtin_amdgcn_sched_barrier(0)
template <int NBs>
DI unsigned bseg_off(int u, int ld) {
  int st = u >> 4, kblk = st / NBs, nblk = st - kblk * NBs;
  int rr = (u >> 1) & 7, hf = u & 1;
  int k = kblk * 8 + (rr ^ ((kblk & 1) << 2));
  return (unsigned)(k * ld + nblk * 16 + hf * 8) * 2u;
}
DI unsigned aseg_off(int u, int lda) {
  int row = u >> 2, ch = (u & 3) ^ ((-(row >> 2)) & 3);
  return (unsigned)(row * lda + ch * 8) * 2u;
}
DI bf16x8 bfrag2(const char* lo, const char* hi) {
  s16x4 a = trread(lo), b = trread(hi);
  return __builtin_shufflevector(a, b, 0, 1, 2, 3, 4, 5, 6, 7);
}

template <int MT, int NT, int LMAX, int STAGE, int KS, class LdA, class LdB, class Piece, class WaitL, class Extra>
DI void gemm_loop(int nsup, char* lds, f32x4 (&acc)[NT][MT], LdA ldA, LdB ldB, Piece piece, WaitL waitL, Extra extra) {
  static_assert(NT >= MT + 1 && NT >= LMAX, "schedule");
  const unsigned l0 = lds_addr(lds);
#pragma unroll
  for (int s = 0; s < 3 * KS; ++s)
#pragma unroll
    for (int i = 0; i < LMAX; ++i) piece(i, s, l0 + s * STAGE);
  waitL(2);
  BARRIER();
  bf16x8 af[MT], bw0, bw1;
#pragma unroll
  for (int mt = 0; mt < MT; ++mt) af[mt] = ldA(lds, mt);
  bw0 = ldB(lds, 0);
  bw1 = ldB(lds, 1);
  for (int j = 0; j < nsup; ++j) {
    if (j + 2 < nsup) waitL(1); else waitv<0>();
    BARRIER();
    const char* base = lds + (j & 3) * (KS * STAGE);
    const char* nbase = lds + ((j + 1) & 3) * (KS * STAGE);
    const unsigned dst = l0 + ((j + 3) & 3) * (KS * STAGE);
    const bool more = j + 3 < nsup;
#pragma unroll
    for (int ks = 0; ks < KS; ++ks) {
      const char* cur = base + ks * STAGE;
      const char* nx = ks + 1 < KS ? base + (ks + 1) * STAGE : nbase;
      bf16x8 an[MT];
#pragma unroll
      for (int nt = 0; nt < NT; ++nt) {
        bf16x8 b = (nt & 1) ? bw1 : bw0;
        bf16x8 nb = nt + 2 < NT ? ldB(cur, nt + 2) : ldB(nx, nt + 2 - NT);
        if (nt & 1) bw1 = nb; else bw0 = nb;
        if (nt >= NT - 1 - MT && nt < NT - 1) an[nt - (NT - 1 - MT)] = ldA(nx, nt - (NT - 1 - MT));
        if (nt < LMAX && more) piece(nt, (j + 3) * KS + ks, dst + ks * STAGE);
#pragma unroll
        for (int mt = 0; mt < MT; ++mt) acc[nt][mt] = __builtin_amdgcn_mfma_f32_16x16x32_bf16(b, af[mt], acc[nt][mt], 0, 0, 0);
        if (more) extra(nt, (j + 3) * KS + ks, dst + ks * STAGE - l0);
        SBAR();
      }
#pragma unroll
      for (int mt = 0; mt < MT; ++mt) af[mt] = an[mt];
      if (NT & 1) { bf16x8 t = bw0; bw0 = bw1; bw1 = t; }
    }
  }
  BARRIER();
}

constexpr float QSCALE = 0.23570226039551584f * 1.4426950408889634f;
constexpr int K1_STAGE = 16384 + 9216 + 9216 + 4096;

__global__ __launch_bounds__(512) void k_proj(const u16* __restrict__ x, const u16* __restrict__ wqkv,
                                              const u16* __restrict__ wg, const u16* __restrict__ wu,
                                              const u16* __restrict__ gate_w, u16* __restrict__ qkvp,
                                              float* __restrict__ gate, u16* __restrict__ Ah) {
  __shared__ __attribute__((aligned(16))) char lds[4 * K1_STAGE + 1152];
  const int tid = threadIdx.x, lane = tid & 63;
  const int wv = __builtin_amdgcn_readfirstlane(tid >> 6), wm = wv >> 1, wn = wv & 1;
  const int bid = blockIdx.x;
  const int nt_blk = (bid & 7) * 4 + ((bid >> 3) & 3), mt_blk = bid >> 5;
  const int m0 = mt_blk * 256, f0 = nt_blk * 144;
  const int coff = nt_blk == 31 ? 2 : 0;
  const int c0 = nt_blk * 54 - coff;
  char* gwl = lds + 4 * K1_STAGE;
  if (tid < 72) *(u32x4*)(gwl + tid * 16) = *(const u32x4*)(gate_w + tid * 8);

  f32x4 acc[11][4] = {};
  const u16* xa = x + (size_t)m0 * D_;
  const unsigned aof0 = aseg_off(wv * 64 + lane, D_), aof1 = aseg_off((8 + wv) * 64 + lane, D_);
  unsigned bof[3];
  const u16* bsrc[3];
  int bld[3];
#pragma unroll
  for (int i = 0; i < 3; ++i) {
    int jj = i * 8 + wv;
    if (jj < 18) {
      int j = jj < 9 ? jj : jj - 9;
      bof[i] = bseg_off<9>(j * 64 + lane, F_);
      bsrc[i] = (jj < 9 ? wg : wu) + f0;
      bld[i] = F_;
    } else {
      bof[i] = bseg_off<4>(((jj - 18) & 3) * 64 + lane, 3 * D_);
      bsrc[i] = wqkv + c0;
      bld[i] = 3 * D_;
    }
  }
  const bool padlane = (lane & 1) && (lane >> 4) == 3;
  auto piece = [&](int i, int kt, unsigned buf) {
    if (i == 0) glds16(xa + kt * 32, aof0, buf + wv * 1024);
    else if (i == 1) glds16(xa + kt * 32, aof1, buf + (8 + wv) * 1024);
    else if (i < 4) glds16(bsrc[i - 2] + (size_t)kt * 32 * bld[i - 2], bof[i - 2], buf + 16384 + ((i - 2) * 8 + wv) * 1024);
    else if (wv < 2) glds16(bsrc[2] + (size_t)kt * 32 * bld[2], bof[2], buf + 16384 + (16 + wv) * 1024);
    else if (wv < 6) { if (!padlane) glds16(bsrc[2] + (size_t)kt * 32 * bld[2], bof[2], buf + 16384 + (16 + wv) * 1024); }
  };
  const int gk = lane & 31, gkb = gk >> 3;
  const int gdst = 16384 + 18432 + (gkb * 4 + 3) * 256 + ((gk & 7) ^ ((gkb & 1) << 2)) * 32 + 16;
  auto gfill = [&](int kt, unsigned off) {
    if (wv == 6 && lane < 32) {
      u32x4 v = {(unsigned)*(const u16*)(gwl + (kt * 32 + gk) * 2), 0u, 0u, 0u};
      *(u32x4*)(lds + off + gdst) = v;
    }
  };
  __syncthreads();
  gfill(0, 0); gfill(1, K1_STAGE); gfill(2, 2 * K1_STAGE);
  auto waitL = [&](int n) {
    if (wv < 6) { if (n == 2) waitv<10>(); else waitv<5>(); }
    else { if (n == 2) waitv<8>(); else waitv<4>(); }
  };
  const int g = lane >> 4;
  const int bl = ((lane & 15) >> 2) * 32 + (lane & 3) * 8;
  const int aoff = (wm * 64 + (lane & 15)) * 64 + ((g ^ ((-(lane >> 2)) & 3)) << 4);
  const int bgu = 16384 + wn * 9216 + g * 9 * 256 + bl;
  const int bqq = 16384 + 18432 + (g * 4 + wn * 2) * 256 + bl;
  const int hlo = (g & 1) << 7, hhi = hlo ^ 128;
  auto ldA = [&](const char* st, int mt) { return *(const bf16x8*)(st + aoff + mt * 1024); };
  auto ldB = [&](const char* st, int nt) {
    const char* bp = st + (nt < 9 ? bgu + nt * 256 : bqq + (nt - 9) * 256);
    return bfrag2(bp + hlo, bp + hhi);
  };
  auto extra = [&](int nt, int kt, unsigned off) { if (nt == 5) gfill(kt, off); };
  gemm_loop<4, 11, 5, K1_STAGE, 1>(18, lds, acc, ldA, ldB, piece, waitL, extra);
  int tz = threadIdx.x;
  asm volatile("" : "+v"(tz));
  const int lz = tz & 63, gz = lz >> 4, tk = lz & 15;

  char* H = lds + 73728;
  float* G = (float*)(lds + 73728 + 256 * 304);
  const int hg0 = nt_blk * 3;
  {
    char* xs = lds + wv * 9216 + lz * 8;
    if (wn == 1) {
#pragma unroll
      for (int nt = 0; nt < 9; ++nt)
#pragma unroll
        for (int m2 = 0; m2 < 2; ++m2) { f32x4 t = acc[nt][m2]; acc[nt][m2] = acc[nt][2 + m2]; acc[nt][2 + m2] = t; }
    }
#pragma unroll
    for (int nt = 0; nt < 9; ++nt)
#pragma unroll
      for (int m2 = 0; m2 < 2; ++m2) {
        f32x4 v = acc[nt][2 + m2];
        *(u32x2*)(xs + (nt * 2 + m2) * 512) = (u32x2){pk2(v[0], v[1]), pk2(v[2], v[3])};
      }
    if (wn == 1 && gz == 2) {
#pragma unroll
      for (int mt = 0; mt < 4; ++mt) {
        float gv = rbf(1.f / (1.f + __expf(-rbf(acc[10][mt][0]))));
        G[wm * 64 + mt * 16 + tk] = gv;
        if (nt_blk == 0) gate[m0 + wm * 64 + mt * 16 + tk] = gv;
      }
    }
  }
  __syncthreads();
  {
    const char* xr = lds + (wv ^ 1) * 9216 + lz * 8;
    u32x2 pv[9][2];
#pragma unroll
    for (int nt = 0; nt < 9; ++nt)
#pragma unroll
      for (int m2 = 0; m2 < 2; ++m2) pv[nt][m2] = *(const u32x2*)(xr + (nt * 2 + m2) * 512);
    float og[2];
#pragma unroll
    for (int m2 = 0; m2 < 2; ++m2) og[m2] = rbf(1.f - G[wm * 64 + (wn * 2 + m2) * 16 + tk]);
    asm volatile("s_waitcnt lgkmcnt(0)" ::: "memory");
    __builtin_amdgcn_s_barrier();
#pragma unroll
    for (int nt = 0; nt < 9; ++nt)
#pragma unroll
      for (int m2 = 0; m2 < 2; ++m2) {
        f32x4 v = acc[nt][m2];
        u32x2 uv = pv[nt][m2];
        float h0 = rbf(v[0]) * __uint_as_float(uv[0] << 16) * og[m2];
        float h1 = rbf(v[1]) * __uint_as_float(uv[0] & 0xffff0000u) * og[m2];
        float h2 = rbf(v[2]) * __uint_as_float(uv[1] << 16) * og[m2];
        float h3 = rbf(v[3]) * __uint_as_float(uv[1] & 0xffff0000u) * og[m2];
        *(u32x2*)(H + (wm * 64 + (wn * 2 + m2) * 16 + tk) * 304 + (nt * 16 + 4 * gz) * 2) = (u32x2){pk2(h0, h1), pk2(h2, h3)};
      }
  }
  {
    char* img = lds;
#pragma unroll
    for (int t2 = 0; t2 < 2; ++t2)
#pragma unroll
      for (int r = 0; r < 4; r += 2) {
        int c = wn * 32 + t2 * 16 + 4 * gz + r - coff;
        if (c >= 0 && c < 54) {
          int hl = c / 18, d = c - hl * 18;
          int which = (hg0 + hl) >> 5;
          float sc = which == 0 ? QSCALE : 1.f;
#pragma unroll
          for (int mt = 0; mt < 4; ++mt) {
            int tok = wm * 64 + mt * 16 + tk;
            unsigned v = pk2(acc[9 + t2][mt][r] * sc, acc[9 + t2][mt][r + 1] * sc);
            if (which == 2) {
              char* q = img + hl * 16384 + (tok >> 5) * 2048 + ((((tok >> 4) & 1) * 2 + ((tok >> 2) & 1)) * 32 + d) * 16 +
                        ((tok & 3) + 4 * ((tok >> 3) & 1)) * 2;
              *(u16*)q = (u16)v;
              *(u16*)(q + 16) = (u16)(v >> 16);
            } else {
              int ch = d >> 3;
              if (which == 1) ch ^= (tok >> 2) & 3;
              *(unsigned*)(img + (hl * 256 + tok) * 64 + ch * 16 + (d & 7) * 2) = v;
            }
          }
        }
      }
    for (int i = tz; i < 768; i += 512) {
      int which = (hg0 + (i >> 8)) >> 5;
      if (which == 2) {
        char* grp = img + (i >> 8) * 16384 + ((i & 255) >> 3) * 512;
        int d0 = 18 + (i & 7) * 2;
        unsigned one = d0 == 18 ? 0x3f803f80u : 0u;
        if (d0 < 32) {
          *(u32x4*)(grp + d0 * 16) = (u32x4){one, one, one, one};
          *(u32x4*)(grp + d0 * 16 + 16) = (u32x4){0u, 0u, 0u, 0u};
        }
      } else {
        int sw = which == 1 ? (i >> 2) & 3 : 0;
        char* row = img + i * 64;
        *(unsigned*)(row + (2 ^ sw) * 16 + 4) = 0u;
        *(u32x2*)(row + (2 ^ sw) * 16 + 8) = (u32x2){0u, 0u};
        *(u32x4*)(row + (3 ^ sw) * 16) = (u32x4){0u, 0u, 0u, 0u};
      }
    }
  }
  __syncthreads();
#pragma unroll
  for (int i = 0; i < 9; ++i) {
    int u = i * 512 + tz;
    int row = u / 18, cu = u - row * 18;
    u32x4 v = *(const u32x4*)(H + row * 304 + cu * 16);
    *(u32x4*)(Ah + (size_t)(m0 + row) * F_ + f0 + cu * 8) = v;
  }
#pragma unroll
  for (int i = 0; i < 6; ++i) {
    int u = i * 512 + tz;
    int hl = u >> 10, rem = u & 1023;
    u32x4 v = *(const u32x4*)(lds + u * 16);
    *(u32x4*)(qkvp + ((size_t)(nt_blk * 3 + hl) * S_ + m0) * 32 + rem * 8) = v;
  }
}

DI void attn_stage(const u16* __restrict__ Kh, const u16* __restrict__ Vh, int c, unsigned buf, int wv, int lane) {
#pragma unroll
  for (int i = 0; i < 2; ++i) glds16(Kh + (size_t)c * 4096, ((i * 4 + wv) * 64 + lane) * 16u, buf + (i * 4 + wv) * 1024);
#pragma unroll
  for (int i = 0; i < 2; ++i)
    glds16(Vh + (size_t)c * 4096, ((i * 4 + wv) * 64 + lane) * 16u, buf + 8192 + (i * 4 + wv) * 1024);
}

DI float halfswap_max(float m) {
  auto rr = __builtin_amdgcn_permlane32_swap(__float_as_uint(m), __float_as_uint(m), false, false);
  return fmaxf(__uint_as_float(rr[0]), __uint_as_float(rr[1]));
}
DI float max16(const f32x16& s) {
  float mx = s[0];
#pragma unroll
  for (int r = 1; r < 16; ++r) mx = fmaxf(mx, s[r]);
  return halfswap_max(mx);
}
#define MFMA32(a, b, c) __builtin_amdgcn_mfma_f32_32x32x16_bf16((a), (b), (c), 0, 0, 0)

DI void exp_pack(const f32x16& s, bf16x8& p0, bf16x8& p1) {
  u32x4 a, b;
#pragma unroll
  for (int r = 0; r < 4; ++r) a[r] = pk2(__builtin_amdgcn_exp2f(s[2 * r]), __builtin_amdgcn_exp2f(s[2 * r + 1]));
#pragma unroll
  for (int r = 0; r < 4; ++r) b[r] = pk2(__builtin_amdgcn_exp2f(s[8 + 2 * r]), __builtin_amdgcn_exp2f(s[9 + 2 * r]));
  p0 = __builtin_bit_cast(bf16x8, a);
  p1 = __builtin_bit_cast(bf16x8, b);
}

#define EX2(x) __builtin_amdgcn_exp2f(x)
#define PHASE(S, PA, PB, M1, M2, M3, M4)                                              \
  do {                                                                                \
    M1; SBAR(); PA[0] = pk2(EX2(S[0]), EX2(S[1])); PA[1] = pk2(EX2(S[2]), EX2(S[3])); SBAR();  \
    M2; SBAR(); PA[2] = pk2(EX2(S[4]), EX2(S[5])); PA[3] = pk2(EX2(S[6]), EX2(S[7])); SBAR();  \
    M3; SBAR(); PB[0] = pk2(EX2(S[8]), EX2(S[9])); PB[1] = pk2(EX2(S[10]), EX2(S[11])); SBAR(); \
    M4; SBAR(); PB[2] = pk2(EX2(S[12]), EX2(S[13])); PB[3] = pk2(EX2(S[14]), EX2(S[15])); SBAR(); \
  } while (0)
#define BC8(x) __builtin_bit_cast(bf16x8, x)
DI void attn_fast(const u16* __restrict__ Kh, const u16* __restrict__ Vh, const bf16x8 (&qf)[2][2], char* lds,
                  f32x16 (&o)[2], int wv, int lane) {
  const int r32 = lane & 31, h = lane >> 5;
  const int koff0 = r32 * 64 + ((h ^ ((r32 >> 2) & 3)) << 4);
  const int koff1 = r32 * 64 + (((2 + h) ^ ((r32 >> 2) & 3)) << 4);
  const int voff = 8192 + lane * 16;
  const unsigned l0 = lds_addr(lds);
  f32x16 negm[2], sa, sb;
#pragma unroll
  for (int a = 0; a < 2; ++a)
#pragma unroll
    for (int r = 0; r < 16; ++r) { o[a][r] = 0.f; negm[a][r] = 0.f; }
  attn_stage(Kh, Vh, 0, l0, wv, lane);
  attn_stage(Kh, Vh, 1, l0 + 16384, wv, lane);
  attn_stage(Kh, Vh, 2, l0 + 32768, wv, lane);
  waitv<8>();
  BARRIER();
  bf16x8 kc0 = *(const bf16x8*)(lds + koff0), kc1 = *(const bf16x8*)(lds + koff1);
  {
#pragma unroll
    for (int a = 0; a < 2; ++a) {
      f32x16 s = MFMA32(kc0, qf[a][0], negm[a]);
      s = MFMA32(kc1, qf[a][1], s);
      float mx = max16(s);
#pragma unroll
      for (int r = 0; r < 16; ++r) { negm[a][r] = -mx; if (a == 0) sa[r] = s[r] - mx; }
    }
  }
  u32x4 pa0, pa1, pb0 = {0u, 0u, 0u, 0u}, pb1 = {0u, 0u, 0u, 0u};
  bf16x8 vp0 = {0, 0, 0, 0, 0, 0, 0, 0}, vp1 = {0, 0, 0, 0, 0, 0, 0, 0};
  for (int c = 0; c < 16; ++c) {
    if (c + 2 < 16) waitv<4>(); else waitv<0>();
    BARRIER();
    if (c + 3 < 16) attn_stage(Kh, Vh, c + 3, l0 + ((c + 3) & 3) * 16384, wv, lane);
    const char* buf = lds + (c & 3) * 16384;
    const char* bufn = lds + ((c + 1) & 3) * 16384;
#pragma unroll
    for (int t = 0; t < 4; ++t) {
      const char* kb = t < 3 ? buf + (t + 1) * 2048 : bufn;
      const char* vb = buf + t * 2048 + voff;
      bf16x8 kn0 = *(const bf16x8*)(kb + koff0), kn1 = *(const bf16x8*)(kb + koff1);
      bf16x8 vc0 = *(const bf16x8*)vb, vc1 = *(const bf16x8*)(vb + 1024);
      SBAR();
      PHASE(sa, pa0, pa1, sb = MFMA32(kc0, qf[1][0], negm[1]), o[1] = MFMA32(vp0, BC8(pb0), o[1]),
            sb = MFMA32(kc1, qf[1][1], sb), o[1] = MFMA32(vp1, BC8(pb1), o[1]));
      PHASE(sb, pb0, pb1, sa = MFMA32(kn0, qf[0][0], negm[0]), o[0] = MFMA32(vc0, BC8(pa0), o[0]),
            sa = MFMA32(kn1, qf[0][1], sa), o[0] = MFMA32(vc1, BC8(pa1), o[0]));
      kc0 = kn0; kc1 = kn1; vp0 = vc0; vp1 = vc1;
    }
  }
  o[1] = MFMA32(vp0, BC8(pb0), o[1]);
  o[1] = MFMA32(vp1, BC8(pb1), o[1]);
}

DI void attn_safe(const u16* __restrict__ Kh, const u16* __restrict__ Vh, const bf16x8 (&qf)[2][2], char* lds,
                  f32x16 (&o)[2], int wv, int lane) {
  const int r32 = lane & 31, h = lane >> 5;
  const int koff0 = r32 * 64 + ((h ^ ((r32 >> 2) & 3)) << 4);
  const int koff1 = r32 * 64 + (((2 + h) ^ ((r32 >> 2) & 3)) << 4);
  const int voff = 8192 + lane * 16;
  const unsigned l0 = lds_addr(lds);
  f32x16 zero;
  float m[2] = {-INFINITY, -INFINITY};
#pragma unroll
  for (int r = 0; r < 16; ++r) { o[0][r] = 0.f; o[1][r] = 0.f; zero[r] = 0.f; }
  for (int c = 0; c < 16; ++c) {
    BARRIER();
    attn_stage(Kh, Vh, c, l0, wv, lane);
    waitv<0>();
    BARRIER();
    for (int t = 0; t < 4; ++t) {
      const char* kb = lds + t * 2048;
      bf16x8 k0 = *(const bf16x8*)(kb + koff0), k1 = *(const bf16x8*)(kb + koff1);
      bf16x8 va = *(const bf16x8*)(kb + voff), vb = *(const bf16x8*)(kb + voff + 1024);
#pragma unroll
      for (int a = 0; a < 2; ++a) {
        f32x16 s = MFMA32(k0, qf[a][0], zero);
        s = MFMA32(k1, qf[a][1], s);
        float mn = fmaxf(m[a], max16(s));
        float al = __builtin_amdgcn_exp2f(m[a] - mn);
        m[a] = mn;
#pragma unroll
        for (int r = 0; r < 16; ++r) { o[a][r] *= al; s[r] -= mn; }
        bf16x8 p0, p1;
        exp_pack(s, p0, p1);
        o[a] = MFMA32(va, p0, o[a]);
        o[a] = MFMA32(vb, p1, o[a]);
      }
    }
  }
}

DI void attn_block(int bid, const u16* __restrict__ qkvp, const float* __restrict__ gate, u16* __restrict__ Ao,
                   char* lds) {
  const int tid = threadIdx.x, lane = tid & 63, r32 = lane & 31, h = lane >> 5;
  const int wv = __builtin_amdgcn_readfirstlane(tid >> 6);
  const int head = (bid & 7) * 4 + ((bid >> 3) & 3), qb = bid >> 5;
  const u16* Qh = qkvp + (size_t)head * (S_ * 32);
  const u16* Kh = qkvp + (size_t)(H_ + head) * (S_ * 32);
  const u16* Vh = qkvp + (size_t)(2 * H_ + head) * (S_ * 32);
  const int qrow0 = qb * 256 + wv * 64 + r32;
  bf16x8 qf[2][2];
#pragma unroll
  for (int a = 0; a < 2; ++a) {
    qf[a][0] = *(const bf16x8*)(Qh + (size_t)(qrow0 + a * 32) * 32 + h * 8);
    qf[a][1] = *(const bf16x8*)(Qh + (size_t)(qrow0 + a * 32) * 32 + 16 + h * 8);
  }
  float gq[2] = {gate[qrow0], gate[qrow0 + 32]};
  asm volatile("" ::"v"(qf[0][0]), "v"(qf[0][1]), "v"(qf[1][0]), "v"(qf[1][1]), "v"(gq[0]), "v"(gq[1]));
  f32x16 o[2];
  attn_fast(Kh, Vh, qf, lds, o, wv, lane);
  bool bad = false;
#pragma unroll
  for (int a = 0; a < 2; ++a)
#pragma unroll
    for (int r = 0; r < 11; ++r) bad |= (r < 8 || h == 0) && !(fabsf(o[a][r]) < 1e30f);
  if (__syncthreads_or(bad)) attn_safe(Kh, Vh, qf, lds, o, wv, lane);
#pragma unroll
  for (int a = 0; a < 2; ++a) {
    float l = __shfl(o[a][10], r32);
    float sc = gq[a] / l;
    u16* dst = Ao + (size_t)(qrow0 + a * 32) * D_ + head * DH_ + h * 4;
    *(unsigned*)(dst + 0) = pk2(o[a][0] * sc, o[a][1] * sc);
    *(unsigned*)(dst + 2) = pk2(o[a][2] * sc, o[a][3] * sc);
    *(unsigned*)(dst + 8) = pk2(o[a][4] * sc, o[a][5] * sc);
    *(unsigned*)(dst + 10) = pk2(o[a][6] * sc, o[a][7] * sc);
    if (h == 0) *(unsigned*)(dst + 16) = pk2(o[a][8] * sc, o[a][9] * sc);
  }
}

DI void gload16(bf16x8& dst, const void* sbase, unsigned voff) {
  asm volatile("global_load_dwordx4 %0, %1, %2" : "=v"(dst) : "v"(voff), "s"(sbase) : "memory");
}
DI void down_block(int bid, const u16* __restrict__ Ah, const u16* __restrict__ wd, float* __restrict__ bout,
                   char* lds) {
  const int tid = threadIdx.x, lane = tid & 63, r32 = lane & 31, h = lane >> 5;
  const int wv = __builtin_amdgcn_readfirstlane(tid >> 6);
  const int mt_blk = (bid & 7) * 4 + ((bid >> 3) & 3), nt_blk = bid >> 5;
  const int m0 = mt_blk * 64, n0 = nt_blk * 96;
  const u16* pa = Ah + (size_t)m0 * F_ + wv * 1152;
  const u16* pb = wd + (size_t)wv * 1152 * D_ + n0;
  char* ring = lds + wv * 18432;
  const unsigned lr = lds_addr(ring);
  const unsigned aof = (unsigned)(r32 * F_ + h * 16) * 2u;
  unsigned bof[6];
#pragma unroll
  for (int i = 0; i < 6; ++i) {
    int u = i * 64 + lane, st = u >> 4, kblk = st / 6, nblk = st - kblk * 6;
    int k = kblk * 8 + (((u >> 1) & 7) ^ ((nblk & 1) << 2));
    bof[i] = (unsigned)(k * D_ + nblk * 16 + (u & 1) * 8) * 2u;
  }
  const int nh = (lane >> 4) & 1;
  const int boff = ((2 * h) * 6 + nh) * 256 + ((lane & 15) >> 2) * 32 + (lane & 3) * 8;
  const int hlo = nh << 7, hhi = hlo ^ 128;
  f32x16 acc[3][2];
#pragma unroll
  for (int i = 0; i < 3; ++i)
#pragma unroll
    for (int j = 0; j < 2; ++j)
#pragma unroll
      for (int r = 0; r < 16; ++r) acc[i][j][r] = 0.f;
  bf16x8 a0[2][2], a1[2][2], a2[2][2];
  auto issue = [&](bf16x8 (&a)[2][2], int k, int slot) {
    const u16* ba = pa + k * 32;
    const u16* bb = pb + (size_t)k * 32 * D_;
#pragma unroll
    for (int i = 0; i < 6; ++i) glds16(bb, bof[i], lr + slot * 6144 + i * 1024);
    gload16(a[0][0], ba, aof);
    gload16(a[0][1], ba, aof + 16);
    gload16(a[1][0], ba, aof + 32 * F_ * 2);
    gload16(a[1][1], ba, aof + 32 * F_ * 2 + 16);
  };
  auto step = [&](bf16x8 (&a)[2][2], int k, int slot, auto wn_, auto more_) {
    asm volatile("s_waitcnt vmcnt(%4)" : "+v"(a[0][0]), "+v"(a[0][1]), "+v"(a[1][0]), "+v"(a[1][1]) : "n"(decltype(wn_)::value) : "memory");
    const char* sl = ring + slot * 6144 + boff;
    bf16x8 bf[2][3];
#pragma unroll
    for (int m = 0; m < 2; ++m)
#pragma unroll
      for (int nt = 0; nt < 3; ++nt) bf[m][nt] = bfrag2(sl + (m * 6 + nt * 2) * 256 + hlo, sl + (m * 6 + nt * 2) * 256 + hhi);
    bf16x8 ac[2][2] = {{a[0][0], a[0][1]}, {a[1][0], a[1][1]}};
    asm volatile("s_waitcnt lgkmcnt(0)" : "+v"(bf[0][0]), "+v"(bf[0][1]), "+v"(bf[0][2]), "+v"(bf[1][0]), "+v"(bf[1][1]), "+v"(bf[1][2]),
                 "+v"(ac[0][0]), "+v"(ac[0][1]), "+v"(ac[1][0]), "+v"(ac[1][1])::"memory");
    if (decltype(more_)::value) issue(a, k + 3, slot);
#pragma unroll
    for (int m = 0; m < 2; ++m)
#pragma unroll
      for (int nt = 0; nt < 3; ++nt)
#pragma unroll
        for (int mt = 0; mt < 2; ++mt) acc[nt][mt] = MFMA32(bf[m][nt], ac[mt][m], acc[nt][mt]);
  };
  issue(a0, 0, 0); issue(a1, 1, 1); issue(a2, 2, 2);
  using I20 = std::integral_constant<int, 20>; using I10 = std::integral_constant<int, 10>;
  using I0 = std::integral_constant<int, 0>; using I1 = std::integral_constant<int, 1>;
#pragma unroll
  for (int k = 0; k < 33; k += 3) {
    step(a0, k, 0, I20{}, I1{});
    step(a1, k + 1, 1, I20{}, I1{});
    step(a2, k + 2, 2, I20{}, I1{});
  }
  step(a0, 33, 0, I20{}, I0{});
  step(a1, 34, 1, I10{}, I0{});
  step(a2, 35, 2, I0{}, I0{});
  __syncthreads();
#pragma unroll
  for (int w = 0; w < 4; ++w) {
    if (w != wv) {
      int slot = w < wv ? wv - 1 : wv;
#pragma unroll
      for (int nt = 0; nt < 3; ++nt)
#pragma unroll
        for (int mt = 0; mt < 2; ++mt)
          *(f32x4*)(lds + ((w * 3 + slot) * 6 + nt * 2 + mt) * 1024 + lane * 16) =
              (f32x4){acc[nt][mt][4 * w], acc[nt][mt][4 * w + 1], acc[nt][mt][4 * w + 2], acc[nt][mt][4 * w + 3]};
    }
  }
  __syncthreads();
#pragma unroll
  for (int w = 0; w < 4; ++w) {
    if (w == wv) {
#pragma unroll
      for (int nt = 0; nt < 3; ++nt)
#pragma unroll
        for (int mt = 0; mt < 2; ++mt) {
          f32x4 v = {acc[nt][mt][4 * w], acc[nt][mt][4 * w + 1], acc[nt][mt][4 * w + 2], acc[nt][mt][4 * w + 3]};
#pragma unroll
          for (int sl = 0; sl < 3; ++sl) v += *(const f32x4*)(lds + ((w * 3 + sl) * 6 + nt * 2 + mt) * 1024 + lane * 16);
          *(f32x4*)(bout + (size_t)(m0 + mt * 32 + r32) * D_ + n0 + nt * 32 + 8 * w + 4 * h) = v;
        }
    }
  }
}

__global__ __launch_bounds__(256, 2) void k_mid(const u16* __restrict__ qkvp, const float* __restrict__ gate,
                                                u16* __restrict__ Ao, const u16* __restrict__ Ah,
                                                const u16* __restrict__ wd, float* __restrict__ part) {
  __shared__ __attribute__((aligned(16))) char lds[73728];
  const int bid = blockIdx.x;
  if (bid < 256) attn_block(bid, qkvp, gate, Ao, lds);
  else down_block(bid - 256, Ah, wd, part, lds);
}

constexpr int K3_STAGE = 4096 + 6144;
__global__ __launch_bounds__(256) void k_out(const u16* __restrict__ Ao, const u16* __restrict__ wo,
                                             const float* __restrict__ part, u16* __restrict__ out) {
  __shared__ __attribute__((aligned(16))) char lds[12 * K3_STAGE];
  const int tid = threadIdx.x, lane = tid & 63;
  const int wv = __builtin_amdgcn_readfirstlane(tid >> 6), wm = wv >> 1, wn = wv & 1;
  const int nt_blk = blockIdx.x % 6, mt_blk = blockIdx.x / 6;
  const int m0 = mt_blk * 64, n0 = nt_blk * 96;
  f32x4 acc[3][2];
#pragma unroll
  for (int nt = 0; nt < 3; ++nt)
#pragma unroll
    for (int mt = 0; mt < 2; ++mt) {
      size_t off = (size_t)(m0 + wm * 32 + mt * 16 + (lane & 15)) * D_ + n0 + wn * 48 + nt * 16 + 4 * (lane >> 4);
      acc[nt][mt] = *(const f32x4*)(part + off);
    }
  const u16* pa = Ao + (size_t)m0 * D_;
  const u16* pb = wo + n0;
  const unsigned aof0 = aseg_off(wv * 64 + lane, D_);
  const unsigned bof0 = bseg_off<6>(wv * 64 + lane, D_), bof1 = bseg_off<6>((4 + (wv & 1)) * 64 + lane, D_);
  auto piece = [&](int i, int kt, unsigned buf) {
    if (i == 0) glds16(pa + kt * 32, aof0, buf + wv * 1024);
    else if (i == 1) glds16(pb + (size_t)kt * 32 * D_, bof0, buf + 4096 + wv * 1024);
    else if (wv < 2) glds16(pb + (size_t)kt * 32 * D_, bof1, buf + 4096 + (4 + wv) * 1024);
  };
  auto waitL = [&](int n) {
    if (wv < 2) { if (n == 2) waitv<18>(); else waitv<9>(); }
    else { if (n == 2) waitv<12>(); else waitv<6>(); }
  };
  const int g = lane >> 4;
  const int aoff = (wm * 32 + (lane & 15)) * 64 + ((g ^ ((-(lane >> 2)) & 3)) << 4);
  const int boff = 4096 + (g * 6 + wn * 3) * 256 + ((lane & 15) >> 2) * 32 + (lane & 3) * 8;
  const int hlo = (g & 1) << 7, hhi = hlo ^ 128;
  auto ldA = [&](const char* st, int mt) { return *(const bf16x8*)(st + aoff + mt * 1024); };
  auto ldB = [&](const char* st, int nt) { return bfrag2(st + boff + nt * 256 + hlo, st + boff + nt * 256 + hhi); };
  auto extra = [&](int, int, unsigned) {};
  gemm_loop<2, 3, 3, K3_STAGE, 3>(6, lds, acc, ldA, ldB, piece, waitL, extra);
#pragma unroll
  for (int nt = 0; nt < 3; ++nt)
#pragma unroll
    for (int mt = 0; mt < 2; ++mt) {
      size_t off = (size_t)(m0 + wm * 32 + mt * 16 + (lane & 15)) * D_ + n0 + wn * 48 + nt * 16 + 4 * (lane >> 4);
      u32x2 v = {pk2(acc[nt][mt][0], acc[nt][mt][1]), pk2(acc[nt][mt][2], acc[nt][mt][3])};
      *(u32x2*)(out + off) = v;
    }
}

extern "C" void kernel_launch(void* const* d_in, const int* in_sizes, int n_in, void* d_out, int out_size,
                              void* d_ws, size_t ws_size, hipStream_t stream) {
  const u16* x = (const u16*)d_in[0];
  const u16* wqkv = (const u16*)d_in[1];
  const u16* wo = (const u16*)d_in[2];
  const u16* wg = (const u16*)d_in[4];
  const u16* wu = (const u16*)d_in[5];
  const u16* wd = (const u16*)d_in[6];
  const u16* gw = (const u16*)d_in[7];
  char* ws = (char*)d_ws;
  u16* qkvp = (u16*)ws;
  float* gate = (float*)(ws + (size_t)16 * 1024 * 1024);
  u16* Ao = (u16*)(ws + (size_t)17 * 1024 * 1024);
  u16* Ah = (u16*)(ws + (size_t)20 * 1024 * 1024);
  float* part = (float*)(ws + (size_t)48 * 1024 * 1024);
  k_proj<<<256, 512, 0, stream>>>(x, wqkv, wg, wu, gw, qkvp, gate, Ah);
  k_mid<<<448, 256, 0, stream>>>(qkvp, gate, Ao, Ah, wd, part);
  k_out<<<192, 256, 0, stream>>>(Ao, wo, part, (u16*)d_out);
}
```

```cpp
#include <hip/hip_runtime.h>
#include <hip/hip_bf16.h>
#include <type_traits>

typedef __attribute__((ext_vector_type(8))) short bf16x8;
typedef __attribute__((ext_vector_type(4))) short s16x4;
typedef __attribute__((ext_vector_type(4))) float f32x4;
typedef __attribute__((ext_vector_type(2))) float f32x2;
typedef __attribute__((ext_vector_type(16))) float f32x16;
typedef __attribute__((ext_vector_type(4))) unsigned u32x4;
typedef __attribute__((ext_vector_type(2))) unsigned u32x2;
typedef __attribute__((ext_vector_type(2))) __bf16 bf16x2_t;
typedef unsigned short u16;

#define DI __device__ __forceinline__
#define LDSP(T) __attribute__((address_space(3))) T

constexpr int S_ = 2048, D_ = 576, H_ = 32, DH_ = 18, F_ = 4608;

DI unsigned pk2(float lo, float hi) {
  f32x2 v = {lo, hi};
  bf16x2_t b = __builtin_convertvector(v, bf16x2_t);
  return __builtin_bit_cast(unsigned, b);
}
DI float rbf(float x) { return __uint_as_float(pk2(x, 0.f) << 16); }
DI float dot2bf(unsigned a, unsigned b, float c) {
  return __builtin_amdgcn_fdot2_f32_bf16(__builtin_bit_cast(bf16x2_t, a), __builtin_bit_cast(bf16x2_t, b), c, false);
}

DI void glds16(const void* sbase, unsigned voff, unsigned lds_base) {
  unsigned sv;
  asm volatile("s_mov_b32 %0, m0\n\ts_mov_b32 m0, %3\n\ts_nop 0\n\tglobal_load_lds_dwordx4 %1, %2\n\ts_mov_b32 m0, %0"
               : "=&s"(sv) : "v"(voff), "s"(sbase), "s"(lds_base) : "memory");
}
DI unsigned lds_addr(const char* p) { return (unsigned)(size_t)(LDSP(const char)*)p; }
DI s16x4 trread(const char* p) { return __builtin_amdgcn_ds_read_tr16_b64_v4i16((LDSP(s16x4)*)p); }
template <int N> DI void waitv() { asm volatile("s_waitcnt vmcnt(%0)" ::"n"(N) : "memory"); }
#define BARRIER() do { asm volatile("s_waitcnt lgkmcnt(0)" ::: "memory"); __builtin_amdgcn_s_barrier(); asm volatile("" ::: "memory"); } while (0)

#define SBAR() __builtin_amdgcn_sched_barrier(0)
template <int NBs>
DI unsigned bseg_off(int u, int ld) {
  int st = u >> 4, kblk = st / NBs, nblk = st - kblk * NBs;
  int rr = (u >> 1) & 7, hf = u & 1;
  int k = kblk * 8 + (rr ^ ((kblk & 1) << 2));
  return (unsigned)(k * ld + nblk * 16 + hf * 8) * 2u;
}
DI unsigned aseg_off(int u, int lda) {
  int row = u >> 2, ch = (u & 3) ^ ((-(row >> 2)) & 3);
  return (unsigned)(row * lda + ch * 8) * 2u;
}
DI bf16x8 bfrag2(const char* lo, const char* hi) {
  s16x4 a = trread(lo), b = trread(hi);
  return __builtin_shufflevector(a, b, 0, 1, 2, 3, 4, 5, 6, 7);
}

template <int N> using IC = std::integral_constant<int, N>;
template <int MT, int NT, int LMAX, int STAGE, int KS, int NR, class LdA, class LdB, class Piece, class WaitL, class Extra, class Bar>
DI void gemm_loop(int nsup, char* lds, f32x4 (&acc)[NT][MT], LdA ldA, LdB ldB, Piece piece, WaitL waitL, Extra extra, Bar bar) {
  static_assert(NT >= MT + 1 && NT >= LMAX, "schedule");
  constexpr int SUP = KS * STAGE;
  const unsigned l0 = lds_addr(lds);
#pragma unroll
  for (int s = 0; s < (NR - 1) * KS; ++s)
#pragma unroll
    for (int i = 0; i < LMAX; ++i) piece(i, s, l0 + s * STAGE);
  waitL(IC<NR - 2>{});
  bar();
  bf16x8 af[MT], bw0, bw1;
#pragma unroll
  for (int mt = 0; mt < MT; ++mt) af[mt] = ldA(lds, mt);
  bw0 = ldB(lds, 0);
  bw1 = ldB(lds, 1);
  int cb = 0;
  for (int j = 0; j < nsup; ++j) {
    if (j + NR - 2 < nsup) waitL(IC<NR - 3>{}); else waitv<0>();
    bar();
    const int nb_ = cb + 1 == NR ? 0 : cb + 1, pb_ = cb == 0 ? NR - 1 : cb - 1;
    const char* base = lds + cb * SUP;
    const char* nbase = lds + nb_ * SUP;
    const unsigned dst = l0 + pb_ * SUP;
    const bool more = j + NR - 1 < nsup;
#pragma unroll
    for (int ks = 0; ks < KS; ++ks) {
      const char* cur = base + ks * STAGE;
      const char* nx = ks + 1 < KS ? base + (ks + 1) * STAGE : nbase;
      bf16x8 an[MT];
#pragma unroll
      for (int nt = 0; nt < NT; ++nt) {
        bf16x8 b = (nt & 1) ? bw1 : bw0;
        bf16x8 nb = nt + 2 < NT ? ldB(cur, nt + 2) : ldB(nx, nt + 2 - NT);
        if (nt & 1) bw1 = nb; else bw0 = nb;
        if (nt >= NT - 1 - MT && nt < NT - 1) an[nt - (NT - 1 - MT)] = ldA(nx, nt - (NT - 1 - MT));
        if (nt < LMAX && more) piece(nt, (j + NR - 1) * KS + ks, dst + ks * STAGE);
#pragma unroll
        for (int mt = 0; mt < MT; ++mt) acc[nt][mt] = __builtin_amdgcn_mfma_f32_16x16x32_bf16(b, af[mt], acc[nt][mt], 0, 0, 0);
        if (more) extra(nt, (j + NR - 1) * KS + ks, dst + ks * STAGE - l0);
        SBAR();
      }
#pragma unroll
      for (int mt = 0; mt < MT; ++mt) af[mt] = an[mt];
      if (NT & 1) { bf16x8 t = bw0; bw0 = bw1; bw1 = t; }
    }
    cb = nb_;
  }
}

struct GroupBar {
  LDSP(unsigned)* ctr;
  unsigned target;
  DI void operator()() {
    target += 4;
    asm volatile("s_waitcnt lgkmcnt(0)" ::: "memory");
    if ((threadIdx.x & 63) == 0) __hip_atomic_fetch_add(ctr, 1u, __ATOMIC_RELAXED, __HIP_MEMORY_SCOPE_WORKGROUP);
    while (true) {
      unsigned v = __builtin_amdgcn_readfirstlane(*(volatile LDSP(unsigned)*)ctr);
      if ((int)(v - target) >= 0) break;
      __builtin_amdgcn_s_sleep(1);
    }
    asm volatile("" ::: "memory");
  }
};

constexpr float QSCALE = 0.23570226039551584f * 1.4426950408889634f;
constexpr int K1_STAGE = 16384 + 9216 + 9216 + 4096;

__global__ __launch_bounds__(512) void k_proj(const u16* __restrict__ x, const u16* __restrict__ wqkv,
                                              const u16* __restrict__ wg, const u16* __restrict__ wu,
                                              const u16* __restrict__ gate_w, u16* __restrict__ qkvp,
                                              float* __restrict__ gate, u16* __restrict__ Ah) {
  __shared__ __attribute__((aligned(16))) char lds[4 * K1_STAGE + 1152];
  const int tid = threadIdx.x, lane = tid & 63;
  const int wv = __builtin_amdgcn_readfirstlane(tid >> 6), wm = wv >> 1, wn = wv & 1;
  const int bid = blockIdx.x;
  const int nt_blk = (bid & 7) * 4 + ((bid >> 3) & 3), mt_blk = bid >> 5;
  const int m0 = mt_blk * 256, f0 = nt_blk * 144;
  const int coff = nt_blk == 31 ? 2 : 0;
  const int c0 = nt_blk * 54 - coff;
  char* gwl = lds + 4 * K1_STAGE;
  if (tid < 72) *(u32x4*)(gwl + tid * 16) = *(const u32x4*)(gate_w + tid * 8);

  f32x4 acc[11][4] = {};
  const u16* xa = x + (size_t)m0 * D_;
  const unsigned aof0 = aseg_off(wv * 64 + lane, D_), aof1 = aseg_off((8 + wv) * 64 + lane, D_);
  unsigned bof[3];
  const u16* bsrc[3];
  int bld[3];
#pragma unroll
  for (int i = 0; i < 3; ++i) {
    int jj = i * 8 + wv;
    if (jj < 18) {
      int j = jj < 9 ? jj : jj - 9;
      bof[i] = bseg_off<9>(j * 64 + lane, F_);
      bsrc[i] = (jj < 9 ? wg : wu) + f0;
      bld[i] = F_;
    } else {
      bof[i] = bseg_off<4>(((jj - 18) & 3) * 64 + lane, 3 * D_);
      bsrc[i] = wqkv + c0;
      bld[i] = 3 * D_;
    }
  }
  const bool padlane = (lane & 1) && (lane >> 4) == 3;
  auto piece = [&](int i, int kt, unsigned buf) {
    if (i == 0) glds16(xa + kt * 32, aof0, buf + wv * 1024);
    else if (i == 1) glds16(xa + kt * 32, aof1, buf + (8 + wv) * 1024);
    else if (i < 4) glds16(bsrc[i - 2] + (size_t)kt * 32 * bld[i - 2], bof[i - 2], buf + 16384 + ((i - 2) * 8 + wv) * 1024);
    else if (wv < 2) glds16(bsrc[2] + (size_t)kt * 32 * bld[2], bof[2], buf + 16384 + (16 + wv) * 1024);
    else if (wv < 6) { if (!padlane) glds16(bsrc[2] + (size_t)kt * 32 * bld[2], bof[2], buf + 16384 + (16 + wv) * 1024); }
  };
  const int gk = lane & 31, gkb = gk >> 3;
  const int gdst = 16384 + 18432 + (gkb * 4 + 3) * 256 + ((gk & 7) ^ ((gkb & 1) << 2)) * 32 + 16;
  auto gfill = [&](int kt, unsigned off) {
    if (wv == 6 && lane < 32) {
      u32x4 v = {(unsigned)*(const u16*)(gwl + (kt * 32 + gk) * 2), 0u, 0u, 0u};
      *(u32x4*)(lds + off + gdst) = v;
    }
  };
  __syncthreads();
  gfill(0, 0); gfill(1, K1_STAGE); gfill(2, 2 * K1_STAGE);
  auto waitL = [&](auto n) { if (wv < 6) waitv<5 * decltype(n)::value>(); else waitv<4 * decltype(n)::value>(); };
  auto bar = [&]() { BARRIER(); };
  const int g = lane >> 4;
  const int bl = ((lane & 15) >> 2) * 32 + (lane & 3) * 8;
  const int aoff = (wm * 64 + (lane & 15)) * 64 + ((g ^ ((-(lane >> 2)) & 3)) << 4);
  const int bgu = 16384 + wn * 9216 + g * 9 * 256 + bl;
  const int bqq = 16384 + 18432 + (g * 4 + wn * 2) * 256 + bl;
  const int hlo = (g & 1) << 7, hhi = hlo ^ 128;
  auto ldA = [&](const char* st, int mt) { return *(const bf16x8*)(st + aoff + mt * 1024); };
  auto ldB = [&](const char* st, int nt) {
    const char* bp = st + (nt < 9 ? bgu + nt * 256 : bqq + (nt - 9) * 256);
    return bfrag2(bp + hlo, bp + hhi);
  };
  auto extra = [&](int nt, int kt, unsigned off) { if (nt == 5) gfill(kt, off); };
  gemm_loop<4, 11, 5, K1_STAGE, 1, 4>(18, lds, acc, ldA, ldB, piece, waitL, extra, bar);
  BARRIER();
  int tz = threadIdx.x;
  asm volatile("" : "+v"(tz));
  const int lz = tz & 63, gz = lz >> 4, tk = lz & 15;

  char* H = lds + 73728;
  float* G = (float*)(lds + 73728 + 256 * 304);
  const int hg0 = nt_blk * 3;
  {
    char* xs = lds + wv * 9216 + lz * 8;
    if (wn == 1) {
#pragma unroll
      for (int nt = 0; nt < 9; ++nt)
#pragma unroll
        for (int m2 = 0; m2 < 2; ++m2) { f32x4 t = acc[nt][m2]; acc[nt][m2] = acc[nt][2 + m2]; acc[nt][2 + m2] = t; }
    }
#pragma unroll
    for (int nt = 0; nt < 9; ++nt)
#pragma unroll
      for (int m2 = 0; m2 < 2; ++m2) {
        f32x4 v = acc[nt][2 + m2];
        *(u32x2*)(xs + (nt * 2 + m2) * 512) = (u32x2){pk2(v[0], v[1]), pk2(v[2], v[3])};
      }
    if (wn == 1 && gz == 2) {
#pragma unroll
      for (int mt = 0; mt < 4; ++mt) {
        float gv = rbf(1.f / (1.f + __expf(-rbf(acc[10][mt][0]))));
        G[wm * 64 + mt * 16 + tk] = gv;
        if (nt_blk == 0) gate[m0 + wm * 64 + mt * 16 + tk] = gv;
      }
    }
  }
  __syncthreads();
  {
    const char* xr = lds + (wv ^ 1) * 9216 + lz * 8;
    u32x2 pv[9][2];
#pragma unroll
    for (int nt = 0; nt < 9; ++nt)
#pragma unroll
      for (int m2 = 0; m2 < 2; ++m2) pv[nt][m2] = *(const u32x2*)(xr + (nt * 2 + m2) * 512);
    float og[2];
#pragma unroll
    for (int m2 = 0; m2 < 2; ++m2) og[m2] = rbf(1.f - G[wm * 64 + (wn * 2 + m2) * 16 + tk]);
    asm volatile("s_waitcnt lgkmcnt(0)" ::: "memory");
    __builtin_amdgcn_s_barrier();
#pragma unroll
    for (int nt = 0; nt < 9; ++nt)
#pragma unroll
      for (int m2 = 0; m2 < 2; ++m2) {
        f32x4 v = acc[nt][m2];
        u32x2 uv = pv[nt][m2];
        float h0 = rbf(v[0]) * __uint_as_float(uv[0] << 16) * og[m2];
        float h1 = rbf(v[1]) * __uint_as_float(uv[0] & 0xffff0000u) * og[m2];
        float h2 = rbf(v[2]) * __uint_as_float(uv[1] << 16) * og[m2];
        float h3 = rbf(v[3]) * __uint_as_float(uv[1] & 0xffff0000u) * og[m2];
        *(u32x2*)(H + (wm * 64 + (wn * 2 + m2) * 16 + tk) * 304 + (nt * 16 + 4 * gz) * 2) = (u32x2){pk2(h0, h1), pk2(h2, h3)};
      }
  }
  {
    char* img = lds;
#pragma unroll
    for (int t2 = 0; t2 < 2; ++t2)
#pragma unroll
      for (int r = 0; r < 4; r += 2) {
        int c = wn * 32 + t2 * 16 + 4 * gz + r - coff;
        if (c >= 0 && c < 54) {
          int hl = c / 18, d = c - hl * 18;
          int which = (hg0 + hl) >> 5;
          float sc = which == 0 ? QSCALE : 1.f;
#pragma unroll
          for (int mt = 0; mt < 4; ++mt) {
            int tok = wm * 64 + mt * 16 + tk;
            unsigned v = pk2(acc[9 + t2][mt][r] * sc, acc[9 + t2][mt][r + 1] * sc);
            if (which == 2) {
              char* q = img + hl * 16384 + (tok >> 5) * 2048 + ((((tok >> 4) & 1) * 2 + ((tok >> 2) & 1)) * 32 + d) * 16 +
                        ((tok & 3) + 4 * ((tok >> 3) & 1)) * 2;
              *(u16*)q = (u16)v;
              *(u16*)(q + 16) = (u16)(v >> 16);
            } else {
              int ch = d >> 3;
              if (which == 1) ch ^= (tok >> 2) & 3;
              *(unsigned*)(img + (hl * 256 + tok) * 64 + ch * 16 + (d & 7) * 2) = v;
            }
          }
        }
      }
    for (int i = tz; i < 768; i += 512) {
      int which = (hg0 + (i >> 8)) >> 5;
      if (which == 2) {
        char* grp = img + (i >> 8) * 16384 + ((i & 255) >> 3) * 512;
        int d0 = 18 + (i & 7) * 2;
        unsigned one = d0 == 18 ? 0x3f803f80u : 0u;
        if (d0 < 32) {
          *(u32x4*)(grp + d0 * 16) = (u32x4){one, one, one, one};
          *(u32x4*)(grp + d0 * 16 + 16) = (u32x4){0u, 0u, 0u, 0u};
        }
      } else {
        int sw = which == 1 ? (i >> 2) & 3 : 0;
        char* row = img + i * 64;
        *(unsigned*)(row + (2 ^ sw) * 16 + 4) = 0u;
        *(u32x2*)(row + (2 ^ sw) * 16 + 8) = (u32x2){0u, 0u};
        *(u32x4*)(row + (3 ^ sw) * 16) = (u32x4){0u, 0u, 0u, 0u};
      }
    }
  }
  __syncthreads();
#pragma unroll
  for (int i = 0; i < 9; ++i) {
    int u = i * 512 + tz;
    int row = u / 18, cu = u - row * 18;
    u32x4 v = *(const u32x4*)(H + row * 304 + cu * 16);
    *(u32x4*)(Ah + (size_t)(m0 + row) * F_ + f0 + cu * 8) = v;
  }
#pragma unroll
  for (int i = 0; i < 6; ++i) {
    int u = i * 512 + tz;
    int hl = u >> 10, rem = u & 1023;
    u32x4 v = *(const u32x4*)(lds + u * 16);
    *(u32x4*)(qkvp + ((size_t)(nt_blk * 3 + hl) * S_ + m0) * 32 + rem * 8) = v;
  }
}

DI void attn_stage(const u16* __restrict__ Kh, const u16* __restrict__ Vh, int c, unsigned buf, int wv, int lane) {
#pragma unroll
  for (int i = 0; i < 2; ++i) glds16(Kh + (size_t)c * 4096, ((i * 4 + wv) * 64 + lane) * 16u, buf + (i * 4 + wv) * 1024);
#pragma unroll
  for (int i = 0; i < 2; ++i)
    glds16(Vh + (size_t)c * 4096, ((i * 4 + wv) * 64 + lane) * 16u, buf + 8192 + (i * 4 + wv) * 1024);
}

DI float halfswap_max(float m) {
  auto rr = __builtin_amdgcn_permlane32_swap(__float_as_uint(m), __float_as_uint(m), false, false);
  return fmaxf(__uint_as_float(rr[0]), __uint_as_float(rr[1]));
}
DI float max16(const f32x16& s) {
  float mx = s[0];
#pragma unroll
  for (int r = 1; r < 16; ++r) mx = fmaxf(mx, s[r]);
  return halfswap_max(mx);
}
#define MFMA32(a, b, c) __builtin_amdgcn_mfma_f32_32x32x16_bf16((a), (b), (c), 0, 0, 0)

DI void exp_pack(const f32x16& s, bf16x8& p0, bf16x8& p1) {
  u32x4 a, b;
#pragma unroll
  for (int r = 0; r < 4; ++r) a[r] = pk2(__builtin_amdgcn_exp2f(s[2 * r]), __builtin_amdgcn_exp2f(s[2 * r + 1]));
#pragma unroll
  for (int r = 0; r < 4; ++r) b[r] = pk2(__builtin_amdgcn_exp2f(s[8 + 2 * r]), __builtin_amdgcn_exp2f(s[9 + 2 * r]));
  p0 = __builtin_bit_cast(bf16x8, a);
  p1 = __builtin_bit_cast(bf16x8, b);
}

#define EX2(x) __builtin_amdgcn_exp2f(x)
#define PHASE(S, PA, PB, M1, M2, M3, M4)                                              \
  do {                                                                                \
    M1; SBAR(); PA[0] = pk2(EX2(S[0]), EX2(S[1])); PA[1] = pk2(EX2(S[2]), EX2(S[3])); SBAR();  \
    M2; SBAR(); PA[2] = pk2(EX2(S[4]), EX2(S[5])); PA[3] = pk2(EX2(S[6]), EX2(S[7])); SBAR();  \
    M3; SBAR(); PB[0] = pk2(EX2(S[8]), EX2(S[9])); PB[1] = pk2(EX2(S[10]), EX2(S[11])); SBAR(); \
    M4; SBAR(); PB[2] = pk2(EX2(S[12]), EX2(S[13])); PB[3] = pk2(EX2(S[14]), EX2(S[15])); SBAR(); \
  } while (0)
#define BC8(x) __builtin_bit_cast(bf16x8, x)
DI void attn_fast(const u16* __restrict__ Kh, const u16* __restrict__ Vh, const bf16x8 (&qf)[2][2], char* lds,
                  f32x16 (&o)[2], int wv, int lane, GroupBar& gb) {
  const int r32 = lane & 31, h = lane >> 5;
  const int koff0 = r32 * 64 + ((h ^ ((r32 >> 2) & 3)) << 4);
  const int koff1 = r32 * 64 + (((2 + h) ^ ((r32 >> 2) & 3)) << 4);
  const int voff = 8192 + lane * 16;
  const unsigned l0 = lds_addr(lds);
  f32x16 negm[2], sa, sb;
#pragma unroll
  for (int a = 0; a < 2; ++a)
#pragma unroll
    for (int r = 0; r < 16; ++r) { o[a][r] = 0.f; negm[a][r] = 0.f; }
  attn_stage(Kh, Vh, 0, l0, wv, lane);
  attn_stage(Kh, Vh, 1, l0 + 16384, wv, lane);
  attn_stage(Kh, Vh, 2, l0 + 32768, wv, lane);
  waitv<8>();
  gb();
  bf16x8 kc0 = *(const bf16x8*)(lds + koff0), kc1 = *(const bf16x8*)(lds + koff1);
  {
#pragma unroll
    for (int a = 0; a < 2; ++a) {
      f32x16 s = MFMA32(kc0, qf[a][0], negm[a]);
      s = MFMA32(kc1, qf[a][1], s);
      float mx = max16(s);
#pragma unroll
      for (int r = 0; r < 16; ++r) { negm[a][r] = -mx; if (a == 0) sa[r] = s[r] - mx; }
    }
  }
  u32x4 pa0, pa1, pb0 = {0u, 0u, 0u, 0u}, pb1 = {0u, 0u, 0u, 0u};
  bf16x8 vp0 = {0, 0, 0, 0, 0, 0, 0, 0}, vp1 = {0, 0, 0, 0, 0, 0, 0, 0};
  for (int c = 0; c < 16; ++c) {
    if (c + 2 < 16) waitv<4>(); else waitv<0>();
    gb();
    if (c + 3 < 16) attn_stage(Kh, Vh, c + 3, l0 + ((c + 3) & 3) * 16384, wv, lane);
    const char* buf = lds + (c & 3) * 16384;
    const char* bufn = lds + ((c + 1) & 3) * 16384;
#pragma unroll
    for (int t = 0; t < 4; ++t) {
      const char* kb = t < 3 ? buf + (t + 1) * 2048 : bufn;
      const char* vb = buf + t * 2048 + voff;
      bf16x8 kn0 = *(const bf16x8*)(kb + koff0), kn1 = *(const bf16x8*)(kb + koff1);
      bf16x8 vc0 = *(const bf16x8*)vb, vc1 = *(const bf16x8*)(vb + 1024);
      SBAR();
      PHASE(sa, pa0, pa1, sb = MFMA32(kc0, qf[1][0], negm[1]), o[1] = MFMA32(vp0, BC8(pb0), o[1]),
            sb = MFMA32(kc1, qf[1][1], sb), o[1] = MFMA32(vp1, BC8(pb1), o[1]));
      PHASE(sb, pb0, pb1, sa = MFMA32(kn0, qf[0][0], negm[0]), o[0] = MFMA32(vc0, BC8(pa0), o[0]),
            sa = MFMA32(kn1, qf[0][1], sa), o[0] = MFMA32(vc1, BC8(pa1), o[0]));
      kc0 = kn0; kc1 = kn1; vp0 = vc0; vp1 = vc1;
    }
  }
  o[1] = MFMA32(vp0, BC8(pb0), o[1]);
  o[1] = MFMA32(vp1, BC8(pb1), o[1]);
}

DI void attn_safe(const u16* __restrict__ Kh, const u16* __restrict__ Vh, const bf16x8 (&qf)[2][2], char* lds,
                  f32x16 (&o)[2], int wv, int lane, GroupBar& gb) {
  const int r32 = lane & 31, h = lane >> 5;
  const int koff0 = r32 * 64 + ((h ^ ((r32 >> 2) & 3)) << 4);
  const int koff1 = r32 * 64 + (((2 + h) ^ ((r32 >> 2) & 3)) << 4);
  const int voff = 8192 + lane * 16;
  const unsigned l0 = lds_addr(lds);
  f32x16 zero;
  float m[2] = {-INFINITY, -INFINITY};
#pragma unroll
  for (int r = 0; r < 16; ++r) { o[0][r] = 0.f; o[1][r] = 0.f; zero[r] = 0.f; }
  for (int c = 0; c < 16; ++c) {
    gb();
    attn_stage(Kh, Vh, c, l0, wv, lane);
    waitv<0>();
    gb();
    for (int t = 0; t < 4; ++t) {
      const char* kb = lds + t * 2048;
      bf16x8 k0 = *(const bf16x8*)(kb + koff0), k1 = *(const bf16x8*)(kb + koff1);
      bf16x8 va = *(const bf16x8*)(kb + voff), vb = *(const bf16x8*)(kb + voff + 1024);
#pragma unroll
      for (int a = 0; a < 2; ++a) {
        f32x16 s = MFMA32(k0, qf[a][0], zero);
        s = MFMA32(k1, qf[a][1], s);
        float mn = fmaxf(m[a], max16(s));
        float al = __builtin_amdgcn_exp2f(m[a] - mn);
        m[a] = mn;
#pragma unroll
        for (int r = 0; r < 16; ++r) { o[a][r] *= al; s[r] -= mn; }
        bf16x8 p0, p1;
        exp_pack(s, p0, p1);
        o[a] = MFMA32(va, p0, o[a]);
        o[a] = MFMA32(vb, p1, o[a]);
      }
    }
  }
}

DI void attn_block(int bid, const u16* __restrict__ qkvp, const float* __restrict__ gate, u16* __restrict__ Ao,
                   char* lds, LDSP(unsigned)* sync) {
  const int tid = threadIdx.x, lane = tid & 63, r32 = lane & 31, h = lane >> 5;
  const int wv = __builtin_amdgcn_readfirstlane(tid >> 6);
  GroupBar gb{sync, 0u};
  const int head = (bid & 7) * 4 + ((bid >> 3) & 3), qb = bid >> 5;
  const u16* Qh = qkvp + (size_t)head * (S_ * 32);
  const u16* Kh = qkvp + (size_t)(H_ + head) * (S_ * 32);
  const u16* Vh = qkvp + (size_t)(2 * H_ + head) * (S_ * 32);
  const int qrow0 = qb * 256 + wv * 64 + r32;
  bf16x8 qf[2][2];
#pragma unroll
  for (int a = 0; a < 2; ++a) {
    qf[a][0] = *(const bf16x8*)(Qh + (size_t)(qrow0 + a * 32) * 32 + h * 8);
    qf[a][1] = *(const bf16x8*)(Qh + (size_t)(qrow0 + a * 32) * 32 + 16 + h * 8);
  }
  float gq[2] = {gate[qrow0], gate[qrow0 + 32]};
  asm volatile("" ::"v"(qf[0][0]), "v"(qf[0][1]), "v"(qf[1][0]), "v"(qf[1][1]), "v"(gq[0]), "v"(gq[1]));
  f32x16 o[2];
  attn_fast(Kh, Vh, qf, lds, o, wv, lane, gb);
  bool bad = false;
#pragma unroll
  for (int a = 0; a < 2; ++a)
#pragma unroll
    for (int r = 0; r < 11; ++r) bad |= (r < 8 || h == 0) && !(fabsf(o[a][r]) < 1e30f);
  if (__any(bad) && lane == 0) *(volatile LDSP(unsigned)*)(sync + 1) = 1u;
  gb();
  if (__builtin_amdgcn_readfirstlane(*(volatile LDSP(unsigned)*)(sync + 1))) attn_safe(Kh, Vh, qf, lds, o, wv, lane, gb);
#pragma unroll
  for (int a = 0; a < 2; ++a) {
    float l = __shfl(o[a][10], r32);
    float sc = gq[a] / l;
    u16* dst = Ao + (size_t)(qrow0 + a * 32) * D_ + head * DH_ + h * 4;
    *(unsigned*)(dst + 0) = pk2(o[a][0] * sc, o[a][1] * sc);
    *(unsigned*)(dst + 2) = pk2(o[a][2] * sc, o[a][3] * sc);
    *(unsigned*)(dst + 8) = pk2(o[a][4] * sc, o[a][5] * sc);
    *(unsigned*)(dst + 10) = pk2(o[a][6] * sc, o[a][7] * sc);
    if (h == 0) *(unsigned*)(dst + 16) = pk2(o[a][8] * sc, o[a][9] * sc);
  }
}

constexpr int K2_STAGE = 8192 + 9216, K2_NR = 5;
DI void down_block(int bid, const u16* __restrict__ Ah, const u16* __restrict__ wd, float* __restrict__ part,
                   char* lds, LDSP(unsigned)* sync) {
  const int tid = threadIdx.x, lane = tid & 63;
  const int wv = __builtin_amdgcn_readfirstlane(tid >> 6) - 4;
  GroupBar gb{sync, 0u};
  const int mt_blk = (bid & 7) * 2 + ((bid >> 3) & 1), rest = bid >> 4;
  const int nt_blk = rest & 3, sp = rest >> 2;
  const int m0 = mt_blk * 128, n0 = nt_blk * 144;
  f32x4 acc[9][2] = {};
  const u16* pa = Ah + (size_t)m0 * F_ + sp * 1152;
  const u16* pb = wd + (size_t)sp * 1152 * D_ + n0;
  const unsigned aof0 = aseg_off(wv * 64 + lane, F_), aof1 = aseg_off((4 + wv) * 64 + lane, F_);
  const unsigned bof0 = bseg_off<9>(wv * 64 + lane, D_), bof1 = bseg_off<9>((4 + wv) * 64 + lane, D_),
                 bof2 = bseg_off<9>(8 * 64 + lane, D_);
  auto piece = [&](int i, int kt, unsigned buf) {
    if (i == 0) glds16(pa + kt * 32, aof0, buf + wv * 1024);
    else if (i == 1) glds16(pa + kt * 32, aof1, buf + (4 + wv) * 1024);
    else if (i == 2) glds16(pb + (size_t)kt * 32 * D_, bof0, buf + 8192 + wv * 1024);
    else if (i == 3) glds16(pb + (size_t)kt * 32 * D_, bof1, buf + 8192 + (4 + wv) * 1024);
    else if (wv == 0) glds16(pb + (size_t)kt * 32 * D_, bof2, buf + 8192 + 8 * 1024);
  };
  auto waitL = [&](auto n) { if (wv == 0) waitv<5 * decltype(n)::value>(); else waitv<4 * decltype(n)::value>(); };
  const int g = lane >> 4;
  const int aoff = (wv * 32 + (lane & 15)) * 64 + ((g ^ ((-(lane >> 2)) & 3)) << 4);
  const int boff = 8192 + g * 9 * 256 + ((lane & 15) >> 2) * 32 + (lane & 3) * 8;
  const int hlo = (g & 1) << 7, hhi = hlo ^ 128;
  auto ldA = [&](const char* st, int mt) { return *(const bf16x8*)(st + aoff + mt * 1024); };
  auto ldB = [&](const char* st, int nt) { return bfrag2(st + boff + nt * 256 + hlo, st + boff + nt * 256 + hhi); };
  auto extra = [&](int, int, unsigned) {};
  auto bar = [&]() { gb(); };
  gemm_loop<2, 9, 5, K2_STAGE, 1, K2_NR>(36, lds, acc, ldA, ldB, piece, waitL, extra, bar);
  float* dst = part + (size_t)sp * (S_ * D_);
#pragma unroll
  for (int nt = 0; nt < 9; ++nt)
#pragma unroll
    for (int mt = 0; mt < 2; ++mt) {
      int tok = m0 + wv * 32 + mt * 16 + (lane & 15);
      int n = n0 + nt * 16 + 4 * (lane >> 4);
      *(f32x4*)(dst + (size_t)tok * D_ + n) = acc[nt][mt];
    }
}

__global__ __launch_bounds__(512) void k_mid(const u16* __restrict__ qkvp, const float* __restrict__ gate,
                                             u16* __restrict__ Ao, const u16* __restrict__ Ah,
                                             const u16* __restrict__ wd, float* __restrict__ part) {
  __shared__ __attribute__((aligned(16))) char lds[65536 + K2_NR * K2_STAGE + 64];
  LDSP(unsigned)* sync = (LDSP(unsigned)*)(lds + 65536 + K2_NR * K2_STAGE);
  if (threadIdx.x < 16) sync[threadIdx.x] = 0u;
  __syncthreads();
  const int bid = blockIdx.x;
  if (__builtin_amdgcn_readfirstlane(threadIdx.x) < 256) attn_block(bid, qkvp, gate, Ao, lds, sync);
  else down_block(bid, Ah, wd, part, lds + 65536, sync + 8);
}

constexpr int K3_STAGE = 4096 + 6144;
__global__ __launch_bounds__(256) void k_out(const u16* __restrict__ Ao, const u16* __restrict__ wo,
                                             const float* __restrict__ part, u16* __restrict__ out) {
  __shared__ __attribute__((aligned(16))) char lds[12 * K3_STAGE];
  const int tid = threadIdx.x, lane = tid & 63;
  const int wv = __builtin_amdgcn_readfirstlane(tid >> 6), wm = wv >> 1, wn = wv & 1;
  const int nt_blk = blockIdx.x % 6, mt_blk = blockIdx.x / 6;
  const int m0 = mt_blk * 64, n0 = nt_blk * 96;
  f32x4 acc[3][2];
#pragma unroll
  for (int nt = 0; nt < 3; ++nt)
#pragma unroll
    for (int mt = 0; mt < 2; ++mt) {
      size_t off = (size_t)(m0 + wm * 32 + mt * 16 + (lane & 15)) * D_ + n0 + wn * 48 + nt * 16 + 4 * (lane >> 4);
      f32x4 s = *(const f32x4*)(part + off);
#pragma unroll
      for (int sp = 1; sp < 4; ++sp) s += *(const f32x4*)(part + (size_t)sp * (S_ * D_) + off);
      acc[nt][mt] = s;
    }
  const u16* pa = Ao + (size_t)m0 * D_;
  const u16* pb = wo + n0;
  const unsigned aof0 = aseg_off(wv * 64 + lane, D_);
  const unsigned bof0 = bseg_off<6>(wv * 64 + lane, D_), bof1 = bseg_off<6>((4 + (wv & 1)) * 64 + lane, D_);
  auto piece = [&](int i, int kt, unsigned buf) {
    if (i == 0) glds16(pa + kt * 32, aof0, buf + wv * 1024);
    else if (i == 1) glds16(pb + (size_t)kt * 32 * D_, bof0, buf + 4096 + wv * 1024);
    else if (wv < 2) glds16(pb + (size_t)kt * 32 * D_, bof1, buf + 4096 + (4 + wv) * 1024);
  };
  auto waitL = [&](auto n) { if (wv < 2) waitv<9 * decltype(n)::value>(); else waitv<6 * decltype(n)::value>(); };
  auto bar = [&]() { BARRIER(); };
  const int g = lane >> 4;
  const int aoff = (wm * 32 + (lane & 15)) * 64 + ((g ^ ((-(lane >> 2)) & 3)) << 4);
  const int boff = 4096 + (g * 6 + wn * 3) * 256 + ((lane & 15) >> 2) * 32 + (lane & 3) * 8;
  const int hlo = (g & 1) << 7, hhi = hlo ^ 128;
  auto ldA = [&](const char* st, int mt) { return *(const bf16x8*)(st + aoff + mt * 1024); };
  auto ldB = [&](const char* st, int nt) { return bfrag2(st + boff + nt * 256 + hlo, st + boff + nt * 256 + hhi); };
  auto extra = [&](int, int, unsigned) {};
  gemm_loop<2, 3, 3, K3_STAGE, 3, 4>(6, lds, acc, ldA, ldB, piece, waitL, extra, bar);
#pragma unroll
  for (int nt = 0; nt < 3; ++nt)
#pragma unroll
    for (int mt = 0; mt < 2; ++mt) {
      size_t off = (size_t)(m0 + wm * 32 + mt * 16 + (lane & 15)) * D_ + n0 + wn * 48 + nt * 16 + 4 * (lane >> 4);
      u32x2 v = {pk2(acc[nt][mt][0], acc[nt][mt][1]), pk2(acc[nt][mt][2], acc[nt][mt][3])};
      *(u32x2*)(out + off) = v;
    }
}

extern "C" void kernel_launch(void* const* d_in, const int* in_sizes, int n_in, void* d_out, int out_size,
                              void* d_ws, size_t ws_size, hipStream_t stream) {
  const u16* x = (const u16*)d_in[0];
  const u16* wqkv = (const u16*)d_in[1];
  const u16* wo = (const u16*)d_in[2];
  const u16* wg = (const u16*)d_in[4];
  const u16* wu = (const u16*)d_in[5];
  const u16* wd = (const u16*)d_in[6];
  const u16* gw = (const u16*)d_in[7];
  char* ws = (char*)d_ws;
  u16* qkvp = (u16*)ws;
  float* gate = (float*)(ws + (size_t)16 * 1024 * 1024);
  u16* Ao = (u16*)(ws + (size_t)17 * 1024 * 1024);
  u16* Ah = (u16*)(ws + (size_t)20 * 1024 * 1024);
  float* part = (float*)(ws + (size_t)48 * 1024 * 1024);
  k_proj<<<256, 512, 0, stream>>>(x, wqkv, wg, wu, gw, qkvp, gate, Ah);
  k_mid<<<256, 512, 0, stream>>>(qkvp, gate, Ao, Ah, wd, part);
  k_out<<<192, 256, 0, stream>>>(Ao, wo, part, (u16*)d_out);
}
```

```cpp
#include <hip/hip_runtime.h>
#include <hip/hip_bf16.h>
#include <type_traits>

typedef __attribute__((ext_vector_type(8))) short bf16x8;
typedef __attribute__((ext_vector_type(4))) short s16x4;
typedef __attribute__((ext_vector_type(4))) float f32x4;
typedef __attribute__((ext_vector_type(2))) float f32x2;
typedef __attribute__((ext_vector_type(16))) float f32x16;
typedef __attribute__((ext_vector_type(4))) unsigned u32x4;
typedef __attribute__((ext_vector_type(2))) unsigned u32x2;
typedef __attribute__((ext_vector_type(2))) __bf16 bf16x2_t;
typedef unsigned short u16;

#define DI __device__ __forceinline__
#define LDSP(T) __attribute__((address_space(3))) T

constexpr int S_ = 2048, D_ = 576, H_ = 32, DH_ = 18, F_ = 4608;

DI unsigned pk2(float lo, float hi) {
  f32x2 v = {lo, hi};
  bf16x2_t b = __builtin_convertvector(v, bf16x2_t);
  return __builtin_bit_cast(unsigned, b);
}
DI float rbf(float x) { return __uint_as_float(pk2(x, 0.f) << 16); }
DI float dot2bf(unsigned a, unsigned b, float c) {
  return __builtin_amdgcn_fdot2_f32_bf16(__builtin_bit_cast(bf16x2_t, a), __builtin_bit_cast(bf16x2_t, b), c, false);
}

DI void glds16(const void* sbase, unsigned voff, unsigned lds_base) {
  unsigned sv;
  asm volatile("s_mov_b32 %0, m0\n\ts_mov_b32 m0, %3\n\ts_nop 0\n\tglobal_load_lds_dwordx4 %1, %2\n\ts_mov_b32 m0, %0"
               : "=&s"(sv) : "v"(voff), "s"(sbase), "s"(lds_base) : "memory");
}
DI unsigned lds_addr(const char* p) { return (unsigned)(size_t)(LDSP(const char)*)p; }
DI s16x4 trread(const char* p) { return __builtin_amdgcn_ds_read_tr16_b64_v4i16((LDSP(s16x4)*)p); }
template <int N> DI void waitv() { asm volatile("s_waitcnt vmcnt(%0)" ::"n"(N) : "memory"); }
#define BARRIER() do { asm volatile("s_waitcnt lgkmcnt(0)" ::: "memory"); __builtin_amdgcn_s_barrier(); asm volatile("" ::: "memory"); } while (0)

#define SBAR() __builtin_amdgcn_sched_barrier(0)
template <int NBs>
DI unsigned bseg_off(int u, int ld) {
  int st = u >> 4, kblk = st / NBs, nblk = st - kblk * NBs;
  int rr = (u >> 1) & 7, hf = u & 1;
  int k = kblk * 8 + (rr ^ ((kblk & 1) << 2));
  return (unsigned)(k * ld + nblk * 16 + hf * 8) * 2u;
}
DI unsigned aseg_off(int u, int lda) {
  int row = u >> 2, ch = (u & 3) ^ ((-(row >> 2)) & 3);
  return (unsigned)(row * lda + ch * 8) * 2u;
}
DI bf16x8 bfrag2(const char* lo, const char* hi) {
  s16x4 a = trread(lo), b = trread(hi);
  return __builtin_shufflevector(a, b, 0, 1, 2, 3, 4, 5, 6, 7);
}

template <int N> using IC = std::integral_constant<int, N>;
template <int MT, int NT, int LMAX, int STAGE, int KS, int NR, class LdA, class LdB, class Piece, class WaitL, class Extra, class Bar>
DI void gemm_loop(int nsup, char* lds, f32x4 (&acc)[NT][MT], LdA ldA, LdB ldB, Piece piece, WaitL waitL, Extra extra, Bar bar) {
  static_assert(NT >= MT + 1 && NT >= LMAX, "schedule");
  constexpr int SUP = KS * STAGE;
  const unsigned l0 = lds_addr(lds);
#pragma unroll
  for (int s = 0; s < (NR - 1) * KS; ++s)
#pragma unroll
    for (int i = 0; i < LMAX; ++i) piece(i, s, l0 + s * STAGE);
  waitL(IC<NR - 2>{});
  bar();
  bf16x8 af[MT], bw0, bw1;
#pragma unroll
  for (int mt = 0; mt < MT; ++mt) af[mt] = ldA(lds, mt);
  bw0 = ldB(lds, 0);
  bw1 = ldB(lds, 1);
  int cb = 0;
  for (int j = 0; j < nsup; ++j) {
    if (j + NR - 2 < nsup) waitL(IC<NR - 3>{}); else waitv<0>();
    bar();
    const int nb_ = cb + 1 == NR ? 0 : cb + 1, pb_ = cb == 0 ? NR - 1 : cb - 1;
    const char* base = lds + cb * SUP;
    const char* nbase = lds + nb_ * SUP;
    const unsigned dst = l0 + pb_ * SUP;
    const bool more = j + NR - 1 < nsup;
#pragma unroll
    for (int ks = 0; ks < KS; ++ks) {
      const char* cur = base + ks * STAGE;
      const char* nx = ks + 1 < KS ? base + (ks + 1) * STAGE : nbase;
      bf16x8 an[MT];
#pragma unroll
      for (int nt = 0; nt < NT; ++nt) {
        bf16x8 b = (nt & 1) ? bw1 : bw0;
        bf16x8 nb = nt + 2 < NT ? ldB(cur, nt + 2) : ldB(nx, nt + 2 - NT);
        if (nt & 1) bw1 = nb; else bw0 = nb;
        if (nt >= NT - 1 - MT && nt < NT - 1) an[nt - (NT - 1 - MT)] = ldA(nx, nt - (NT - 1 - MT));
        if (nt < LMAX && more) piece(nt, (j + NR - 1) * KS + ks, dst + ks * STAGE);
#pragma unroll
        for (int mt = 0; mt < MT; ++mt) acc[nt][mt] = __builtin_amdgcn_mfma_f32_16x16x32_bf16(b, af[mt], acc[nt][mt], 0, 0, 0);
        if (more) extra(nt, (j + NR - 1) * KS + ks, dst + ks * STAGE - l0);
        SBAR();
      }
#pragma unroll
      for (int mt = 0; mt < MT; ++mt) af[mt] = an[mt];
      if (NT & 1) { bf16x8 t = bw0; bw0 = bw1; bw1 = t; }
    }
    cb = nb_;
  }
}

constexpr float QSCALE = 0.23570226039551584f * 1.4426950408889634f;
constexpr int K1_STAGE = 16384 + 9216 + 9216 + 4096;

__global__ __launch_bounds__(512) void k_proj(const u16* __restrict__ x, const u16* __restrict__ wqkv,
                                              const u16* __restrict__ wg, const u16* __restrict__ wu,
                                              const u16* __restrict__ gate_w, u16* __restrict__ qkvp,
                                              float* __restrict__ gate, u16* __restrict__ Ah) {
  __shared__ __attribute__((aligned(16))) char lds[4 * K1_STAGE + 1152];
  const int tid = threadIdx.x, lane = tid & 63;
  const int wv = __builtin_amdgcn_readfirstlane(tid >> 6), wm = wv >> 1, wn = wv & 1;
  const int bid = blockIdx.x;
  const int nt_blk = (bid & 7) * 4 + ((bid >> 3) & 3), mt_blk = bid >> 5;
  const int m0 = mt_blk * 256, f0 = nt_blk * 144;
  const int coff = nt_blk == 31 ? 2 : 0;
  const int c0 = nt_blk * 54 - coff;
  char* gwl = lds + 4 * K1_STAGE;
  if (tid < 72) *(u32x4*)(gwl + tid * 16) = *(const u32x4*)(gate_w + tid * 8);

  f32x4 acc[11][4] = {};
  const u16* xa = x + (size_t)m0 * D_;
  const unsigned aof0 = aseg_off(wv * 64 + lane, D_), aof1 = aseg_off((8 + wv) * 64 + lane, D_);
  unsigned bof[3];
  const u16* bsrc[3];
  int bld[3];
#pragma unroll
  for (int i = 0; i < 3; ++i) {
    int jj = i * 8 + wv;
    if (jj < 18) {
      int j = jj < 9 ? jj : jj - 9;
      bof[i] = bseg_off<9>(j * 64 + lane, F_);
      bsrc[i] = (jj < 9 ? wg : wu) + f0;
      bld[i] = F_;
    } else {
      bof[i] = bseg_off<4>(((jj - 18) & 3) * 64 + lane, 3 * D_);
      bsrc[i] = wqkv + c0;
      bld[i] = 3 * D_;
    }
  }
  const bool padlane = (lane & 1) && (lane >> 4) == 3;
  auto piece = [&](int i, int kt, unsigned buf) {
    if (i == 0) glds16(xa + kt * 32, aof0, buf + wv * 1024);
    else if (i == 1) glds16(xa + kt * 32, aof1, buf + (8 + wv) * 1024);
    else if (i < 4) glds16(bsrc[i - 2] + (size_t)kt * 32 * bld[i - 2], bof[i - 2], buf + 16384 + ((i - 2) * 8 + wv) * 1024);
    else if (wv < 2) glds16(bsrc[2] + (size_t)kt * 32 * bld[2], bof[2], buf + 16384 + (16 + wv) * 1024);
    else if (wv < 6) { if (!padlane) glds16(bsrc[2] + (size_t)kt * 32 * bld[2], bof[2], buf + 16384 + (16 + wv) * 1024); }
  };
  const int gk = lane & 31, gkb = gk >> 3;
  const int gdst = 16384 + 18432 + (gkb * 4 + 3) * 256 + ((gk & 7) ^ ((gkb & 1) << 2)) * 32 + 16;
  auto gfill = [&](int kt, unsigned off) {
    if (wv == 6 && lane < 32) {
      u32x4 v = {(unsigned)*(const u16*)(gwl + (kt * 32 + gk) * 2), 0u, 0u, 0u};
      *(u32x4*)(lds + off + gdst) = v;
    }
  };
  __syncthreads();
  gfill(0, 0); gfill(1, K1_STAGE); gfill(2, 2 * K1_STAGE);
  auto waitL = [&](auto n) { if (wv < 6) waitv<5 * decltype(n)::value>(); else waitv<4 * decltype(n)::value>(); };
  auto bar = [&]() { BARRIER(); };
  const int g = lane >> 4;
  const int bl = ((lane & 15) >> 2) * 32 + (lane & 3) * 8;
  const int aoff = (wm * 64 + (lane & 15)) * 64 + ((g ^ ((-(lane >> 2)) & 3)) << 4);
  const int bgu = 16384 + wn * 9216 + g * 9 * 256 + bl;
  const int bqq = 16384 + 18432 + (g * 4 + wn * 2) * 256 + bl;
  const int hlo = (g & 1) << 7, hhi = hlo ^ 128;
  auto ldA = [&](const char* st, int mt) { return *(const bf16x8*)(st + aoff + mt * 1024); };
  auto ldB = [&](const char* st, int nt) {
    const char* bp = st + (nt < 9 ? bgu + nt * 256 : bqq + (nt - 9) * 256);
    return bfrag2(bp + hlo, bp + hhi);
  };
  auto extra = [&](int nt, int kt, unsigned off) { if (nt == 5) gfill(kt, off); };
  gemm_loop<4, 11, 5, K1_STAGE, 1, 4>(18, lds, acc, ldA, ldB, piece, waitL, extra, bar);
  BARRIER();
  int tz = threadIdx.x;
  asm volatile("" : "+v"(tz));
  const int lz = tz & 63, gz = lz >> 4, tk = lz & 15;

  char* H = lds + 73728;
  float* G = (float*)(lds + 73728 + 256 * 304);
  const int hg0 = nt_blk * 3;
  {
    char* xs = lds + wv * 9216 + lz * 8;
    if (wn == 1) {
#pragma unroll
      for (int nt = 0; nt < 9; ++nt)
#pragma unroll
        for (int m2 = 0; m2 < 2; ++m2) { f32x4 t = acc[nt][m2]; acc[nt][m2] = acc[nt][2 + m2]; acc[nt][2 + m2] = t; }
    }
#pragma unroll
    for (int nt = 0; nt < 9; ++nt)
#pragma unroll
      for (int m2 = 0; m2 < 2; ++m2) {
        f32x4 v = acc[nt][2 + m2];
        *(u32x2*)(xs + (nt * 2 + m2) * 512) = (u32x2){pk2(v[0], v[1]), pk2(v[2], v[3])};
      }
    if (wn == 1 && gz == 2) {
#pragma unroll
      for (int mt = 0; mt < 4; ++mt) {
        float gv = rbf(1.f / (1.f + __expf(-rbf(acc[10][mt][0]))));
        G[wm * 64 + mt * 16 + tk] = gv;
        if (nt_blk == 0) gate[m0 + wm * 64 + mt * 16 + tk] = gv;
      }
    }
  }
  __syncthreads();
  {
    const char* xr = lds + (wv ^ 1) * 9216 + lz * 8;
    u32x2 pv[9][2];
#pragma unroll
    for (int nt = 0; nt < 9; ++nt)
#pragma unroll
      for (int m2 = 0; m2 < 2; ++m2) pv[nt][m2] = *(const u32x2*)(xr + (nt * 2 + m2) * 512);
    float og[2];
#pragma unroll
    for (int m2 = 0; m2 < 2; ++m2) og[m2] = rbf(1.f - G[wm * 64 + (wn * 2 + m2) * 16 + tk]);
    asm volatile("s_waitcnt lgkmcnt(0)" ::: "memory");
    __builtin_amdgcn_s_barrier();
#pragma unroll
    for (int nt = 0; nt < 9; ++nt)
#pragma unroll
      for (int m2 = 0; m2 < 2; ++m2) {
        f32x4 v = acc[nt][m2];
        u32x2 uv = pv[nt][m2];
        float h0 = rbf(v[0]) * __uint_as_float(uv[0] << 16) * og[m2];
        float h1 = rbf(v[1]) * __uint_as_float(uv[0] & 0xffff0000u) * og[m2];
        float h2 = rbf(v[2]) * __uint_as_float(uv[1] << 16) * og[m2];
        float h3 = rbf(v[3]) * __uint_as_float(uv[1] & 0xffff0000u) * og[m2];
        *(u32x2*)(H + (wm * 64 + (wn * 2 + m2) * 16 + tk) * 304 + (nt * 16 + 4 * gz) * 2) = (u32x2){pk2(h0, h1), pk2(h2, h3)};
      }
  }
  {
    char* img = lds;
#pragma unroll
    for (int t2 = 0; t2 < 2; ++t2)
#pragma unroll
      for (int r = 0; r < 4; r += 2) {
        int c = wn * 32 + t2 * 16 + 4 * gz + r - coff;
        if (c >= 0 && c < 54) {
          int hl = c / 18, d = c - hl * 18;
          int which = (hg0 + hl) >> 5;
          float sc = which == 0 ? QSCALE : 1.f;
#pragma unroll
          for (int mt = 0; mt < 4; ++mt) {
            int tok = wm * 64 + mt * 16 + tk;
            unsigned v = pk2(acc[9 + t2][mt][r] * sc, acc[9 + t2][mt][r + 1] * sc);
            if (which == 2) {
              char* q = img + hl * 16384 + (tok >> 5) * 2048 + ((((tok >> 4) & 1) * 2 + ((tok >> 2) & 1)) * 32 + d) * 16 +
                        ((tok & 3) + 4 * ((tok >> 3) & 1)) * 2;
              *(u16*)q = (u16)v;
              *(u16*)(q + 16) = (u16)(v >> 16);
            } else {
              int ch = d >> 3;
              if (which == 1) ch ^= (tok >> 2) & 3;
              *(unsigned*)(img + (hl * 256 + tok) * 64 + ch * 16 + (d & 7) * 2) = v;
            }
          }
        }
      }
    for (int i = tz; i < 768; i += 512) {
      int which = (hg0 + (i >> 8)) >> 5;
      if (which == 2) {
        char* grp = img + (i >> 8) * 16384 + ((i & 255) >> 3) * 512;
        int d0 = 18 + (i & 7) * 2;
        unsigned one = d0 == 18 ? 0x3f803f80u : 0u;
        if (d0 < 32) {
          *(u32x4*)(grp + d0 * 16) = (u32x4){one, one, one, one};
          *(u32x4*)(grp + d0 * 16 + 16) = (u32x4){0u, 0u, 0u, 0u};
        }
      } else {
        int sw = which == 1 ? (i >> 2) & 3 : 0;
        char* row = img + i * 64;
        *(unsigned*)(row + (2 ^ sw) * 16 + 4) = 0u;
        *(u32x2*)(row + (2 ^ sw) * 16 + 8) = (u32x2){0u, 0u};
        *(u32x4*)(row + (3 ^ sw) * 16) = (u32x4){0u, 0u, 0u, 0u};
      }
    }
  }
  __syncthreads();
#pragma unroll
  for (int i = 0; i < 9; ++i) {
    int u = i * 512 + tz;
    int row = u / 18, cu = u - row * 18;
    u32x4 v = *(const u32x4*)(H + row * 304 + cu * 16);
    *(u32x4*)(Ah + (size_t)(m0 + row) * F_ + f0 + cu * 8) = v;
  }
#pragma unroll
  for (int i = 0; i < 6; ++i) {
    int u = i * 512 + tz;
    int hl = u >> 10, rem = u & 1023;
    u32x4 v = *(const u32x4*)(lds + u * 16);
    *(u32x4*)(qkvp + ((size_t)(nt_blk * 3 + hl) * S_ + m0) * 32 + rem * 8) = v;
  }
}

DI void attn_stage(const u16* __restrict__ Kh, const u16* __restrict__ Vh, int c, unsigned buf, int wv, int lane) {
#pragma unroll
  for (int i = 0; i < 2; ++i) glds16(Kh + (size_t)c * 4096, ((i * 4 + wv) * 64 + lane) * 16u, buf + (i * 4 + wv) * 1024);
#pragma unroll
  for (int i = 0; i < 2; ++i)
    glds16(Vh + (size_t)c * 4096, ((i * 4 + wv) * 64 + lane) * 16u, buf + 8192 + (i * 4 + wv) * 1024);
}

DI float halfswap_max(float m) {
  auto rr = __builtin_amdgcn_permlane32_swap(__float_as_uint(m), __float_as_uint(m), false, false);
  return fmaxf(__uint_as_float(rr[0]), __uint_as_float(rr[1]));
}
DI float max16(const f32x16& s) {
  float mx = s[0];
#pragma unroll
  for (int r = 1; r < 16; ++r) mx = fmaxf(mx, s[r]);
  return halfswap_max(mx);
}
#define MFMA32(a, b, c) __builtin_amdgcn_mfma_f32_32x32x16_bf16((a), (b), (c), 0, 0, 0)

DI void exp_pack(const f32x16& s, bf16x8& p0, bf16x8& p1) {
  u32x4 a, b;
#pragma unroll
  for (int r = 0; r < 4; ++r) a[r] = pk2(__builtin_amdgcn_exp2f(s[2 * r]), __builtin_amdgcn_exp2f(s[2 * r + 1]));
#pragma unroll
  for (int r = 0; r < 4; ++r) b[r] = pk2(__builtin_amdgcn_exp2f(s[8 + 2 * r]), __builtin_amdgcn_exp2f(s[9 + 2 * r]));
  p0 = __builtin_bit_cast(bf16x8, a);
  p1 = __builtin_bit_cast(bf16x8, b);
}

#define EX2(x) __builtin_amdgcn_exp2f(x)
#define PHASE(S, PA, PB, M1, M2, M3, M4)                                              \
  do {                                                                                \
    M1; SBAR(); PA[0] = pk2(EX2(S[0]), EX2(S[1])); PA[1] = pk2(EX2(S[2]), EX2(S[3])); SBAR();  \
    M2; SBAR(); PA[2] = pk2(EX2(S[4]), EX2(S[5])); PA[3] = pk2(EX2(S[6]), EX2(S[7])); SBAR();  \
    M3; SBAR(); PB[0] = pk2(EX2(S[8]), EX2(S[9])); PB[1] = pk2(EX2(S[10]), EX2(S[11])); SBAR(); \
    M4; SBAR(); PB[2] = pk2(EX2(S[12]), EX2(S[13])); PB[3] = pk2(EX2(S[14]), EX2(S[15])); SBAR(); \
  } while (0)
#define BC8(x) __builtin_bit_cast(bf16x8, x)
DI void attn_fast(const u16* __restrict__ Kh, const u16* __restrict__ Vh, const bf16x8 (&qf)[2][2], char* lds,
                  f32x16 (&o)[2], int wv, int lane) {
  const int r32 = lane & 31, h = lane >> 5;
  const int koff0 = r32 * 64 + ((h ^ ((r32 >> 2) & 3)) << 4);
  const int koff1 = r32 * 64 + (((2 + h) ^ ((r32 >> 2) & 3)) << 4);
  const int voff = 8192 + lane * 16;
  const unsigned l0 = lds_addr(lds);
  f32x16 negm[2], sa, sb;
#pragma unroll
  for (int a = 0; a < 2; ++a)
#pragma unroll
    for (int r = 0; r < 16; ++r) { o[a][r] = 0.f; negm[a][r] = 0.f; }
  attn_stage(Kh, Vh, 0, l0, wv, lane);
  attn_stage(Kh, Vh, 1, l0 + 16384, wv, lane);
  attn_stage(Kh, Vh, 2, l0 + 32768, wv, lane);
  waitv<8>();
  BARRIER();
  bf16x8 kc0 = *(const bf16x8*)(lds + koff0), kc1 = *(const bf16x8*)(lds + koff1);
  {
#pragma unroll
    for (int a = 0; a < 2; ++a) {
      f32x16 s = MFMA32(kc0, qf[a][0], negm[a]);
      s = MFMA32(kc1, qf[a][1], s);
      float mx = max16(s);
#pragma unroll
      for (int r = 0; r < 16; ++r) { negm[a][r] = -mx; if (a == 0) sa[r] = s[r] - mx; }
    }
  }
  u32x4 pa0, pa1, pb0 = {0u, 0u, 0u, 0u}, pb1 = {0u, 0u, 0u, 0u};
  bf16x8 vp0 = {0, 0, 0, 0, 0, 0, 0, 0}, vp1 = {0, 0, 0, 0, 0, 0, 0, 0};
  for (int c = 0; c < 16; ++c) {
    if (c + 2 < 16) waitv<4>(); else waitv<0>();
    BARRIER();
    if (c + 3 < 16) attn_stage(Kh, Vh, c + 3, l0 + ((c + 3) & 3) * 16384, wv, lane);
    const char* buf = lds + (c & 3) * 16384;
    const char* bufn = lds + ((c + 1) & 3) * 16384;
#pragma unroll
    for (int t = 0; t < 4; ++t) {
      const char* kb = t < 3 ? buf + (t + 1) * 2048 : bufn;
      const char* vb = buf + t * 2048 + voff;
      bf16x8 kn0 = *(const bf16x8*)(kb + koff0), kn1 = *(const bf16x8*)(kb + koff1);
      bf16x8 vc0 = *(const bf16x8*)vb, vc1 = *(const bf16x8*)(vb + 1024);
      SBAR();
      PHASE(sa, pa0, pa1, sb = MFMA32(kc0, qf[1][0], negm[1]), o[1] = MFMA32(vp0, BC8(pb0), o[1]),
            sb = MFMA32(kc1, qf[1][1], sb), o[1] = MFMA32(vp1, BC8(pb1), o[1]));
      PHASE(sb, pb0, pb1, sa = MFMA32(kn0, qf[0][0], negm[0]), o[0] = MFMA32(vc0, BC8(pa0), o[0]),
            sa = MFMA32(kn1, qf[0][1], sa), o[0] = MFMA32(vc1, BC8(pa1), o[0]));
      kc0 = kn0; kc1 = kn1; vp0 = vc0; vp1 = vc1;
    }
  }
  o[1] = MFMA32(vp0, BC8(pb0), o[1]);
  o[1] = MFMA32(vp1, BC8(pb1), o[1]);
}

DI void attn_safe(const u16* __restrict__ Kh, const u16* __restrict__ Vh, const bf16x8 (&qf)[2][2], char* lds,
                  f32x16 (&o)[2], int wv, int lane) {
  const int r32 = lane & 31, h = lane >> 5;
  const int koff0 = r32 * 64 + ((h ^ ((r32 >> 2) & 3)) << 4);
  const int koff1 = r32 * 64 + (((2 + h) ^ ((r32 >> 2) & 3)) << 4);
  const int voff = 8192 + lane * 16;
  const unsigned l0 = lds_addr(lds);
  f32x16 zero;
  float m[2] = {-INFINITY, -INFINITY};
#pragma unroll
  for (int r = 0; r < 16; ++r) { o[0][r] = 0.f; o[1][r] = 0.f; zero[r] = 0.f; }
  for (int c = 0; c < 16; ++c) {
    BARRIER();
    attn_stage(Kh, Vh, c, l0, wv, lane);
    waitv<0>();
    BARRIER();
    for (int t = 0; t < 4; ++t) {
      const char* kb = lds + t * 2048;
      bf16x8 k0 = *(const bf16x8*)(kb + koff0), k1 = *(const bf16x8*)(kb + koff1);
      bf16x8 va = *(const bf16x8*)(kb + voff), vb = *(const bf16x8*)(kb + voff + 1024);
#pragma unroll
      for (int a = 0; a < 2; ++a) {
        f32x16 s = MFMA32(k0, qf[a][0], zero);
        s = MFMA32(k1, qf[a][1], s);
        float mn = fmaxf(m[a], max16(s));
        float al = __builtin_amdgcn_exp2f(m[a] - mn);
        m[a] = mn;
#pragma unroll
        for (int r = 0; r < 16; ++r) { o[a][r] *= al; s[r] -= mn; }
        bf16x8 p0, p1;
        exp_pack(s, p0, p1);
        o[a] = MFMA32(va, p0, o[a]);
        o[a] = MFMA32(vb, p1, o[a]);
      }
    }
  }
}

DI void attn_block(int bid, const u16* __restrict__ qkvp, const float* __restrict__ gate, u16* __restrict__ Ao,
                   char* lds) {
  const int tid = threadIdx.x, lane = tid & 63, r32 = lane & 31, h = lane >> 5;
  const int wv = __builtin_amdgcn_readfirstlane(tid >> 6);
  const int head = (bid & 7) * 4 + ((bid >> 3) & 3), qb = bid >> 5;
  const u16* Qh = qkvp + (size_t)head * (S_ * 32);
  const u16* Kh = qkvp + (size_t)(H_ + head) * (S_ * 32);
  const u16* Vh = qkvp + (size_t)(2 * H_ + head) * (S_ * 32);
  const int qrow0 = qb * 256 + wv * 64 + r32;
  bf16x8 qf[2][2];
#pragma unroll
  for (int a = 0; a < 2; ++a) {
    qf[a][0] = *(const bf16x8*)(Qh + (size_t)(qrow0 + a * 32) * 32 + h * 8);
    qf[a][1] = *(const bf16x8*)(Qh + (size_t)(qrow0 + a * 32) * 32 + 16 + h * 8);
  }
  float gq[2] = {gate[qrow0], gate[qrow0 + 32]};
  asm volatile("" ::"v"(qf[0][0]), "v"(qf[0][1]), "v"(qf[1][0]), "v"(qf[1][1]), "v"(gq[0]), "v"(gq[1]));
  f32x16 o[2];
  attn_fast(Kh, Vh, qf, lds, o, wv, lane);
  bool bad = false;
#pragma unroll
  for (int a = 0; a < 2; ++a)
#pragma unroll
    for (int r = 0; r < 11; ++r) bad |= (r < 8 || h == 0) && !(fabsf(o[a][r]) < 1e30f);
  if (__syncthreads_or(bad)) attn_safe(Kh, Vh, qf, lds, o, wv, lane);
#pragma unroll
  for (int a = 0; a < 2; ++a) {
    float l = __shfl(o[a][10], r32);
    float sc = gq[a] / l;
    u16* dst = Ao + (size_t)(qrow0 + a * 32) * D_ + head * DH_ + h * 4;
    *(unsigned*)(dst + 0) = pk2(o[a][0] * sc, o[a][1] * sc);
    *(unsigned*)(dst + 2) = pk2(o[a][2] * sc, o[a][3] * sc);
    *(unsigned*)(dst + 8) = pk2(o[a][4] * sc, o[a][5] * sc);
    *(unsigned*)(dst + 10) = pk2(o[a][6] * sc, o[a][7] * sc);
    if (h == 0) *(unsigned*)(dst + 16) = pk2(o[a][8] * sc, o[a][9] * sc);
  }
}

constexpr int K2_STAGE = 16384 + 18432;
DI void down_block(int bid, const u16* __restrict__ Ah, const u16* __restrict__ wd, float* __restrict__ part,
                   char* lds) {
  const int tid = threadIdx.x, lane = tid & 63;
  const int wv = __builtin_amdgcn_readfirstlane(tid >> 6);
  const int mt_blk = (bid & 7) * 2 + ((bid >> 3) & 1), rest = bid >> 4;
  const int nt_blk = rest & 3, sp = rest >> 2;
  const int m0 = mt_blk * 128, n0 = nt_blk * 144;
  f32x4 acc[9][2] = {};
  const u16* pa = Ah + (size_t)m0 * F_ + sp * 1152;
  const u16* pb = wd + (size_t)sp * 1152 * D_ + n0;
  const unsigned l0 = lds_addr(lds);
  unsigned aof[4], bof[5];
#pragma unroll
  for (int i = 0; i < 4; ++i) {
    int u = (i * 4 + wv) * 64 + lane, row = u >> 3, ch = (u & 7) ^ (row & 7);
    aof[i] = (unsigned)(row * F_ + ch * 8) * 2u;
  }
#pragma unroll
  for (int i = 0; i < 5; ++i) bof[i] = bseg_off<9>((i < 4 ? i * 4 + wv : 16 + (wv & 1)) * 64 + lane, D_);
  auto piece = [&](int i, int kt, unsigned buf) {
    if (i < 4) glds16(pa + kt * 64, aof[i], buf + (i * 4 + wv) * 1024);
    else if (i < 8) glds16(pb + (size_t)kt * 64 * D_, bof[i - 4], buf + 16384 + ((i - 4) * 4 + wv) * 1024);
    else if (wv < 2) glds16(pb + (size_t)kt * 64 * D_, bof[4], buf + 16384 + (16 + wv) * 1024);
  };
  const int g = lane >> 4;
  const int aoff0 = (wv * 32 + (lane & 15)) * 128 + ((g ^ (lane & 7)) << 4), aoff1 = aoff0 ^ 64;
  const int boff = 16384 + g * 9 * 256 + ((lane & 15) >> 2) * 32 + (lane & 3) * 8;
  const int hlo = (g & 1) << 7, hhi = hlo ^ 128;
#pragma unroll
  for (int i = 0; i < 9; ++i) piece(i, 0, l0);
  for (int kt = 0; kt < 18; ++kt) {
    waitv<0>();
    BARRIER();
    const char* cur = lds + (kt & 1) * K2_STAGE;
    const unsigned dst = l0 + ((kt + 1) & 1) * K2_STAGE;
    const bool more = kt + 1 < 18;
    bf16x8 af[2][2];
#pragma unroll
    for (int mt = 0; mt < 2; ++mt) {
      af[0][mt] = *(const bf16x8*)(cur + aoff0 + mt * 2048);
      af[1][mt] = *(const bf16x8*)(cur + aoff1 + mt * 2048);
    }
    bf16x8 bw0 = bfrag2(cur + boff + hlo, cur + boff + hhi);
    bf16x8 bw1 = bfrag2(cur + boff + 256 + hlo, cur + boff + 256 + hhi);
#pragma unroll
    for (int q = 0; q < 18; ++q) {
      bf16x8 b = (q & 1) ? bw1 : bw0;
      if (q + 2 < 18) {
        const int q2 = q + 2, o2 = ((q2 / 9) * 36 + q2 % 9) * 256;
        bf16x8 nb = bfrag2(cur + boff + o2 + hlo, cur + boff + o2 + hhi);
        if (q & 1) bw1 = nb; else bw0 = nb;
      }
      if (q < 9 && more) piece(q, kt + 1, dst);
#pragma unroll
      for (int mt = 0; mt < 2; ++mt)
        acc[q % 9][mt] = __builtin_amdgcn_mfma_f32_16x16x32_bf16(b, af[q / 9][mt], acc[q % 9][mt], 0, 0, 0);
      SBAR();
    }
  }
  float* dst = part + (size_t)sp * (S_ * D_);
#pragma unroll
  for (int nt = 0; nt < 9; ++nt)
#pragma unroll
    for (int mt = 0; mt < 2; ++mt) {
      int tok = m0 + wv * 32 + mt * 16 + (lane & 15);
      int n = n0 + nt * 16 + 4 * (lane >> 4);
      *(f32x4*)(dst + (size_t)tok * D_ + n) = acc[nt][mt];
    }
}

__global__ __launch_bounds__(256, 2) void k_mid(const u16* __restrict__ qkvp, const float* __restrict__ gate,
                                                u16* __restrict__ Ao, const u16* __restrict__ Ah,
                                                const u16* __restrict__ wd, float* __restrict__ part) {
  __shared__ __attribute__((aligned(16))) char lds[2 * K2_STAGE];
  const int bid = blockIdx.x;
  if (bid < 256) attn_block(bid, qkvp, gate, Ao, lds);
  else down_block(bid - 256, Ah, wd, part, lds);
}

constexpr int K3_STAGE = 4096 + 6144;
__global__ __launch_bounds__(256) void k_out(const u16* __restrict__ Ao, const u16* __restrict__ wo,
                                             const float* __restrict__ part, u16* __restrict__ out) {
  __shared__ __attribute__((aligned(16))) char lds[12 * K3_STAGE];
  const int tid = threadIdx.x, lane = tid & 63;
  const int wv = __builtin_amdgcn_readfirstlane(tid >> 6), wm = wv >> 1, wn = wv & 1;
  const int nt_blk = blockIdx.x % 6, mt_blk = blockIdx.x / 6;
  const int m0 = mt_blk * 64, n0 = nt_blk * 96;
  f32x4 acc[3][2];
#pragma unroll
  for (int nt = 0; nt < 3; ++nt)
#pragma unroll
    for (int mt = 0; mt < 2; ++mt) {
      size_t off = (size_t)(m0 + wm * 32 + mt * 16 + (lane & 15)) * D_ + n0 + wn * 48 + nt * 16 + 4 * (lane >> 4);
      f32x4 s = *(const f32x4*)(part + off);
#pragma unroll
      for (int sp = 1; sp < 4; ++sp) s += *(const f32x4*)(part + (size_t)sp * (S_ * D_) + off);
      acc[nt][mt] = s;
    }
  const u16* pa = Ao + (size_t)m0 * D_;
  const u16* pb = wo + n0;
  const unsigned aof0 = aseg_off(wv * 64 + lane, D_);
  const unsigned bof0 = bseg_off<6>(wv * 64 + lane, D_), bof1 = bseg_off<6>((4 + (wv & 1)) * 64 + lane, D_);
  auto piece = [&](int i, int kt, unsigned buf) {
    if (i == 0) glds16(pa + kt * 32, aof0, buf + wv * 1024);
    else if (i == 1) glds16(pb + (size_t)kt * 32 * D_, bof0, buf + 4096 + wv * 1024);
    else if (wv < 2) glds16(pb + (size_t)kt * 32 * D_, bof1, buf + 4096 + (4 + wv) * 1024);
  };
  auto waitL = [&](auto n) { if (wv < 2) waitv<9 * decltype(n)::value>(); else waitv<6 * decltype(n)::value>(); };
  auto bar = [&]() { BARRIER(); };
  const int g = lane >> 4;
  const int aoff = (wm * 32 + (lane & 15)) * 64 + ((g ^ ((-(lane >> 2)) & 3)) << 4);
  const int boff = 4096 + (g * 6 + wn * 3) * 256 + ((lane & 15) >> 2) * 32 + (lane & 3) * 8;
  const int hlo = (g & 1) << 7, hhi = hlo ^ 128;
  auto ldA = [&](const char* st, int mt) { return *(const bf16x8*)(st + aoff + mt * 1024); };
  auto ldB = [&](const char* st, int nt) { return bfrag2(st + boff + nt * 256 + hlo, st + boff + nt * 256 + hhi); };
  auto extra = [&](int, int, unsigned) {};
  gemm_loop<2, 3, 3, K3_STAGE, 3, 4>(6, lds, acc, ldA, ldB, piece, waitL, extra, bar);
#pragma unroll
  for (int nt = 0; nt < 3; ++nt)
#pragma unroll
    for (int mt = 0; mt < 2; ++mt) {
      size_t off = (size_t)(m0 + wm * 32 + mt * 16 + (lane & 15)) * D_ + n0 + wn * 48 + nt * 16 + 4 * (lane >> 4);
      u32x2 v = {pk2(acc[nt][mt][0], acc[nt][mt][1]), pk2(acc[nt][mt][2], acc[nt][mt][3])};
      *(u32x2*)(out + off) = v;
    }
}

extern "C" void kernel_launch(void* const* d_in, const int* in_sizes, int n_in, void* d_out, int out_size,
                              void* d_ws, size_t ws_size, hipStream_t stream) {
  const u16* x = (const u16*)d_in[0];
  const u16* wqkv = (const u16*)d_in[1];
  const u16* wo = (const u16*)d_in[2];
  const u16* wg = (const u16*)d_in[4];
  const u16* wu = (const u16*)d_in[5];
  const u16* wd = (const u16*)d_in[6];
  const u16* gw = (const u16*)d_in[7];
  char* ws = (char*)d_ws;
  u16* qkvp = (u16*)ws;
  float* gate = (float*)(ws + (size_t)16 * 1024 * 1024);
  u16* Ao = (u16*)(ws + (size_t)17 * 1024 * 1024);
  u16* Ah = (u16*)(ws + (size_t)20 * 1024 * 1024);
  float* part = (float*)(ws + (size_t)48 * 1024 * 1024);
  k_proj<<<256, 512, 0, stream>>>(x, wqkv, wg, wu, gw, qkvp, gate, Ah);
  k_mid<<<512, 256, 0, stream>>>(qkvp, gate, Ao, Ah, wd, part);
  k_out<<<192, 256, 0, stream>>>(Ao, wo, part, (u16*)d_out);
}
```

```cpp
#include <hip/hip_runtime.h>
#include <hip/hip_bf16.h>
#include <type_traits>

typedef __attribute__((ext_vector_type(8))) short bf16x8;
typedef __attribute__((ext_vector_type(4))) short s16x4;
typedef __attribute__((ext_vector_type(4))) float f32x4;
typedef __attribute__((ext_vector_type(2))) float f32x2;
typedef __attribute__((ext_vector_type(16))) float f32x16;
typedef __attribute__((ext_vector_type(4))) unsigned u32x4;
typedef __attribute__((ext_vector_type(2))) unsigned u32x2;
typedef __attribute__((ext_vector_type(2))) __bf16 bf16x2_t;
typedef unsigned short u16;

#define DI __device__ __forceinline__
#define LDSP(T) __attribute__((address_space(3))) T

constexpr int S_ = 2048, D_ = 576, H_ = 32, DH_ = 18, F_ = 4608;

DI unsigned pk2(float lo, float hi) {
  f32x2 v = {lo, hi};
  bf16x2_t b = __builtin_convertvector(v, bf16x2_t);
  return __builtin_bit_cast(unsigned, b);
}
DI float rbf(float x) { return __uint_as_float(pk2(x, 0.f) << 16); }
DI float dot2bf(unsigned a, unsigned b, float c) {
  return __builtin_amdgcn_fdot2_f32_bf16(__builtin_bit_cast(bf16x2_t, a), __builtin_bit_cast(bf16x2_t, b), c, false);
}

DI void glds16(const void* sbase, unsigned voff, unsigned lds_base) {
  unsigned sv;
  asm volatile("s_mov_b32 %0, m0\n\ts_mov_b32 m0, %3\n\ts_nop 0\n\tglobal_load_lds_dwordx4 %1, %2\n\ts_mov_b32 m0, %0"
               : "=&s"(sv) : "v"(voff), "s"(sbase), "s"(__builtin_amdgcn_readfirstlane(lds_base)) : "memory");
}
DI unsigned lds_addr(const char* p) { return (unsigned)(size_t)(LDSP(const char)*)p; }
DI s16x4 trread(const char* p) { return __builtin_amdgcn_ds_read_tr16_b64_v4i16((LDSP(s16x4)*)p); }
template <int N> DI void waitv() { asm volatile("s_waitcnt vmcnt(%0)" ::"n"(N) : "memory"); }
#define BARRIER() do { asm volatile("s_waitcnt lgkmcnt(0)" ::: "memory"); __builtin_amdgcn_s_barrier(); asm volatile("" ::: "memory"); } while (0)

#define SBAR() __builtin_amdgcn_sched_barrier(0)
template <int NBs>
DI unsigned bseg_off(int u, int ld) {
  int st = u >> 4, kblk = st / NBs, nblk = st - kblk * NBs;
  int rr = (u >> 1) & 7, hf = u & 1;
  int k = kblk * 8 + (rr ^ ((kblk & 1) << 2));
  return (unsigned)(k * ld + nblk * 16 + hf * 8) * 2u;
}
DI unsigned aseg_off(int u, int lda) {
  int row = u >> 2, ch = (u & 3) ^ ((-(row >> 2)) & 3);
  return (unsigned)(row * lda + ch * 8) * 2u;
}
DI bf16x8 bfrag2(const char* lo, const char* hi) {
  s16x4 a = trread(lo), b = trread(hi);
  return __builtin_shufflevector(a, b, 0, 1, 2, 3, 4, 5, 6, 7);
}

template <int N> using IC = std::integral_constant<int, N>;
template <int MT, int NT, int LMAX, int STAGE, int KS, int NR, class LdA, class LdB, class Piece, class WaitL, class Extra, class Bar>
DI void gemm_loop(int nsup, char* lds, f32x4 (&acc)[NT][MT], LdA ldA, LdB ldB, Piece piece, WaitL waitL, Extra extra, Bar bar) {
  static_assert(NT >= MT + 1 && NT >= LMAX, "schedule");
  constexpr int SUP = KS * STAGE;
  const unsigned l0 = lds_addr(lds);
#pragma unroll
  for (int s = 0; s < (NR - 1) * KS; ++s)
#pragma unroll
    for (int i = 0; i < LMAX; ++i) piece(i, s, l0 + s * STAGE);
  waitL(IC<NR - 2>{});
  bar();
  bf16x8 af[MT], bw0, bw1;
#pragma unroll
  for (int mt = 0; mt < MT; ++mt) af[mt] = ldA(lds, mt);
  bw0 = ldB(lds, 0);
  bw1 = ldB(lds, 1);
  int cb = 0;
  auto body = [&](int j, auto wn_, auto more_) {
    constexpr bool more = decltype(more_)::value;
    waitL(wn_);
    bar();
    const int nb_ = cb + 1 == NR ? 0 : cb + 1, pb_ = cb == 0 ? NR - 1 : cb - 1;
    const char* base = lds + cb * SUP;
    const char* nbase = lds + nb_ * SUP;
    const unsigned dst = l0 + pb_ * SUP;
#pragma unroll
    for (int ks = 0; ks < KS; ++ks) {
      const char* cur = base + ks * STAGE;
      const char* nx = ks + 1 < KS ? base + (ks + 1) * STAGE : nbase;
      bf16x8 an[MT];
#pragma unroll
      for (int nt = 0; nt < NT; ++nt) {
        bf16x8 b = (nt & 1) ? bw1 : bw0;
        bf16x8 nb = nt + 2 < NT ? ldB(cur, nt + 2) : ldB(nx, nt + 2 - NT);
        if (nt & 1) bw1 = nb; else bw0 = nb;
        if (nt >= NT - 1 - MT && nt < NT - 1) an[nt - (NT - 1 - MT)] = ldA(nx, nt - (NT - 1 - MT));
        if (nt < LMAX && more) piece(nt, (j + NR - 1) * KS + ks, dst + ks * STAGE);
#pragma unroll
        for (int mt = 0; mt < MT; ++mt) acc[nt][mt] = __builtin_amdgcn_mfma_f32_16x16x32_bf16(b, af[mt], acc[nt][mt], 0, 0, 0);
        if (more) extra(nt, (j + NR - 1) * KS + ks, dst + ks * STAGE - l0);
        SBAR();
      }
#pragma unroll
      for (int mt = 0; mt < MT; ++mt) af[mt] = an[mt];
      if (NT & 1) { bf16x8 t = bw0; bw0 = bw1; bw1 = t; }
    }
    cb = nb_;
  };
  for (int j = 0; j < nsup - (NR - 1); ++j) body(j, IC<NR - 3>{}, IC<1>{});
#pragma unroll 1
  for (int j = nsup - (NR - 1); j < nsup; ++j) body(j, IC<0>{}, IC<0>{});
}

constexpr float QSCALE = 0.23570226039551584f * 1.4426950408889634f;
constexpr int K1_STAGE = 16384 + 9216 + 9216 + 4096;

__global__ __launch_bounds__(512) void k_proj(const u16* __restrict__ x, const u16* __restrict__ wqkv,
                                              const u16* __restrict__ wg, const u16* __restrict__ wu,
                                              const u16* __restrict__ gate_w, u16* __restrict__ qkvp,
                                              float* __restrict__ gate, u16* __restrict__ Ah) {
  __shared__ __attribute__((aligned(16))) char lds[4 * K1_STAGE + 1152];
  const int tid = threadIdx.x, lane = tid & 63;
  const int wv = __builtin_amdgcn_readfirstlane(tid >> 6), wm = wv >> 1, wn = wv & 1;
  const int bid = blockIdx.x;
  const int nt_blk = (bid & 7) * 4 + ((bid >> 3) & 3), mt_blk = bid >> 5;
  const int m0 = mt_blk * 256, f0 = nt_blk * 144;
  const int coff = nt_blk == 31 ? 2 : 0;
  const int c0 = nt_blk * 54 - coff;
  char* gwl = lds + 4 * K1_STAGE;
  if (tid < 72) *(u32x4*)(gwl + tid * 16) = *(const u32x4*)(gate_w + tid * 8);

  f32x4 acc[11][4] = {};
  const u16* xa = x + (size_t)m0 * D_;
  const unsigned aof0 = aseg_off(wv * 64 + lane, D_), aof1 = aseg_off((8 + wv) * 64 + lane, D_);
  unsigned bof[3];
  const u16* bsrc[3];
  int bld[3];
#pragma unroll
  for (int i = 0; i < 3; ++i) {
    int jj = i * 8 + wv;
    if (jj >= 22) jj -= 6;
    if (jj < 18) {
      int j = jj < 9 ? jj : jj - 9;
      bof[i] = bseg_off<9>(j * 64 + lane, F_);
      bsrc[i] = (jj < 9 ? wg : wu) + f0;
      bld[i] = F_;
    } else {
      bof[i] = bseg_off<4>(((jj - 18) & 3) * 64 + lane, 3 * D_);
      bsrc[i] = wqkv + c0;
      bld[i] = 3 * D_;
    }
  }
  const bool padlane = wv >= 2 && wv < 6 && (lane & 1) && (lane >> 4) == 3;
  auto piece = [&](int i, int kt, unsigned buf) {
    if (i == 0) glds16(xa + kt * 32, aof0, buf + wv * 1024);
    else if (i == 1) glds16(xa + kt * 32, aof1, buf + (8 + wv) * 1024);
    else if (i < 4) glds16(bsrc[i - 2] + (size_t)kt * 32 * bld[i - 2], bof[i - 2], buf + 16384 + ((i - 2) * 8 + wv) * 1024);
    else if (!padlane) glds16(bsrc[2] + (size_t)kt * 32 * bld[2], bof[2], buf + 16384 + (16 + (wv < 6 ? wv : wv - 6)) * 1024);
  };
  const int gk = lane & 31, gkb = gk >> 3;
  const int gdst = 16384 + 18432 + (gkb * 4 + 3) * 256 + ((gk & 7) ^ ((gkb & 1) << 2)) * 32 + 16;
  auto gfill = [&](int kt, unsigned off) {
    if (wv == 6 && lane < 32) {
      u32x4 v = {(unsigned)*(const u16*)(gwl + (kt * 32 + gk) * 2), 0u, 0u, 0u};
      *(u32x4*)(lds + off + gdst) = v;
    }
  };
  __syncthreads();
  gfill(0, 0); gfill(1, K1_STAGE); gfill(2, 2 * K1_STAGE);
  auto waitL = [&](auto n) { waitv<5 * decltype(n)::value>(); };
  auto bar = [&]() { BARRIER(); };
  const int g = lane >> 4;
  const int bl = ((lane & 15) >> 2) * 32 + (lane & 3) * 8;
  const int aoff = (wm * 64 + (lane & 15)) * 64 + ((g ^ ((-(lane >> 2)) & 3)) << 4);
  const int bgu = 16384 + wn * 9216 + g * 9 * 256 + bl;
  const int bqq = 16384 + 18432 + (g * 4 + wn * 2) * 256 + bl;
  const int hlo = (g & 1) << 7, hhi = hlo ^ 128;
  auto ldA = [&](const char* st, int mt) { return *(const bf16x8*)(st + aoff + mt * 1024); };
  auto ldB = [&](const char* st, int nt) {
    const char* bp = st + (nt < 9 ? bgu + nt * 256 : bqq + (nt - 9) * 256);
    return bfrag2(bp + hlo, bp + hhi);
  };
  auto extra = [&](int nt, int kt, unsigned off) { if (nt == 5) gfill(kt, off); };
  gemm_loop<4, 11, 5, K1_STAGE, 1, 4>(18, lds, acc, ldA, ldB, piece, waitL, extra, bar);
  BARRIER();
  int tz = threadIdx.x;
  asm volatile("" : "+v"(tz));
  const int lz = tz & 63, gz = lz >> 4, tk = lz & 15;

  char* H = lds + 73728;
  float* G = (float*)(lds + 73728 + 256 * 304);
  const int hg0 = nt_blk * 3;
  {
    char* xs = lds + wv * 9216 + lz * 8;
    if (wn == 1) {
#pragma unroll
      for (int nt = 0; nt < 9; ++nt)
#pragma unroll
        for (int m2 = 0; m2 < 2; ++m2) { f32x4 t = acc[nt][m2]; acc[nt][m2] = acc[nt][2 + m2]; acc[nt][2 + m2] = t; }
    }
#pragma unroll
    for (int nt = 0; nt < 9; ++nt)
#pragma unroll
      for (int m2 = 0; m2 < 2; ++m2) {
        f32x4 v = acc[nt][2 + m2];
        *(u32x2*)(xs + (nt * 2 + m2) * 512) = (u32x2){pk2(v[0], v[1]), pk2(v[2], v[3])};
      }
    if (wn == 1 && gz == 2) {
#pragma unroll
      for (int mt = 0; mt < 4; ++mt) {
        float gv = rbf(1.f / (1.f + __expf(-rbf(acc[10][mt][0]))));
        G[wm * 64 + mt * 16 + tk] = gv;
        if (nt_blk == 0) gate[m0 + wm * 64 + mt * 16 + tk] = gv;
      }
    }
  }
  __syncthreads();
  {
    const char* xr = lds + (wv ^ 1) * 9216 + lz * 8;
    u32x2 pv[9][2];
#pragma unroll
    for (int nt = 0; nt < 9; ++nt)
#pragma unroll
      for (int m2 = 0; m2 < 2; ++m2) pv[nt][m2] = *(const u32x2*)(xr + (nt * 2 + m2) * 512);
    float og[2];
#pragma unroll
    for (int m2 = 0; m2 < 2; ++m2) og[m2] = rbf(1.f - G[wm * 64 + (wn * 2 + m2) * 16 + tk]);
    asm volatile("s_waitcnt lgkmcnt(0)" ::: "memory");
    __builtin_amdgcn_s_barrier();
#pragma unroll
    for (int nt = 0; nt < 9; ++nt)
#pragma unroll
      for (int m2 = 0; m2 < 2; ++m2) {
        f32x4 v = acc[nt][m2];
        u32x2 uv = pv[nt][m2];
        float h0 = v[0] * og[m2] * __uint_as_float(uv[0] << 16);
        float h1 = v[1] * og[m2] * __uint_as_float(uv[0] & 0xffff0000u);
        float h2 = v[2] * og[m2] * __uint_as_float(uv[1] << 16);
        float h3 = v[3] * og[m2] * __uint_as_float(uv[1] & 0xffff0000u);
        *(u32x2*)(H + (wm * 64 + (wn * 2 + m2) * 16 + tk) * 304 + (nt * 16 + 4 * gz) * 2) = (u32x2){pk2(h0, h1), pk2(h2, h3)};
      }
  }
  {
    char* img = lds;
#pragma unroll
    for (int t2 = 0; t2 < 2; ++t2)
#pragma unroll
      for (int r = 0; r < 4; r += 2) {
        int c = wn * 32 + t2 * 16 + 4 * gz + r - coff;
        if (c >= 0 && c < 54) {
          int hl = c / 18, d = c - hl * 18;
          int which = (hg0 + hl) >> 5;
          float sc = which == 0 ? QSCALE : 1.f;
#pragma unroll
          for (int mt = 0; mt < 4; ++mt) {
            int tok = wm * 64 + mt * 16 + tk;
            unsigned v = pk2(acc[9 + t2][mt][r] * sc, acc[9 + t2][mt][r + 1] * sc);
            if (which == 2) {
              char* q = img + hl * 16384 + (tok >> 5) * 2048 + ((((tok >> 4) & 1) * 2 + ((tok >> 2) & 1)) * 32 + d) * 16 +
                        ((tok & 3) + 4 * ((tok >> 3) & 1)) * 2;
              *(u16*)q = (u16)v;
              *(u16*)(q + 16) = (u16)(v >> 16);
            } else {
              int ch = d >> 3;
              if (which == 1) ch ^= (tok >> 2) & 3;
              *(unsigned*)(img + (hl * 256 + tok) * 64 + ch * 16 + (d & 7) * 2) = v;
            }
          }
        }
      }
    for (int i = tz; i < 768; i += 512) {
      int which = (hg0 + (i >> 8)) >> 5;
      if (which == 2) {
        char* grp = img + (i >> 8) * 16384 + ((i & 255) >> 3) * 512;
        int d0 = 18 + (i & 7) * 2;
        unsigned one = d0 == 18 ? 0x3f803f80u : 0u;
        if (d0 < 32) {
          *(u32x4*)(grp + d0 * 16) = (u32x4){one, one, one, one};
          *(u32x4*)(grp + d0 * 16 + 16) = (u32x4){0u, 0u, 0u, 0u};
        }
      } else {
        int sw = which == 1 ? (i >> 2) & 3 : 0;
        char* row = img + i * 64;
        *(unsigned*)(row + (2 ^ sw) * 16 + 4) = 0u;
        *(u32x2*)(row + (2 ^ sw) * 16 + 8) = (u32x2){0u, 0u};
        *(u32x4*)(row + (3 ^ sw) * 16) = (u32x4){0u, 0u, 0u, 0u};
      }
    }
  }
  __syncthreads();
#pragma unroll
  for (int i = 0; i < 9; ++i) {
    int u = i * 512 + tz;
    int row = u / 18, cu = u - row * 18;
    u32x4 v = *(const u32x4*)(H + row * 304 + cu * 16);
    *(u32x4*)(Ah + (size_t)(m0 + row) * F_ + f0 + cu * 8) = v;
  }
#pragma unroll
  for (int i = 0; i < 6; ++i) {
    int u = i * 512 + tz;
    int hl = u >> 10, rem = u & 1023;
    u32x4 v = *(const u32x4*)(lds + u * 16);
    *(u32x4*)(qkvp + ((size_t)(nt_blk * 3 + hl) * S_ + m0) * 32 + rem * 8) = v;
  }
}

DI void attn_stage(const u16* __restrict__ Kh, const u16* __restrict__ Vh, int c, unsigned buf, int wv, int lane) {
#pragma unroll
  for (int i = 0; i < 2; ++i) glds16(Kh + (size_t)c * 4096, ((i * 4 + wv) * 64 + lane) * 16u, buf + (i * 4 + wv) * 1024);
#pragma unroll
  for (int i = 0; i < 2; ++i)
    glds16(Vh + (size_t)c * 4096, ((i * 4 + wv) * 64 + lane) * 16u, buf + 8192 + (i * 4 + wv) * 1024);
}

DI float halfswap_max(float m) {
  auto rr = __builtin_amdgcn_permlane32_swap(__float_as_uint(m), __float_as_uint(m), false, false);
  return fmaxf(__uint_as_float(rr[0]), __uint_as_float(rr[1]));
}
DI float max16(const f32x16& s) {
  float mx = s[0];
#pragma unroll
  for (int r = 1; r < 16; ++r) mx = fmaxf(mx, s[r]);
  return halfswap_max(mx);
}
#define MFMA32(a, b, c) __builtin_amdgcn_mfma_f32_32x32x16_bf16((a), (b), (c), 0, 0, 0)

DI void exp_pack(const f32x16& s, bf16x8& p0, bf16x8& p1) {
  u32x4 a, b;
#pragma unroll
  for (int r = 0; r < 4; ++r) a[r] = pk2(__builtin_amdgcn_exp2f(s[2 * r]), __builtin_amdgcn_exp2f(s[2 * r + 1]));
#pragma unroll
  for (int r = 0; r < 4; ++r) b[r] = pk2(__builtin_amdgcn_exp2f(s[8 + 2 * r]), __builtin_amdgcn_exp2f(s[9 + 2 * r]));
  p0 = __builtin_bit_cast(bf16x8, a);
  p1 = __builtin_bit_cast(bf16x8, b);
}

#define EX2(x) __builtin_amdgcn_exp2f(x)
#define PHASE(S, PA, PB, M1, M2, M3, M4)                                              \
  do {                                                                                \
    M1; SBAR(); PA[0] = pk2(EX2(S[0]), EX2(S[1])); PA[1] = pk2(EX2(S[2]), EX2(S[3])); SBAR();  \
    M2; SBAR(); PA[2] = pk2(EX2(S[4]), EX2(S[5])); PA[3] = pk2(EX2(S[6]), EX2(S[7])); SBAR();  \
    M3; SBAR(); PB[0] = pk2(EX2(S[8]), EX2(S[9])); PB[1] = pk2(EX2(S[10]), EX2(S[11])); SBAR(); \
    M4; SBAR(); PB[2] = pk2(EX2(S[12]), EX2(S[13])); PB[3] = pk2(EX2(S[14]), EX2(S[15])); SBAR(); \
  } while (0)
#define BC8(x) __builtin_bit_cast(bf16x8, x)
DI void attn_fast(const u16* __restrict__ Kh, const u16* __restrict__ Vh, const bf16x8 (&qf)[2][2], char* lds,
                  f32x16 (&o)[2], int wv, int lane) {
  const int r32 = lane & 31, h = lane >> 5;
  const int koff0 = r32 * 64 + ((h ^ ((r32 >> 2) & 3)) << 4);
  const int koff1 = r32 * 64 + (((2 + h) ^ ((r32 >> 2) & 3)) << 4);
  const int voff = 8192 + lane * 16;
  const unsigned l0 = lds_addr(lds);
  f32x16 negm[2], sa, sb;
#pragma unroll
  for (int a = 0; a < 2; ++a)
#pragma unroll
    for (int r = 0; r < 16; ++r) { o[a][r] = 0.f; negm[a][r] = 0.f; }
  attn_stage(Kh, Vh, 0, l0, wv, lane);
  attn_stage(Kh, Vh, 1, l0 + 16384, wv, lane);
  attn_stage(Kh, Vh, 2, l0 + 32768, wv, lane);
  waitv<8>();
  BARRIER();
  bf16x8 kc0 = *(const bf16x8*)(lds + koff0), kc1 = *(const bf16x8*)(lds + koff1);
  {
#pragma unroll
    for (int a = 0; a < 2; ++a) {
      f32x16 s = MFMA32(kc0, qf[a][0], negm[a]);
      s = MFMA32(kc1, qf[a][1], s);
      float mx = max16(s);
#pragma unroll
      for (int r = 0; r < 16; ++r) { negm[a][r] = -mx; if (a == 0) sa[r] = s[r] - mx; }
    }
  }
  u32x4 pa0, pa1, pb0 = {0u, 0u, 0u, 0u}, pb1 = {0u, 0u, 0u, 0u};
  bf16x8 vp0 = {0, 0, 0, 0, 0, 0, 0, 0}, vp1 = {0, 0, 0, 0, 0, 0, 0, 0};
  for (int c = 0; c < 16; ++c) {
    if (c + 2 < 16) waitv<4>(); else waitv<0>();
    BARRIER();
    if (c + 3 < 16) attn_stage(Kh, Vh, c + 3, l0 + ((c + 3) & 3) * 16384, wv, lane);
    const char* buf = lds + (c & 3) * 16384;
    const char* bufn = lds + ((c + 1) & 3) * 16384;
#pragma unroll
    for (int t = 0; t < 4; ++t) {
      const char* kb = t < 3 ? buf + (t + 1) * 2048 : bufn;
      const char* vb = buf + t * 2048 + voff;
      bf16x8 kn0 = *(const bf16x8*)(kb + koff0), kn1 = *(const bf16x8*)(kb + koff1);
      bf16x8 vc0 = *(const bf16x8*)vb, vc1 = *(const bf16x8*)(vb + 1024);
      SBAR();
      PHASE(sa, pa0, pa1, sb = MFMA32(kc0, qf[1][0], negm[1]), o[1] = MFMA32(vp0, BC8(pb0), o[1]),
            sb = MFMA32(kc1, qf[1][1], sb), o[1] = MFMA32(vp1, BC8(pb1), o[1]));
      PHASE(sb, pb0, pb1, sa = MFMA32(kn0, qf[0][0], negm[0]), o[0] = MFMA32(vc0, BC8(pa0), o[0]),
            sa = MFMA32(kn1, qf[0][1], sa), o[0] = MFMA32(vc1, BC8(pa1), o[0]));
      kc0 = kn0; kc1 = kn1; vp0 = vc0; vp1 = vc1;
    }
  }
  o[1] = MFMA32(vp0, BC8(pb0), o[1]);
  o[1] = MFMA32(vp1, BC8(pb1), o[1]);
}

DI void attn_safe(const u16* __restrict__ Kh, const u16* __restrict__ Vh, const bf16x8 (&qf)[2][2], char* lds,
                  f32x16 (&o)[2], int wv, int lane) {
  const int r32 = lane & 31, h = lane >> 5;
  const int koff0 = r32 * 64 + ((h ^ ((r32 >> 2) & 3)) << 4);
  const int koff1 = r32 * 64 + (((2 + h) ^ ((r32 >> 2) & 3)) << 4);
  const int voff = 8192 + lane * 16;
  const unsigned l0 = lds_addr(lds);
  f32x16 zero;
  float m[2] = {-INFINITY, -INFINITY};
#pragma unroll
  for (int r = 0; r < 16; ++r) { o[0][r] = 0.f; o[1][r] = 0.f; zero[r] = 0.f; }
  for (int c = 0; c < 16; ++c) {
    BARRIER();
    attn_stage(Kh, Vh, c, l0, wv, lane);
    waitv<0>();
    BARRIER();
    for (int t = 0; t < 4; ++t) {
      const char* kb = lds + t * 2048;
      bf16x8 k0 = *(const bf16x8*)(kb + koff0), k1 = *(const bf16x8*)(kb + koff1);
      bf16x8 va = *(const bf16x8*)(kb + voff), vb = *(const bf16x8*)(kb + voff + 1024);
#pragma unroll
      for (int a = 0; a < 2; ++a) {
        f32x16 s = MFMA32(k0, qf[a][0], zero);
        s = MFMA32(k1, qf[a][1], s);
        float mn = fmaxf(m[a], max16(s));
        float al = __builtin_amdgcn_exp2f(m[a] - mn);
        m[a] = mn;
#pragma unroll
        for (int r = 0; r < 16; ++r) { o[a][r] *= al; s[r] -= mn; }
        bf16x8 p0, p1;
        exp_pack(s, p0, p1);
        o[a] = MFMA32(va, p0, o[a]);
        o[a] = MFMA32(vb, p1, o[a]);
      }
    }
  }
}

DI void attn_block(int bid, const u16* __restrict__ qkvp, const float* __restrict__ gate, u16* __restrict__ Ao,
                   char* lds) {
  const int tid = threadIdx.x, lane = tid & 63, r32 = lane & 31, h = lane >> 5;
  const int wv = __builtin_amdgcn_readfirstlane(tid >> 6);
  const int head = (bid & 7) * 4 + ((bid >> 3) & 3), qb = bid >> 5;
  const u16* Qh = qkvp + (size_t)head * (S_ * 32);
  const u16* Kh = qkvp + (size_t)(H_ + head) * (S_ * 32);
  const u16* Vh = qkvp + (size_t)(2 * H_ + head) * (S_ * 32);
  const int qrow0 = qb * 256 + wv * 64 + r32;
  bf16x8 qf[2][2];
#pragma unroll
  for (int a = 0; a < 2; ++a) {
    qf[a][0] = *(const bf16x8*)(Qh + (size_t)(qrow0 + a * 32) * 32 + h * 8);
    qf[a][1] = *(const bf16x8*)(Qh + (size_t)(qrow0 + a * 32) * 32 + 16 + h * 8);
  }
  float gq[2] = {gate[qrow0], gate[qrow0 + 32]};
  asm volatile("" ::"v"(qf[0][0]), "v"(qf[0][1]), "v"(qf[1][0]), "v"(qf[1][1]), "v"(gq[0]), "v"(gq[1]));
  f32x16 o[2];
  attn_fast(Kh, Vh, qf, lds, o, wv, lane);
  bool bad = false;
#pragma unroll
  for (int a = 0; a < 2; ++a)
#pragma unroll
    for (int r = 0; r < 11; ++r) bad |= (r < 8 || h == 0) && !(fabsf(o[a][r]) < 1e30f);
  if (__syncthreads_or(bad)) attn_safe(Kh, Vh, qf, lds, o, wv, lane);
  unsigned pw[2][5];
#pragma unroll
  for (int a = 0; a < 2; ++a) {
    float l = __shfl(o[a][10], r32);
    float sc = gq[a] / l;
#pragma unroll
    for (int i = 0; i < 5; ++i) pw[a][i] = pk2(o[a][2 * i] * sc, o[a][2 * i + 1] * sc);
  }
  unsigned xs[5], ys[5];
#pragma unroll
  for (int i = 0; i < 5; ++i) {
    auto r = __builtin_amdgcn_permlane32_swap(pw[0][i], pw[1][i], false, false);
    xs[i] = r[0];
    ys[i] = r[1];
  }
  unsigned* dst = (unsigned*)(Ao + (size_t)(qrow0 + h * 32) * D_ + head * DH_);
  typedef __attribute__((ext_vector_type(4), aligned(4))) unsigned u32x4a;
  *(u32x4a*)dst = (u32x4a){xs[0], xs[1], ys[0], ys[1]};
  *(u32x4a*)(dst + 4) = (u32x4a){xs[2], xs[3], ys[2], ys[3]};
  dst[8] = xs[4];
}

constexpr int K2_STAGE = 16384 + 18432;
DI void down_block(int bid, const u16* __restrict__ Ah, const u16* __restrict__ wd, u16* __restrict__ part,
                   char* lds) {
  const int tid = threadIdx.x, lane = tid & 63;
  const int wv = __builtin_amdgcn_readfirstlane(tid >> 6);
  const int mt_blk = (bid & 7) * 2 + ((bid >> 3) & 1), rest = bid >> 4;
  const int nt_blk = rest & 3, sp = rest >> 2;
  const int m0 = mt_blk * 128, n0 = nt_blk * 144;
  f32x4 acc[9][2] = {};
  const u16* pa = Ah + (size_t)m0 * F_ + sp * 1152;
  const u16* pb = wd + (size_t)sp * 1152 * D_ + n0;
  const unsigned l0 = lds_addr(lds);
  unsigned aof[4], bof[5];
#pragma unroll
  for (int i = 0; i < 4; ++i) {
    int u = (i * 4 + wv) * 64 + lane, row = u >> 3, ch = (u & 7) ^ (row & 7);
    aof[i] = (unsigned)(row * F_ + ch * 8) * 2u;
  }
#pragma unroll
  for (int i = 0; i < 5; ++i) bof[i] = bseg_off<9>((i < 4 ? i * 4 + wv : 16 + (wv & 1)) * 64 + lane, D_);
  auto piece = [&](int i, int kt, unsigned buf) {
    if (i < 4) glds16(pa + kt * 64, aof[i], buf + (i * 4 + wv) * 1024);
    else if (i < 8) glds16(pb + (size_t)kt * 64 * D_, bof[i - 4], buf + 16384 + ((i - 4) * 4 + wv) * 1024);
    else if (wv < 2) glds16(pb + (size_t)kt * 64 * D_, bof[4], buf + 16384 + (16 + wv) * 1024);
  };
  const int g = lane >> 4;
  const int aoff0 = (wv * 32 + (lane & 15)) * 128 + ((g ^ (lane & 7)) << 4), aoff1 = aoff0 ^ 64;
  const int boff = 16384 + g * 9 * 256 + ((lane & 15) >> 2) * 32 + (lane & 3) * 8;
  const int hlo = (g & 1) << 7, hhi = hlo ^ 128;
#pragma unroll
  for (int i = 0; i < 9; ++i) piece(i, 0, l0);
  for (int kt = 0; kt < 18; ++kt) {
    waitv<0>();
    BARRIER();
    const char* cur = lds + (kt & 1) * K2_STAGE;
    const unsigned dst = l0 + ((kt + 1) & 1) * K2_STAGE;
    const bool more = kt + 1 < 18;
    bf16x8 af[2][2];
#pragma unroll
    for (int mt = 0; mt < 2; ++mt) {
      af[0][mt] = *(const bf16x8*)(cur + aoff0 + mt * 2048);
      af[1][mt] = *(const bf16x8*)(cur + aoff1 + mt * 2048);
    }
    bf16x8 bw0 = bfrag2(cur + boff + hlo, cur + boff + hhi);
    bf16x8 bw1 = bfrag2(cur + boff + 256 + hlo, cur + boff + 256 + hhi);
#pragma unroll
    for (int q = 0; q < 18; ++q) {
      bf16x8 b = (q & 1) ? bw1 : bw0;
      if (q + 2 < 18) {
        const int q2 = q + 2, o2 = ((q2 / 9) * 36 + q2 % 9) * 256;
        bf16x8 nb = bfrag2(cur + boff + o2 + hlo, cur + boff + o2 + hhi);
        if (q & 1) bw1 = nb; else bw0 = nb;
      }
      if (q < 9 && more) piece(q, kt + 1, dst);
#pragma unroll
      for (int mt = 0; mt < 2; ++mt)
        acc[q % 9][mt] = __builtin_amdgcn_mfma_f32_16x16x32_bf16(b, af[q / 9][mt], acc[q % 9][mt], 0, 0, 0);
      SBAR();
    }
  }
  u16* dst = part + ((size_t)((sp * 64 + mt_blk * 4 + wv) * 36 + nt_blk * 9) * 64 + lane) * 8;
#pragma unroll
  for (int nt = 0; nt < 9; ++nt)
    *(u32x4*)(dst + nt * 512) = (u32x4){pk2(acc[nt][0][0], acc[nt][0][1]), pk2(acc[nt][0][2], acc[nt][0][3]),
                                        pk2(acc[nt][1][0], acc[nt][1][1]), pk2(acc[nt][1][2], acc[nt][1][3])};
}

__global__ __launch_bounds__(256, 2) void k_mid(const u16* __restrict__ qkvp, const float* __restrict__ gate,
                                                u16* __restrict__ Ao, const u16* __restrict__ Ah,
                                                const u16* __restrict__ wd, u16* __restrict__ part) {
  __shared__ __attribute__((aligned(16))) char lds[2 * K2_STAGE];
  const int bid = blockIdx.x;
  if (bid < 256) attn_block(bid, qkvp, gate, Ao, lds);
  else down_block(bid - 256, Ah, wd, part, lds);
}

constexpr int K3_STAGE = 4096 + 6144;
__global__ __launch_bounds__(256) void k_out(const u16* __restrict__ Ao, const u16* __restrict__ wo,
                                             const u16* __restrict__ part, u16* __restrict__ out) {
  __shared__ __attribute__((aligned(16))) char lds[15 * K3_STAGE];
  const int tid = threadIdx.x, lane = tid & 63;
  const int wv = __builtin_amdgcn_readfirstlane(tid >> 6), wm = wv >> 1, wn = wv & 1;
  const int nt_blk = blockIdx.x % 6, mt_blk = blockIdx.x / 6;
  const int m0 = mt_blk * 64, n0 = nt_blk * 96;
  f32x4 acc[3][2] = {};
  const u16* pa = Ao + (size_t)m0 * D_;
  const u16* pb = wo + n0;
  const unsigned aof0 = aseg_off(wv * 64 + lane, D_);
  const unsigned bof0 = bseg_off<6>(wv * 64 + lane, D_), bof1 = bseg_off<6>((4 + (wv & 1)) * 64 + lane, D_);
  auto piece = [&](int i, int kt, unsigned buf) {
    if (i == 0) glds16(pa + kt * 32, aof0, buf + wv * 1024);
    else if (i == 1) glds16(pb + (size_t)kt * 32 * D_, bof0, buf + 4096 + wv * 1024);
    else if (wv < 2) glds16(pb + (size_t)kt * 32 * D_, bof1, buf + 4096 + (4 + wv) * 1024);
  };
  auto waitL = [&](auto n) { if (wv < 2) waitv<9 * decltype(n)::value>(); else waitv<6 * decltype(n)::value>(); };
  auto bar = [&]() { BARRIER(); };
  const int g = lane >> 4;
  const int aoff = (wm * 32 + (lane & 15)) * 64 + ((g ^ ((-(lane >> 2)) & 3)) << 4);
  const int boff = 4096 + (g * 6 + wn * 3) * 256 + ((lane & 15) >> 2) * 32 + (lane & 3) * 8;
  const int hlo = (g & 1) << 7, hhi = hlo ^ 128;
  auto ldA = [&](const char* st, int mt) { return *(const bf16x8*)(st + aoff + mt * 1024); };
  auto ldB = [&](const char* st, int nt) { return bfrag2(st + boff + nt * 256 + hlo, st + boff + nt * 256 + hhi); };
  auto extra = [&](int, int, unsigned) {};
  u32x4 pv[3][4];
  auto extra0 = [&]() {
#pragma unroll
    for (int nt = 0; nt < 3; ++nt)
#pragma unroll
      for (int sp = 0; sp < 4; ++sp)
        pv[nt][sp] = __builtin_nontemporal_load(
            (const u32x4*)(part + ((size_t)((sp * 64 + mt_blk * 2 + wm) * 36 + nt_blk * 6 + wn * 3 + nt) * 64 + lane) * 8));
  };
  extra0();
  gemm_loop<2, 3, 3, K3_STAGE, 3, 5>(6, lds, acc, ldA, ldB, piece, waitL, extra, bar);
#pragma unroll
  for (int nt = 0; nt < 3; ++nt)
#pragma unroll
    for (int mt = 0; mt < 2; ++mt) {
      size_t off = (size_t)(m0 + wm * 32 + mt * 16 + (lane & 15)) * D_ + n0 + wn * 48 + nt * 16 + 4 * (lane >> 4);
#pragma unroll
      for (int sp = 0; sp < 4; ++sp) {
        u32x2 w = {pv[nt][sp][mt * 2], pv[nt][sp][mt * 2 + 1]};
        acc[nt][mt] += (f32x4){__uint_as_float(w[0] << 16), __uint_as_float(w[0] & 0xffff0000u), __uint_as_float(w[1] << 16),
                               __uint_as_float(w[1] & 0xffff0000u)};
      }
      u32x2 v = {pk2(acc[nt][mt][0], acc[nt][mt][1]), pk2(acc[nt][mt][2], acc[nt][mt][3])};
      *(u32x2*)(out + off) = v;
    }
}

extern "C" void kernel_launch(void* const* d_in, const int* in_sizes, int n_in, void* d_out, int out_size,
                              void* d_ws, size_t ws_size, hipStream_t stream) {
  const u16* x = (const u16*)d_in[0];
  const u16* wqkv = (const u16*)d_in[1];
  const u16* wo = (const u16*)d_in[2];
  const u16* wg = (const u16*)d_in[4];
  const u16* wu = (const u16*)d_in[5];
  const u16* wd = (const u16*)d_in[6];
  const u16* gw = (const u16*)d_in[7];
  char* ws = (char*)d_ws;
  u16* qkvp = (u16*)ws;
  float* gate = (float*)(ws + (size_t)16 * 1024 * 1024);
  u16* Ao = (u16*)(ws + (size_t)17 * 1024 * 1024);
  u16* Ah = (u16*)(ws + (size_t)20 * 1024 * 1024);
  u16* part = (u16*)(ws + (size_t)48 * 1024 * 1024);
  k_proj<<<256, 512, 0, stream>>>(x, wqkv, wg, wu, gw, qkvp, gate, Ah);
  k_mid<<<512, 256, 0, stream>>>(qkvp, gate, Ao, Ah, wd, part);
  k_out<<<192, 256, 0, stream>>>(Ao, wo, part, (u16*)d_out);
}
```

```cpp
#include <hip/hip_runtime.h>
#include <hip/hip_bf16.h>
#include <type_traits>

typedef __attribute__((ext_vector_type(8))) short bf16x8;
typedef __attribute__((ext_vector_type(4))) short s16x4;
typedef __attribute__((ext_vector_type(4))) float f32x4;
typedef __attribute__((ext_vector_type(2))) float f32x2;
typedef __attribute__((ext_vector_type(16))) float f32x16;
typedef __attribute__((ext_vector_type(4))) unsigned u32x4;
typedef __attribute__((ext_vector_type(2))) unsigned u32x2;
typedef __attribute__((ext_vector_type(2))) __bf16 bf16x2_t;
typedef unsigned short u16;

#define DI __device__ __forceinline__
#define LDSP(T) __attribute__((address_space(3))) T

constexpr int S_ = 2048, D_ = 576, H_ = 32, DH_ = 18, F_ = 4608;

DI unsigned pk2(float lo, float hi) {
  f32x2 v = {lo, hi};
  bf16x2_t b = __builtin_convertvector(v, bf16x2_t);
  return __builtin_bit_cast(unsigned, b);
}
DI float rbf(float x) { return __uint_as_float(pk2(x, 0.f) << 16); }
DI float dot2bf(unsigned a, unsigned b, float c) {
  return __builtin_amdgcn_fdot2_f32_bf16(__builtin_bit_cast(bf16x2_t, a), __builtin_bit_cast(bf16x2_t, b), c, false);
}

DI void glds16(const void* sbase, unsigned voff, unsigned lds_base) {
  unsigned sv;
  asm volatile("s_mov_b32 %0, m0\n\ts_mov_b32 m0, %3\n\ts_nop 0\n\tglobal_load_lds_dwordx4 %1, %2\n\ts_mov_b32 m0, %0"
               : "=&s"(sv) : "v"(voff), "s"(sbase), "s"(__builtin_amdgcn_readfirstlane(lds_base)) : "memory");
}
DI unsigned lds_addr(const char* p) { return (unsigned)(size_t)(LDSP(const char)*)p; }
DI s16x4 trread(const char* p) { return __builtin_amdgcn_ds_read_tr16_b64_v4i16((LDSP(s16x4)*)p); }
template <int N> DI void waitv() { asm volatile("s_waitcnt vmcnt(%0)" ::"n"(N) : "memory"); }
#define BARRIER() do { asm volatile("s_waitcnt lgkmcnt(0)" ::: "memory"); __builtin_amdgcn_s_barrier(); asm volatile("" ::: "memory"); } while (0)

#define SBAR() __builtin_amdgcn_sched_barrier(0)
template <int NBs>
DI unsigned bseg_off(int u, int ld) {
  int st = u >> 4, kblk = st / NBs, nblk = st - kblk * NBs;
  int rr = (u >> 1) & 7, hf = u & 1;
  int k = kblk * 8 + (rr ^ ((kblk & 1) << 2));
  return (unsigned)(k * ld + nblk * 16 + hf * 8) * 2u;
}
DI unsigned aseg_off(int u, int lda) {
  int row = u >> 2, ch = (u & 3) ^ ((-(row >> 2)) & 3);
  return (unsigned)(row * lda + ch * 8) * 2u;
}
DI bf16x8 bfrag2(const char* lo, const char* hi) {
  s16x4 a = trread(lo), b = trread(hi);
  return __builtin_shufflevector(a, b, 0, 1, 2, 3, 4, 5, 6, 7);
}

template <int N> using IC = std::integral_constant<int, N>;
template <int MT, int NT, int LMAX, int STAGE, int KS, int NR, class LdA, class LdB, class Piece, class WaitL, class Extra, class Bar, class Pre>
DI void gemm_loop(int nsup, char* lds, f32x4 (&acc)[NT][MT], LdA ldA, LdB ldB, Piece piece, WaitL waitL, Extra extra, Bar bar, Pre pre) {
  static_assert(NT >= MT + 1 && NT >= LMAX, "schedule");
  constexpr int SUP = KS * STAGE;
  const unsigned l0 = lds_addr(lds);
#pragma unroll
  for (int s = 0; s < (NR - 1) * KS; ++s)
#pragma unroll
    for (int i = 0; i < LMAX; ++i) piece(i, s, l0 + s * STAGE);
  pre();
  waitL(IC<NR - 2>{});
  bar();
  bf16x8 af[MT], bw0, bw1;
#pragma unroll
  for (int mt = 0; mt < MT; ++mt) af[mt] = ldA(lds, mt);
  bw0 = ldB(lds, 0);
  bw1 = ldB(lds, 1);
  int cb = 0;
  auto body = [&](int j, auto wn_, auto more_) {
    constexpr bool more = decltype(more_)::value;
    waitL(wn_);
    bar();
    const int nb_ = cb + 1 == NR ? 0 : cb + 1, pb_ = cb == 0 ? NR - 1 : cb - 1;
    const char* base = lds + cb * SUP;
    const char* nbase = lds + nb_ * SUP;
    const unsigned dst = l0 + pb_ * SUP;
#pragma unroll
    for (int ks = 0; ks < KS; ++ks) {
      const char* cur = base + ks * STAGE;
      const char* nx = ks + 1 < KS ? base + (ks + 1) * STAGE : nbase;
      bf16x8 an[MT];
#pragma unroll
      for (int nt = 0; nt < NT; ++nt) {
        bf16x8 b = (nt & 1) ? bw1 : bw0;
        bf16x8 nb = nt + 2 < NT ? ldB(cur, nt + 2) : ldB(nx, nt + 2 - NT);
        if (nt & 1) bw1 = nb; else bw0 = nb;
        if (nt >= NT - 1 - MT && nt < NT - 1) an[nt - (NT - 1 - MT)] = ldA(nx, nt - (NT - 1 - MT));
        if (nt < LMAX && more) piece(nt, (j + NR - 1) * KS + ks, dst + ks * STAGE);
#pragma unroll
        for (int mt = 0; mt < MT; ++mt) acc[nt][mt] = __builtin_amdgcn_mfma_f32_16x16x32_bf16(b, af[mt], acc[nt][mt], 0, 0, 0);
        if (more) extra(nt, (j + NR - 1) * KS + ks, dst + ks * STAGE - l0);
        SBAR();
      }
#pragma unroll
      for (int mt = 0; mt < MT; ++mt) af[mt] = an[mt];
      if (NT & 1) { bf16x8 t = bw0; bw0 = bw1; bw1 = t; }
    }
    cb = nb_;
  };
  for (int j = 0; j < nsup - (NR - 1); ++j) body(j, IC<NR - 3>{}, IC<1>{});
#pragma unroll 1
  for (int j = nsup - (NR - 1); j < nsup; ++j) body(j, IC<0>{}, IC<0>{});
}

constexpr float QSCALE = 0.23570226039551584f * 1.4426950408889634f;
constexpr int K1_STAGE = 16384 + 9216 + 9216 + 4096;

__global__ __launch_bounds__(512) void k_proj(const u16* __restrict__ x, const u16* __restrict__ wqkv,
                                              const u16* __restrict__ wg, const u16* __restrict__ wu,
                                              const u16* __restrict__ gate_w, u16* __restrict__ qkvp,
                                              float* __restrict__ gate, u16* __restrict__ Ah) {
  __shared__ __attribute__((aligned(16))) char lds[4 * K1_STAGE + 1152];
  const int tid = threadIdx.x, lane = tid & 63;
  const int wv = __builtin_amdgcn_readfirstlane(tid >> 6), wm = wv >> 1, wn = wv & 1;
  const int bid = blockIdx.x;
  const int nt_blk = (bid & 7) * 4 + ((bid >> 3) & 3), mt_blk = bid >> 5;
  const int m0 = mt_blk * 256, f0 = nt_blk * 144;
  const int coff = nt_blk == 31 ? 2 : 0;
  const int c0 = nt_blk * 54 - coff;
  char* gwl = lds + 4 * K1_STAGE;

  f32x4 acc[11][4] = {};
  const u16* xa = x + (size_t)m0 * D_;
  const unsigned aof0 = aseg_off(wv * 64 + lane, D_), aof1 = aseg_off((8 + wv) * 64 + lane, D_);
  unsigned bof[3];
  const u16* bsrc[3];
  int bld[3];
#pragma unroll
  for (int i = 0; i < 3; ++i) {
    int jj = i * 8 + wv;
    if (jj >= 22) jj -= 6;
    if (jj < 18) {
      int j = jj < 9 ? jj : jj - 9;
      bof[i] = bseg_off<9>(j * 64 + lane, F_);
      bsrc[i] = (jj < 9 ? wg : wu) + f0;
      bld[i] = F_;
    } else {
      bof[i] = bseg_off<4>(((jj - 18) & 3) * 64 + lane, 3 * D_);
      bsrc[i] = wqkv + c0;
      bld[i] = 3 * D_;
    }
  }
  const bool padlane = wv >= 2 && wv < 6 && (lane & 1) && (lane >> 4) == 3;
  auto piece = [&](int i, int kt, unsigned buf) {
    if (i == 0) glds16(xa + kt * 32, aof0, buf + wv * 1024);
    else if (i == 1) glds16(xa + kt * 32, aof1, buf + (8 + wv) * 1024);
    else if (i < 4) glds16(bsrc[i - 2] + (size_t)kt * 32 * bld[i - 2], bof[i - 2], buf + 16384 + ((i - 2) * 8 + wv) * 1024);
    else if (!padlane) glds16(bsrc[2] + (size_t)kt * 32 * bld[2], bof[2], buf + 16384 + (16 + (wv < 6 ? wv : wv - 6)) * 1024);
  };
  const int gk = lane & 31, gkb = gk >> 3;
  const int gdst = 16384 + 18432 + (gkb * 4 + 3) * 256 + ((gk & 7) ^ ((gkb & 1) << 2)) * 32 + 16;
  auto gfill = [&](int kt, unsigned off) {
    if (wv == 6 && lane < 32) {
      u32x4 v = {(unsigned)*(const u16*)(gwl + (kt * 32 + gk) * 2), 0u, 0u, 0u};
      *(u32x4*)(lds + off + gdst) = v;
    }
  };
  auto pre = [&]() {
    if (wv == 6) {
      waitv<15>();
      gfill(0, 0); gfill(1, K1_STAGE); gfill(2, 2 * K1_STAGE);
    }
  };
  if (wv == 6) {
    glds16(gate_w, lane * 16u, lds_addr(gwl));
    if (lane < 8) glds16(gate_w + 512, lane * 16u, lds_addr(gwl) + 1024);
  }
  auto waitL = [&](auto n) { waitv<5 * decltype(n)::value>(); };
  auto bar = [&]() { BARRIER(); };
  const int g = lane >> 4;
  const int bl = ((lane & 15) >> 2) * 32 + (lane & 3) * 8;
  const int aoff = (wm * 64 + (lane & 15)) * 64 + ((g ^ ((-(lane >> 2)) & 3)) << 4);
  const int bgu = 16384 + wn * 9216 + g * 9 * 256 + bl;
  const int bqq = 16384 + 18432 + (g * 4 + wn * 2) * 256 + bl;
  const int hlo = (g & 1) << 7, hhi = hlo ^ 128;
  auto ldA = [&](const char* st, int mt) { return *(const bf16x8*)(st + aoff + mt * 1024); };
  auto ldB = [&](const char* st, int nt) {
    const char* bp = st + (nt < 9 ? bgu + nt * 256 : bqq + (nt - 9) * 256);
    return bfrag2(bp + hlo, bp + hhi);
  };
  auto extra = [&](int nt, int kt, unsigned off) { if (nt == 5) gfill(kt, off); };
  gemm_loop<4, 11, 5, K1_STAGE, 1, 4>(18, lds, acc, ldA, ldB, piece, waitL, extra, bar, pre);
  BARRIER();
  int tz = threadIdx.x;
  asm volatile("" : "+v"(tz));
  const int lz = tz & 63, gz = lz >> 4, tk = lz & 15;

  char* H = lds + 73728;
  float* G = (float*)(lds + 73728 + 256 * 304);
  const int hg0 = nt_blk * 3;
  {
    char* xs = lds + wv * 9216 + lz * 8;
    if (wn == 1) {
#pragma unroll
      for (int nt = 0; nt < 9; ++nt)
#pragma unroll
        for (int m2 = 0; m2 < 2; ++m2) { f32x4 t = acc[nt][m2]; acc[nt][m2] = acc[nt][2 + m2]; acc[nt][2 + m2] = t; }
    }
#pragma unroll
    for (int nt = 0; nt < 9; ++nt)
#pragma unroll
      for (int m2 = 0; m2 < 2; ++m2) {
        f32x4 v = acc[nt][2 + m2];
        *(u32x2*)(xs + (nt * 2 + m2) * 512) = (u32x2){pk2(v[0], v[1]), pk2(v[2], v[3])};
      }
    if (wn == 1 && gz == 2) {
#pragma unroll
      for (int mt = 0; mt < 4; ++mt) {
        float gv = rbf(1.f / (1.f + __expf(-rbf(acc[10][mt][0]))));
        G[wm * 64 + mt * 16 + tk] = gv;
        if (nt_blk == 0) gate[m0 + wm * 64 + mt * 16 + tk] = gv;
      }
    }
  }
  __syncthreads();
  {
    const char* xr = lds + (wv ^ 1) * 9216 + lz * 8;
    u32x2 pv[9][2];
#pragma unroll
    for (int nt = 0; nt < 9; ++nt)
#pragma unroll
      for (int m2 = 0; m2 < 2; ++m2) pv[nt][m2] = *(const u32x2*)(xr + (nt * 2 + m2) * 512);
    float og[2];
#pragma unroll
    for (int m2 = 0; m2 < 2; ++m2) og[m2] = rbf(1.f - G[wm * 64 + (wn * 2 + m2) * 16 + tk]);
    asm volatile("s_waitcnt lgkmcnt(0)" ::: "memory");
    __builtin_amdgcn_s_barrier();
#pragma unroll
    for (int nt = 0; nt < 9; ++nt)
#pragma unroll
      for (int m2 = 0; m2 < 2; ++m2) {
        f32x4 v = acc[nt][m2];
        u32x2 uv = pv[nt][m2];
        float h0 = v[0] * og[m2] * __uint_as_float(uv[0] << 16);
        float h1 = v[1] * og[m2] * __uint_as_float(uv[0] & 0xffff0000u);
        float h2 = v[2] * og[m2] * __uint_as_float(uv[1] << 16);
        float h3 = v[3] * og[m2] * __uint_as_float(uv[1] & 0xffff0000u);
        *(u32x2*)(H + (wm * 64 + (wn * 2 + m2) * 16 + tk) * 304 + (nt * 16 + 4 * gz) * 2) = (u32x2){pk2(h0, h1), pk2(h2, h3)};
      }
  }
  {
    char* img = lds;
#pragma unroll
    for (int t2 = 0; t2 < 2; ++t2)
#pragma unroll
      for (int r = 0; r < 4; r += 2) {
        int c = wn * 32 + t2 * 16 + 4 * gz + r - coff;
        if (c >= 0 && c < 54) {
          int hl = c / 18, d = c - hl * 18;
          int which = (hg0 + hl) >> 5;
          float sc = which == 0 ? QSCALE : 1.f;
#pragma unroll
          for (int mt = 0; mt < 4; ++mt) {
            int tok = wm * 64 + mt * 16 + tk;
            unsigned v = pk2(acc[9 + t2][mt][r] * sc, acc[9 + t2][mt][r + 1] * sc);
            if (which == 2) {
              char* q = img + hl * 16384 + (tok >> 5) * 2048 + ((((tok >> 4) & 1) * 2 + ((tok >> 2) & 1)) * 32 + d) * 16 +
                        ((tok & 3) + 4 * ((tok >> 3) & 1)) * 2;
              *(u16*)q = (u16)v;
              *(u16*)(q + 16) = (u16)(v >> 16);
            } else {
              int ch = d >> 3;
              if (which == 1) ch ^= (tok >> 2) & 3;
              *(unsigned*)(img + (hl * 256 + tok) * 64 + ch * 16 + (d & 7) * 2) = v;
            }
          }
        }
      }
    for (int i = tz; i < 768; i += 512) {
      int which = (hg0 + (i >> 8)) >> 5;
      if (which == 2) {
        char* grp = img + (i >> 8) * 16384 + ((i & 255) >> 3) * 512;
        int d0 = 18 + (i & 7) * 2;
        unsigned one = d0 == 18 ? 0x3f803f80u : 0u;
        if (d0 < 32) {
          *(u32x4*)(grp + d0 * 16) = (u32x4){one, one, one, one};
          *(u32x4*)(grp + d0 * 16 + 16) = (u32x4){0u, 0u, 0u, 0u};
        }
      } else {
        int sw = which == 1 ? (i >> 2) & 3 : 0;
        char* row = img + i * 64;
        *(unsigned*)(row + (2 ^ sw) * 16 + 4) = 0u;
        *(u32x2*)(row + (2 ^ sw) * 16 + 8) = (u32x2){0u, 0u};
        *(u32x4*)(row + (3 ^ sw) * 16) = (u32x4){0u, 0u, 0u, 0u};
      }
    }
  }
  __syncthreads();
#pragma unroll
  for (int i = 0; i < 9; ++i) {
    int u = i * 512 + tz;
    int row = u / 18, cu = u - row * 18;
    u32x4 v = *(const u32x4*)(H + row * 304 + cu * 16);
    *(u32x4*)(Ah + (size_t)(m0 + row) * F_ + f0 + cu * 8) = v;
  }
#pragma unroll
  for (int i = 0; i < 6; ++i) {
    int u = i * 512 + tz;
    int hl = u >> 10, rem = u & 1023;
    u32x4 v = *(const u32x4*)(lds + u * 16);
    *(u32x4*)(qkvp + ((size_t)(nt_blk * 3 + hl) * S_ + m0) * 32 + rem * 8) = v;
  }
}

DI void attn_stage(const u16* __restrict__ Kh, const u16* __restrict__ Vh, int c, unsigned buf, int wv, int lane) {
#pragma unroll
  for (int i = 0; i < 2; ++i) glds16(Kh + (size_t)c * 4096, ((i * 4 + wv) * 64 + lane) * 16u, buf + (i * 4 + wv) * 1024);
#pragma unroll
  for (int i = 0; i < 2; ++i)
    glds16(Vh + (size_t)c * 4096, ((i * 4 + wv) * 64 + lane) * 16u, buf + 8192 + (i * 4 + wv) * 1024);
}

DI float halfswap_max(float m) {
  auto rr = __builtin_amdgcn_permlane32_swap(__float_as_uint(m), __float_as_uint(m), false, false);
  return fmaxf(__uint_as_float(rr[0]), __uint_as_float(rr[1]));
}
DI float max16(const f32x16& s) {
  float mx = s[0];
#pragma unroll
  for (int r = 1; r < 16; ++r) mx = fmaxf(mx, s[r]);
  return halfswap_max(mx);
}
#define MFMA32(a, b, c) __builtin_amdgcn_mfma_f32_32x32x16_bf16((a), (b), (c), 0, 0, 0)

DI void exp_pack(const f32x16& s, bf16x8& p0, bf16x8& p1) {
  u32x4 a, b;
#pragma unroll
  for (int r = 0; r < 4; ++r) a[r] = pk2(__builtin_amdgcn_exp2f(s[2 * r]), __builtin_amdgcn_exp2f(s[2 * r + 1]));
#pragma unroll
  for (int r = 0; r < 4; ++r) b[r] = pk2(__builtin_amdgcn_exp2f(s[8 + 2 * r]), __builtin_amdgcn_exp2f(s[9 + 2 * r]));
  p0 = __builtin_bit_cast(bf16x8, a);
  p1 = __builtin_bit_cast(bf16x8, b);
}

#define EX2(x) __builtin_amdgcn_exp2f(x)
#define PHASE(S, PA, PB, M1, M2, M3, M4)                                              \
  do {                                                                                \
    M1; SBAR(); PA[0] = pk2(EX2(S[0]), EX2(S[1])); PA[1] = pk2(EX2(S[2]), EX2(S[3])); SBAR();  \
    M2; SBAR(); PA[2] = pk2(EX2(S[4]), EX2(S[5])); PA[3] = pk2(EX2(S[6]), EX2(S[7])); SBAR();  \
    M3; SBAR(); PB[0] = pk2(EX2(S[8]), EX2(S[9])); PB[1] = pk2(EX2(S[10]), EX2(S[11])); SBAR(); \
    M4; SBAR(); PB[2] = pk2(EX2(S[12]), EX2(S[13])); PB[3] = pk2(EX2(S[14]), EX2(S[15])); SBAR(); \
  } while (0)
#define BC8(x) __builtin_bit_cast(bf16x8, x)
DI void attn_fast(const u16* __restrict__ Kh, const u16* __restrict__ Vh, const bf16x8 (&qf)[2][2], char* lds,
                  f32x16 (&o)[2], int wv, int lane) {
  const int r32 = lane & 31, h = lane >> 5;
  const int koff0 = r32 * 64 + ((h ^ ((r32 >> 2) & 3)) << 4);
  const int koff1 = r32 * 64 + (((2 + h) ^ ((r32 >> 2) & 3)) << 4);
  const int voff = 8192 + lane * 16;
  const unsigned l0 = lds_addr(lds);
  f32x16 negm[2], sa, sb;
#pragma unroll
  for (int a = 0; a < 2; ++a)
#pragma unroll
    for (int r = 0; r < 16; ++r) { o[a][r] = 0.f; negm[a][r] = 0.f; }
  waitv<8>();
  BARRIER();
  bf16x8 kc0 = *(const bf16x8*)(lds + koff0), kc1 = *(const bf16x8*)(lds + koff1);
  {
#pragma unroll
    for (int a = 0; a < 2; ++a) {
      f32x16 s = MFMA32(kc0, qf[a][0], negm[a]);
      s = MFMA32(kc1, qf[a][1], s);
      float mx = max16(s);
#pragma unroll
      for (int r = 0; r < 16; ++r) { negm[a][r] = -mx; if (a == 0) sa[r] = s[r] - mx; }
    }
  }
  u32x4 pa0, pa1, pb0 = {0u, 0u, 0u, 0u}, pb1 = {0u, 0u, 0u, 0u};
  bf16x8 vp0 = {0, 0, 0, 0, 0, 0, 0, 0}, vp1 = {0, 0, 0, 0, 0, 0, 0, 0};
  for (int c = 0; c < 16; ++c) {
    if (c + 2 < 16) waitv<4>(); else waitv<0>();
    BARRIER();
    if (c + 3 < 16) attn_stage(Kh, Vh, c + 3, l0 + ((c + 3) & 3) * 16384, wv, lane);
    const char* buf = lds + (c & 3) * 16384;
    const char* bufn = lds + ((c + 1) & 3) * 16384;
#pragma unroll
    for (int t = 0; t < 4; ++t) {
      const char* kb = t < 3 ? buf + (t + 1) * 2048 : bufn;
      const char* vb = buf + t * 2048 + voff;
      bf16x8 kn0 = *(const bf16x8*)(kb + koff0), kn1 = *(const bf16x8*)(kb + koff1);
      bf16x8 vc0 = *(const bf16x8*)vb, vc1 = *(const bf16x8*)(vb + 1024);
      SBAR();
      PHASE(sa, pa0, pa1, sb = MFMA32(kc0, qf[1][0], negm[1]), o[1] = MFMA32(vp0, BC8(pb0), o[1]),
            sb = MFMA32(kc1, qf[1][1], sb), o[1] = MFMA32(vp1, BC8(pb1), o[1]));
      PHASE(sb, pb0, pb1, sa = MFMA32(kn0, qf[0][0], negm[0]), o[0] = MFMA32(vc0, BC8(pa0), o[0]),
            sa = MFMA32(kn1, qf[0][1], sa), o[0] = MFMA32(vc1, BC8(pa1), o[0]));
      kc0 = kn0; kc1 = kn1; vp0 = vc0; vp1 = vc1;
    }
  }
  o[1] = MFMA32(vp0, BC8(pb0), o[1]);
  o[1] = MFMA32(vp1, BC8(pb1), o[1]);
}

DI void attn_safe(const u16* __restrict__ Kh, const u16* __restrict__ Vh, const bf16x8 (&qf)[2][2], char* lds,
                  f32x16 (&o)[2], int wv, int lane) {
  const int r32 = lane & 31, h = lane >> 5;
  const int koff0 = r32 * 64 + ((h ^ ((r32 >> 2) & 3)) << 4);
  const int koff1 = r32 * 64 + (((2 + h) ^ ((r32 >> 2) & 3)) << 4);
  const int voff = 8192 + lane * 16;
  const unsigned l0 = lds_addr(lds);
  f32x16 zero;
  float m[2] = {-INFINITY, -INFINITY};
#pragma unroll
  for (int r = 0; r < 16; ++r) { o[0][r] = 0.f; o[1][r] = 0.f; zero[r] = 0.f; }
  for (int c = 0; c < 16; ++c) {
    BARRIER();
    attn_stage(Kh, Vh, c, l0, wv, lane);
    waitv<0>();
    BARRIER();
    for (int t = 0; t < 4; ++t) {
      const char* kb = lds + t * 2048;
      bf16x8 k0 = *(const bf16x8*)(kb + koff0), k1 = *(const bf16x8*)(kb + koff1);
      bf16x8 va = *(const bf16x8*)(kb + voff), vb = *(const bf16x8*)(kb + voff + 1024);
#pragma unroll
      for (int a = 0; a < 2; ++a) {
        f32x16 s = MFMA32(k0, qf[a][0], zero);
        s = MFMA32(k1, qf[a][1], s);
        float mn = fmaxf(m[a], max16(s));
        float al = __builtin_amdgcn_exp2f(m[a] - mn);
        m[a] = mn;
#pragma unroll
        for (int r = 0; r < 16; ++r) { o[a][r] *= al; s[r] -= mn; }
        bf16x8 p0, p1;
        exp_pack(s, p0, p1);
        o[a] = MFMA32(va, p0, o[a]);
        o[a] = MFMA32(vb, p1, o[a]);
      }
    }
  }
}

DI void attn_block(int bid, const u16* __restrict__ qkvp, const float* __restrict__ gate, u16* __restrict__ Ao,
                   char* lds) {
  const int tid = threadIdx.x, lane = tid & 63, r32 = lane & 31, h = lane >> 5;
  const int wv = __builtin_amdgcn_readfirstlane(tid >> 6);
  const int head = (bid & 7) * 4 + ((bid >> 3) & 3), qb = bid >> 5;
  const u16* Qh = qkvp + (size_t)head * (S_ * 32);
  const u16* Kh = qkvp + (size_t)(H_ + head) * (S_ * 32);
  const u16* Vh = qkvp + (size_t)(2 * H_ + head) * (S_ * 32);
  const int qrow0 = qb * 256 + wv * 64 + r32;
  {
    const unsigned l0 = lds_addr(lds);
    attn_stage(Kh, Vh, 0, l0, wv, lane);
    attn_stage(Kh, Vh, 1, l0 + 16384, wv, lane);
    attn_stage(Kh, Vh, 2, l0 + 32768, wv, lane);
  }
  bf16x8 qf[2][2];
#pragma unroll
  for (int a = 0; a < 2; ++a) {
    qf[a][0] = *(const bf16x8*)(Qh + (size_t)(qrow0 + a * 32) * 32 + h * 8);
    qf[a][1] = *(const bf16x8*)(Qh + (size_t)(qrow0 + a * 32) * 32 + 16 + h * 8);
  }
  float gq[2] = {gate[qrow0], gate[qrow0 + 32]};
  asm volatile("" ::"v"(qf[0][0]), "v"(qf[0][1]), "v"(qf[1][0]), "v"(qf[1][1]), "v"(gq[0]), "v"(gq[1]));
  f32x16 o[2];
  attn_fast(Kh, Vh, qf, lds, o, wv, lane);
  bool bad = false;
#pragma unroll
  for (int a = 0; a < 2; ++a)
#pragma unroll
    for (int r = 0; r < 11; ++r) bad |= (r < 8 || h == 0) && !(fabsf(o[a][r]) < 1e30f);
  if (__syncthreads_or(bad)) attn_safe(Kh, Vh, qf, lds, o, wv, lane);
  unsigned pw[2][5];
#pragma unroll
  for (int a = 0; a < 2; ++a) {
    float l = __shfl(o[a][10], r32);
    float sc = gq[a] / l;
#pragma unroll
    for (int i = 0; i < 5; ++i) pw[a][i] = pk2(o[a][2 * i] * sc, o[a][2 * i + 1] * sc);
  }
  unsigned xs[5], ys[5];
#pragma unroll
  for (int i = 0; i < 5; ++i) {
    auto r = __builtin_amdgcn_permlane32_swap(pw[0][i], pw[1][i], false, false);
    xs[i] = r[0];
    ys[i] = r[1];
  }
  unsigned* dst = (unsigned*)(Ao + (size_t)(qrow0 + h * 32) * D_ + head * DH_);
  typedef __attribute__((ext_vector_type(4), aligned(4))) unsigned u32x4a;
  *(u32x4a*)dst = (u32x4a){xs[0], xs[1], ys[0], ys[1]};
  *(u32x4a*)(dst + 4) = (u32x4a){xs[2], xs[3], ys[2], ys[3]};
  dst[8] = xs[4];
}

constexpr int K2_STAGE = 16384 + 18432;
DI void down_block(int bid, const u16* __restrict__ Ah, const u16* __restrict__ wd, u16* __restrict__ part,
                   char* lds) {
  const int tid = threadIdx.x, lane = tid & 63;
  const int wv = __builtin_amdgcn_readfirstlane(tid >> 6);
  const int mt_blk = (bid & 7) * 2 + ((bid >> 3) & 1), rest = bid >> 4;
  const int nt_blk = rest & 3, sp = rest >> 2;
  const int m0 = mt_blk * 128, n0 = nt_blk * 144;
  f32x4 acc[9][2] = {};
  const u16* pa = Ah + (size_t)m0 * F_ + sp * 1152;
  const u16* pb = wd + (size_t)sp * 1152 * D_ + n0;
  const unsigned l0 = lds_addr(lds);
  unsigned aof[4], bof[5];
#pragma unroll
  for (int i = 0; i < 4; ++i) {
    int u = (i * 4 + wv) * 64 + lane, row = u >> 3, ch = (u & 7) ^ (row & 7);
    aof[i] = (unsigned)(row * F_ + ch * 8) * 2u;
  }
#pragma unroll
  for (int i = 0; i < 5; ++i) bof[i] = bseg_off<9>((i < 4 ? i * 4 + wv : 16 + (wv & 1)) * 64 + lane, D_);
  auto piece = [&](int i, int kt, unsigned buf) {
    if (i < 4) glds16(pa + kt * 64, aof[i], buf + (i * 4 + wv) * 1024);
    else if (i < 8) glds16(pb + (size_t)kt * 64 * D_, bof[i - 4], buf + 16384 + ((i - 4) * 4 + wv) * 1024);
    else if (wv < 2) glds16(pb + (size_t)kt * 64 * D_, bof[4], buf + 16384 + (16 + wv) * 1024);
  };
  const int g = lane >> 4;
  const int aoff0 = (wv * 32 + (lane & 15)) * 128 + ((g ^ (lane & 7)) << 4), aoff1 = aoff0 ^ 64;
  const int boff = 16384 + g * 9 * 256 + ((lane & 15) >> 2) * 32 + (lane & 3) * 8;
  const int hlo = (g & 1) << 7, hhi = hlo ^ 128;
#pragma unroll
  for (int i = 0; i < 9; ++i) piece(i, 0, l0);
  for (int kt = 0; kt < 18; ++kt) {
    waitv<0>();
    BARRIER();
    const char* cur = lds + (kt & 1) * K2_STAGE;
    const unsigned dst = l0 + ((kt + 1) & 1) * K2_STAGE;
    const bool more = kt + 1 < 18;
    bf16x8 af[2][2];
#pragma unroll
    for (int mt = 0; mt < 2; ++mt) {
      af[0][mt] = *(const bf16x8*)(cur + aoff0 + mt * 2048);
      af[1][mt] = *(const bf16x8*)(cur + aoff1 + mt * 2048);
    }
    bf16x8 bw0 = bfrag2(cur + boff + hlo, cur + boff + hhi);
    bf16x8 bw1 = bfrag2(cur + boff + 256 + hlo, cur + boff + 256 + hhi);
#pragma unroll
    for (int q = 0; q < 18; ++q) {
      bf16x8 b = (q & 1) ? bw1 : bw0;
      if (q + 2 < 18) {
        const int q2 = q + 2, o2 = ((q2 / 9) * 36 + q2 % 9) * 256;
        bf16x8 nb = bfrag2(cur + boff + o2 + hlo, cur + boff + o2 + hhi);
        if (q & 1) bw1 = nb; else bw0 = nb;
      }
      if (q < 9 && more) piece(q, kt + 1, dst);
#pragma unroll
      for (int mt = 0; mt < 2; ++mt)
        acc[q % 9][mt] = __builtin_amdgcn_mfma_f32_16x16x32_bf16(b, af[q / 9][mt], acc[q % 9][mt], 0, 0, 0);
      SBAR();
    }
  }
  u16* dst = part + ((size_t)((sp * 64 + mt_blk * 4 + wv) * 36 + nt_blk * 9) * 64 + lane) * 8;
#pragma unroll
  for (int nt = 0; nt < 9; ++nt)
    *(u32x4*)(dst + nt * 512) = (u32x4){pk2(acc[nt][0][0], acc[nt][0][1]), pk2(acc[nt][0][2], acc[nt][0][3]),
                                        pk2(acc[nt][1][0], acc[nt][1][1]), pk2(acc[nt][1][2], acc[nt][1][3])};
}

__global__ __launch_bounds__(256, 2) void k_mid(const u16* __restrict__ qkvp, const float* __restrict__ gate,
                                                u16* __restrict__ Ao, const u16* __restrict__ Ah,
                                                const u16* __restrict__ wd, u16* __restrict__ part) {
  __shared__ __attribute__((aligned(16))) char lds[2 * K2_STAGE];
  const int bid = blockIdx.x;
  if (bid >= 256) attn_block(bid - 256, qkvp, gate, Ao, lds);
  else down_block(bid, Ah, wd, part, lds);
}

constexpr int K3_STAGE = 4096 + 6144;
__global__ __launch_bounds__(256) void k_out(const u16* __restrict__ Ao, const u16* __restrict__ wo,
                                             const u16* __restrict__ part, u16* __restrict__ out) {
  __shared__ __attribute__((aligned(16))) char lds[15 * K3_STAGE];
  const int tid = threadIdx.x, lane = tid & 63;
  const int wv = __builtin_amdgcn_readfirstlane(tid >> 6), wm = wv >> 1, wn = wv & 1;
  const int nt_blk = blockIdx.x % 6, mt_blk = blockIdx.x / 6;
  const int m0 = mt_blk * 64, n0 = nt_blk * 96;
  f32x4 acc[3][2] = {};
  const u16* pa = Ao + (size_t)m0 * D_;
  const u16* pb = wo + n0;
  const unsigned aof0 = aseg_off(wv * 64 + lane, D_);
  const unsigned bof0 = bseg_off<6>(wv * 64 + lane, D_), bof1 = bseg_off<6>((4 + (wv & 1)) * 64 + lane, D_);
  auto piece = [&](int i, int kt, unsigned buf) {
    if (i == 0) glds16(pa + kt * 32, aof0, buf + wv * 1024);
    else if (i == 1) glds16(pb + (size_t)kt * 32 * D_, bof0, buf + 4096 + wv * 1024);
    else if (wv < 2) glds16(pb + (size_t)kt * 32 * D_, bof1, buf + 4096 + (4 + wv) * 1024);
  };
  auto waitL = [&](auto n) { if (wv < 2) waitv<9 * decltype(n)::value>(); else waitv<6 * decltype(n)::value>(); };
  auto bar = [&]() { BARRIER(); };
  const int g = lane >> 4;
  const int aoff = (wm * 32 + (lane & 15)) * 64 + ((g ^ ((-(lane >> 2)) & 3)) << 4);
  const int boff = 4096 + (g * 6 + wn * 3) * 256 + ((lane & 15) >> 2) * 32 + (lane & 3) * 8;
  const int hlo = (g & 1) << 7, hhi = hlo ^ 128;
  auto ldA = [&](const char* st, int mt) { return *(const bf16x8*)(st + aoff + mt * 1024); };
  auto ldB = [&](const char* st, int nt) { return bfrag2(st + boff + nt * 256 + hlo, st + boff + nt * 256 + hhi); };
  auto extra = [&](int, int, unsigned) {};
  u32x4 pv[3][4];
  auto extra0 = [&]() {
#pragma unroll
    for (int nt = 0; nt < 3; ++nt)
#pragma unroll
      for (int sp = 0; sp < 4; ++sp)
        pv[nt][sp] = __builtin_nontemporal_load(
            (const u32x4*)(part + ((size_t)((sp * 64 + mt_blk * 2 + wm) * 36 + nt_blk * 6 + wn * 3 + nt) * 64 + lane) * 8));
  };
  gemm_loop<2, 3, 3, K3_STAGE, 3, 5>(6, lds, acc, ldA, ldB, piece, waitL, extra, bar, extra0);
#pragma unroll
  for (int nt = 0; nt < 3; ++nt)
#pragma unroll
    for (int mt = 0; mt < 2; ++mt) {
      size_t off = (size_t)(m0 + wm * 32 + mt * 16 + (lane & 15)) * D_ + n0 + wn * 48 + nt * 16 + 4 * (lane >> 4);
#pragma unroll
      for (int sp = 0; sp < 4; ++sp) {
        u32x2 w = {pv[nt][sp][mt * 2], pv[nt][sp][mt * 2 + 1]};
        acc[nt][mt] += (f32x4){__uint_as_float(w[0] << 16), __uint_as_float(w[0] & 0xffff0000u), __uint_as_float(w[1] << 16),
                               __uint_as_float(w[1] & 0xffff0000u)};
      }
      u32x2 v = {pk2(acc[nt][mt][0], acc[nt][mt][1]), pk2(acc[nt][mt][2], acc[nt][mt][3])};
      *(u32x2*)(out + off) = v;
    }
}

extern "C" void kernel_launch(void* const* d_in, const int* in_sizes, int n_in, void* d_out, int out_size,
                              void* d_ws, size_t ws_size, hipStream_t stream) {
  const u16* x = (const u16*)d_in[0];
  const u16* wqkv = (const u16*)d_in[1];
  const u16* wo = (const u16*)d_in[2];
  const u16* wg = (const u16*)d_in[4];
  const u16* wu = (const u16*)d_in[5];
  const u16* wd = (const u16*)d_in[6];
  const u16* gw = (const u16*)d_in[7];
  char* ws = (char*)d_ws;
  u16* qkvp = (u16*)ws;
  float* gate = (float*)(ws + (size_t)16 * 1024 * 1024);
  u16* Ao = (u16*)(ws + (size_t)17 * 1024 * 1024);
  u16* Ah = (u16*)(ws + (size_t)20 * 1024 * 1024);
  u16* part = (u16*)(ws + (size_t)48 * 1024 * 1024);
  k_proj<<<256, 512, 0, stream>>>(x, wqkv, wg, wu, gw, qkvp, gate, Ah);
  k_mid<<<512, 256, 0, stream>>>(qkvp, gate, Ao, Ah, wd, part);
  k_out<<<192, 256, 0, stream>>>(Ao, wo, part, (u16*)d_out);
}
```

```cpp
#include <hip/hip_runtime.h>
#include <hip/hip_bf16.h>
#include <type_traits>

typedef __attribute__((ext_vector_type(8))) short bf16x8;
typedef __attribute__((ext_vector_type(4))) short s16x4;
typedef __attribute__((ext_vector_type(4))) float f32x4;
typedef __attribute__((ext_vector_type(2))) float f32x2;
typedef __attribute__((ext_vector_type(16))) float f32x16;
typedef __attribute__((ext_vector_type(4))) unsigned u32x4;
typedef __attribute__((ext_vector_type(2))) unsigned u32x2;
typedef __attribute__((ext_vector_type(2))) __bf16 bf16x2_t;
typedef unsigned short u16;

#define DI __device__ __forceinline__
#define LDSP(T) __attribute__((address_space(3))) T

constexpr int S_ = 2048, D_ = 576, H_ = 32, DH_ = 18, F_ = 4608;

DI unsigned pk2(float lo, float hi) {
  f32x2 v = {lo, hi};
  bf16x2_t b = __builtin_convertvector(v, bf16x2_t);
  return __builtin_bit_cast(unsigned, b);
}
DI float rbf(float x) { return __uint_as_float(pk2(x, 0.f) << 16); }
DI float dot2bf(unsigned a, unsigned b, float c) {
  return __builtin_amdgcn_fdot2_f32_bf16(__builtin_bit_cast(bf16x2_t, a), __builtin_bit_cast(bf16x2_t, b), c, false);
}

DI void glds16(const void* sbase, unsigned voff, unsigned lds_base) {
  unsigned sv;
  asm volatile("s_mov_b32 %0, m0\n\ts_mov_b32 m0, %3\n\ts_nop 0\n\tglobal_load_lds_dwordx4 %1, %2\n\ts_mov_b32 m0, %0"
               : "=&s"(sv) : "v"(voff), "s"(sbase), "s"(__builtin_amdgcn_readfirstlane(lds_base)) : "memory");
}
DI unsigned lds_addr(const char* p) { return (unsigned)(size_t)(LDSP(const char)*)p; }
DI s16x4 trread(const char* p) { return __builtin_amdgcn_ds_read_tr16_b64_v4i16((LDSP(s16x4)*)p); }
template <int N> DI void waitv() { asm volatile("s_waitcnt vmcnt(%0)" ::"n"(N) : "memory"); }
#define BARRIER() do { asm volatile("s_waitcnt lgkmcnt(0)" ::: "memory"); __builtin_amdgcn_s_barrier(); asm volatile("" ::: "memory"); } while (0)

#define SBAR() __builtin_amdgcn_sched_barrier(0)
template <int NBs>
DI unsigned bseg_off(int u, int ld) {
  int st = u >> 4, kblk = st / NBs, nblk = st - kblk * NBs;
  int rr = (u >> 1) & 7, hf = u & 1;
  int k = kblk * 8 + (rr ^ ((kblk & 1) << 2));
  return (unsigned)(k * ld + nblk * 16 + hf * 8) * 2u;
}
DI unsigned aseg_off(int u, int lda) {
  int row = u >> 2, ch = (u & 3) ^ ((-(row >> 2)) & 3);
  return (unsigned)(row * lda + ch * 8) * 2u;
}
DI bf16x8 bfrag2(const char* lo, const char* hi) {
  s16x4 a = trread(lo), b = trread(hi);
  return __builtin_shufflevector(a, b, 0, 1, 2, 3, 4, 5, 6, 7);
}

template <int N> using IC = std::integral_constant<int, N>;
template <int MT, int NT, int LMAX, int STAGE, int KS, int NR, class LdA, class LdB, class Piece, class WaitL, class Extra, class Bar, class Pre>
DI void gemm_loop(int nsup, char* lds, f32x4 (&acc)[NT][MT], LdA ldA, LdB ldB, Piece piece, WaitL waitL, Extra extra, Bar bar, Pre pre) {
  static_assert(NT >= MT + 1 && NT >= LMAX, "schedule");
  constexpr int SUP = KS * STAGE;
  const unsigned l0 = lds_addr(lds);
#pragma unroll
  for (int s = 0; s < (NR - 1) * KS; ++s)
#pragma unroll
    for (int i = 0; i < LMAX; ++i) piece(i, s, l0 + s * STAGE);
  pre();
  waitL(IC<NR - 2>{});
  bar();
  bf16x8 af[MT], bw0, bw1;
#pragma unroll
  for (int mt = 0; mt < MT; ++mt) af[mt] = ldA(lds, mt);
  bw0 = ldB(lds, 0);
  bw1 = ldB(lds, 1);
  int cb = 0;
  auto body = [&](int j, auto wn_, auto more_) {
    constexpr bool more = decltype(more_)::value;
    waitL(wn_);
    bar();
    const int nb_ = cb + 1 == NR ? 0 : cb + 1, pb_ = cb == 0 ? NR - 1 : cb - 1;
    const char* base = lds + cb * SUP;
    const char* nbase = lds + nb_ * SUP;
    const unsigned dst = l0 + pb_ * SUP;
#pragma unroll
    for (int ks = 0; ks < KS; ++ks) {
      const char* cur = base + ks * STAGE;
      const char* nx = ks + 1 < KS ? base + (ks + 1) * STAGE : nbase;
      bf16x8 an[MT];
#pragma unroll
      for (int nt = 0; nt < NT; ++nt) {
        bf16x8 b = (nt & 1) ? bw1 : bw0;
        bf16x8 nb = nt + 2 < NT ? ldB(cur, nt + 2) : ldB(nx, nt + 2 - NT);
        if (nt & 1) bw1 = nb; else bw0 = nb;
        if (nt >= NT - 1 - MT && nt < NT - 1) an[nt - (NT - 1 - MT)] = ldA(nx, nt - (NT - 1 - MT));
        if (nt < LMAX && more) piece(nt, (j + NR - 1) * KS + ks, dst + ks * STAGE);
#pragma unroll
        for (int mt = 0; mt < MT; ++mt) acc[nt][mt] = __builtin_amdgcn_mfma_f32_16x16x32_bf16(b, af[mt], acc[nt][mt], 0, 0, 0);
        if (more) extra(nt, (j + NR - 1) * KS + ks, dst + ks * STAGE - l0);
        SBAR();
      }
#pragma unroll
      for (int mt = 0; mt < MT; ++mt) af[mt] = an[mt];
      if (NT & 1) { bf16x8 t = bw0; bw0 = bw1; bw1 = t; }
    }
    cb = nb_;
  };
  for (int j = 0; j < nsup - (NR - 1); ++j) body(j, IC<NR - 3>{}, IC<1>{});
#pragma unroll 1
  for (int j = nsup - (NR - 1); j < nsup; ++j) body(j, IC<0>{}, IC<0>{});
}

constexpr float QSCALE = 0.23570226039551584f * 1.4426950408889634f;
constexpr int K1_STAGE = 16384 + 9216 + 9216 + 4096;

__global__ __launch_bounds__(512) void k_proj(const u16* __restrict__ x, const u16* __restrict__ wqkv,
                                              const u16* __restrict__ wg, const u16* __restrict__ wu,
                                              const u16* __restrict__ gate_w, u16* __restrict__ qkvp,
                                              float* __restrict__ gate, u16* __restrict__ Ah) {
  __shared__ __attribute__((aligned(16))) char lds[4 * K1_STAGE + 1152];
  const int tid = threadIdx.x, lane = tid & 63;
  const int wv = __builtin_amdgcn_readfirstlane(tid >> 6), wm = wv >> 1, wn = wv & 1;
  const int bid = blockIdx.x;
  const int nt_blk = (bid & 7) * 4 + ((bid >> 3) & 3), mt_blk = bid >> 5;
  const int m0 = mt_blk * 256, f0 = nt_blk * 144;
  const int coff = nt_blk == 31 ? 2 : 0;
  const int c0 = nt_blk * 54 - coff;
  char* gwl = lds + 4 * K1_STAGE;

  f32x4 acc[11][4] = {};
  const u16* xa = x + (size_t)m0 * D_;
  const unsigned aof0 = aseg_off(wv * 64 + lane, D_), aof1 = aseg_off((8 + wv) * 64 + lane, D_);
  unsigned bof[3];
  const u16* bsrc[3];
  int bld[3];
#pragma unroll
  for (int i = 0; i < 3; ++i) {
    int jj = i * 8 + wv;
    if (jj >= 22) jj -= 6;
    if (jj < 18) {
      int j = jj < 9 ? jj : jj - 9;
      bof[i] = bseg_off<9>(j * 64 + lane, F_);
      bsrc[i] = (jj < 9 ? wg : wu) + f0;
      bld[i] = F_;
    } else {
      bof[i] = bseg_off<4>(((jj - 18) & 3) * 64 + lane, 3 * D_);
      bsrc[i] = wqkv + c0;
      bld[i] = 3 * D_;
    }
  }
  const bool padlane = wv >= 2 && wv < 6 && (lane & 1) && (lane >> 4) == 3;
  auto piece = [&](int i, int kt, unsigned buf) {
    if (i == 0) glds16(xa + kt * 32, aof0, buf + wv * 1024);
    else if (i == 1) glds16(xa + kt * 32, aof1, buf + (8 + wv) * 1024);
    else if (i < 4) glds16(bsrc[i - 2] + (size_t)kt * 32 * bld[i - 2], bof[i - 2], buf + 16384 + ((i - 2) * 8 + wv) * 1024);
    else if (!padlane) glds16(bsrc[2] + (size_t)kt * 32 * bld[2], bof[2], buf + 16384 + (16 + (wv < 6 ? wv : wv - 6)) * 1024);
  };
  const int gk = lane & 31, gkb = gk >> 3;
  const int gdst = 16384 + 18432 + (gkb * 4 + 3) * 256 + ((gk & 7) ^ ((gkb & 1) << 2)) * 32 + 16;
  auto gfill = [&](int kt, unsigned off) {
    if (wv == 6 && lane < 32) {
      u32x4 v = {(unsigned)*(const u16*)(gwl + (kt * 32 + gk) * 2), 0u, 0u, 0u};
      *(u32x4*)(lds + off + gdst) = v;
    }
  };
  auto pre = [&]() {
    if (wv == 6) {
      waitv<15>();
      gfill(0, 0); gfill(1, K1_STAGE); gfill(2, 2 * K1_STAGE);
    }
  };
  if (wv == 6) {
    glds16(gate_w, lane * 16u, lds_addr(gwl));
    if (lane < 8) glds16(gate_w + 512, lane * 16u, lds_addr(gwl) + 1024);
  }
  auto waitL = [&](auto n) { waitv<5 * decltype(n)::value>(); };
  auto bar = [&]() { BARRIER(); };
  const int g = lane >> 4;
  const int bl = ((lane & 15) >> 2) * 32 + (lane & 3) * 8;
  const int aoff = (wm * 64 + (lane & 15)) * 64 + ((g ^ ((-(lane >> 2)) & 3)) << 4);
  const int bgu = 16384 + wn * 9216 + g * 9 * 256 + bl;
  const int bqq = 16384 + 18432 + (g * 4 + wn * 2) * 256 + bl;
  const int hlo = (g & 1) << 7, hhi = hlo ^ 128;
  auto ldA = [&](const char* st, int mt) { return *(const bf16x8*)(st + aoff + mt * 1024); };
  auto ldB = [&](const char* st, int nt) {
    const char* bp = st + (nt < 9 ? bgu + nt * 256 : bqq + (nt - 9) * 256);
    return bfrag2(bp + hlo, bp + hhi);
  };
  auto extra = [&](int nt, int kt, unsigned off) { if (nt == 5) gfill(kt, off); };
  gemm_loop<4, 11, 5, K1_STAGE, 1, 4>(18, lds, acc, ldA, ldB, piece, waitL, extra, bar, pre);
  BARRIER();
  int tz = threadIdx.x;
  asm volatile("" : "+v"(tz));
  const int lz = tz & 63, gz = lz >> 4, tk = lz & 15;

  float* G = (float*)(lds + 73728);
  const int hg0 = nt_blk * 3;
  {
    char* xs = lds + wv * 9216 + lz * 8;
    if (wn == 1) {
#pragma unroll
      for (int nt = 0; nt < 9; ++nt)
#pragma unroll
        for (int m2 = 0; m2 < 2; ++m2) { f32x4 t = acc[nt][m2]; acc[nt][m2] = acc[nt][2 + m2]; acc[nt][2 + m2] = t; }
    }
#pragma unroll
    for (int nt = 0; nt < 9; ++nt)
#pragma unroll
      for (int m2 = 0; m2 < 2; ++m2) {
        f32x4 v = acc[nt][2 + m2];
        *(u32x2*)(xs + (nt * 2 + m2) * 512) = (u32x2){pk2(v[0], v[1]), pk2(v[2], v[3])};
      }
    if (wn == 1 && gz == 2) {
#pragma unroll
      for (int mt = 0; mt < 4; ++mt) {
        float gv = rbf(1.f / (1.f + __expf(-rbf(acc[10][mt][0]))));
        G[wm * 64 + mt * 16 + tk] = gv;
        if (nt_blk == 0) gate[m0 + wm * 64 + mt * 16 + tk] = gv;
      }
    }
  }
  __syncthreads();
  {
    const char* xr = lds + (wv ^ 1) * 9216 + lz * 8;
    u32x2 pv[9][2];
#pragma unroll
    for (int nt = 0; nt < 9; ++nt)
#pragma unroll
      for (int m2 = 0; m2 < 2; ++m2) pv[nt][m2] = *(const u32x2*)(xr + (nt * 2 + m2) * 512);
    float og[2];
#pragma unroll
    for (int m2 = 0; m2 < 2; ++m2) og[m2] = rbf(1.f - G[wm * 64 + (wn * 2 + m2) * 16 + tk]);
    asm volatile("s_waitcnt lgkmcnt(0)" ::: "memory");
    __builtin_amdgcn_s_barrier();
#pragma unroll
    for (int nt = 0; nt < 9; ++nt)
#pragma unroll
      for (int m2 = 0; m2 < 2; ++m2) {
        f32x4 v = acc[nt][m2];
        u32x2 uv = pv[nt][m2];
        float h0 = v[0] * og[m2] * __uint_as_float(uv[0] << 16);
        float h1 = v[1] * og[m2] * __uint_as_float(uv[0] & 0xffff0000u);
        float h2 = v[2] * og[m2] * __uint_as_float(uv[1] << 16);
        float h3 = v[3] * og[m2] * __uint_as_float(uv[1] & 0xffff0000u);
        pv[nt][m2] = (u32x2){pk2(h0, h1), pk2(h2, h3)};
      }
#pragma unroll
    for (int m2 = 0; m2 < 2; ++m2) {
      char* hb = (char*)Ah + ((size_t)((mt_blk * 16 + wm * 4 + wn * 2 + m2) * 144 + ((nt_blk * 9) >> 1)) * 64 + lz) * 16;
      if ((nt_blk & 1) == 0) {
#pragma unroll
        for (int i = 0; i < 4; ++i)
          *(u32x4*)(hb + i * 1024) = (u32x4){pv[2 * i][m2][0], pv[2 * i][m2][1], pv[2 * i + 1][m2][0], pv[2 * i + 1][m2][1]};
        *(u32x2*)(hb + 4096) = pv[8][m2];
      } else {
        *(u32x2*)(hb + 8) = pv[0][m2];
#pragma unroll
        for (int i = 0; i < 4; ++i)
          *(u32x4*)(hb + (i + 1) * 1024) =
              (u32x4){pv[2 * i + 1][m2][0], pv[2 * i + 1][m2][1], pv[2 * i + 2][m2][0], pv[2 * i + 2][m2][1]};
      }
    }
  }
  {
    char* img = lds;
#pragma unroll
    for (int t2 = 0; t2 < 2; ++t2)
#pragma unroll
      for (int r = 0; r < 4; r += 2) {
        int c = wn * 32 + t2 * 16 + 4 * gz + r - coff;
        if (c >= 0 && c < 54) {
          int hl = c / 18, d = c - hl * 18;
          int which = (hg0 + hl) >> 5;
          float sc = which == 0 ? QSCALE : 1.f;
#pragma unroll
          for (int mt = 0; mt < 4; ++mt) {
            int tok = wm * 64 + mt * 16 + tk;
            unsigned v = pk2(acc[9 + t2][mt][r] * sc, acc[9 + t2][mt][r + 1] * sc);
            if (which == 2) {
              char* q = img + hl * 16384 + (tok >> 5) * 2048 + ((((tok >> 4) & 1) * 2 + ((tok >> 2) & 1)) * 32 + d) * 16 +
                        ((tok & 3) + 4 * ((tok >> 3) & 1)) * 2;
              *(u16*)q = (u16)v;
              *(u16*)(q + 16) = (u16)(v >> 16);
            } else {
              int ch = d >> 3;
              if (which == 1) ch ^= (tok >> 2) & 3;
              *(unsigned*)(img + (hl * 256 + tok) * 64 + ch * 16 + (d & 7) * 2) = v;
            }
          }
        }
      }
    for (int i = tz; i < 768; i += 512) {
      int which = (hg0 + (i >> 8)) >> 5;
      if (which == 2) {
        char* grp = img + (i >> 8) * 16384 + ((i & 255) >> 3) * 512;
        int d0 = 18 + (i & 7) * 2;
        unsigned one = d0 == 18 ? 0x3f803f80u : 0u;
        if (d0 < 32) {
          *(u32x4*)(grp + d0 * 16) = (u32x4){one, one, one, one};
          *(u32x4*)(grp + d0 * 16 + 16) = (u32x4){0u, 0u, 0u, 0u};
        }
      } else {
        int sw = which == 1 ? (i >> 2) & 3 : 0;
        char* row = img + i * 64;
        *(unsigned*)(row + (2 ^ sw) * 16 + 4) = 0u;
        *(u32x2*)(row + (2 ^ sw) * 16 + 8) = (u32x2){0u, 0u};
        *(u32x4*)(row + (3 ^ sw) * 16) = (u32x4){0u, 0u, 0u, 0u};
      }
    }
  }
  __syncthreads();
#pragma unroll
  for (int i = 0; i < 6; ++i) {
    int u = i * 512 + tz;
    int hl = u >> 10, rem = u & 1023;
    u32x4 v = *(const u32x4*)(lds + u * 16);
    *(u32x4*)(qkvp + ((size_t)(nt_blk * 3 + hl) * S_ + m0) * 32 + rem * 8) = v;
  }
}

DI void attn_stage(const u16* __restrict__ Kh, const u16* __restrict__ Vh, int c, unsigned buf, int wv, int lane) {
#pragma unroll
  for (int i = 0; i < 2; ++i) glds16(Kh + (size_t)c * 4096, ((i * 4 + wv) * 64 + lane) * 16u, buf + (i * 4 + wv) * 1024);
#pragma unroll
  for (int i = 0; i < 2; ++i)
    glds16(Vh + (size_t)c * 4096, ((i * 4 + wv) * 64 + lane) * 16u, buf + 8192 + (i * 4 + wv) * 1024);
}

DI float halfswap_max(float m) {
  auto rr = __builtin_amdgcn_permlane32_swap(__float_as_uint(m), __float_as_uint(m), false, false);
  return fmaxf(__uint_as_float(rr[0]), __uint_as_float(rr[1]));
}
DI float max16(const f32x16& s) {
  float mx = s[0];
#pragma unroll
  for (int r = 1; r < 16; ++r) mx = fmaxf(mx, s[r]);
  return halfswap_max(mx);
}
#define MFMA32(a, b, c) __builtin_amdgcn_mfma_f32_32x32x16_bf16((a), (b), (c), 0, 0, 0)

DI void exp_pack(const f32x16& s, bf16x8& p0, bf16x8& p1) {
  u32x4 a, b;
#pragma unroll
  for (int r = 0; r < 4; ++r) a[r] = pk2(__builtin_amdgcn_exp2f(s[2 * r]), __builtin_amdgcn_exp2f(s[2 * r + 1]));
#pragma unroll
  for (int r = 0; r < 4; ++r) b[r] = pk2(__builtin_amdgcn_exp2f(s[8 + 2 * r]), __builtin_amdgcn_exp2f(s[9 + 2 * r]));
  p0 = __builtin_bit_cast(bf16x8, a);
  p1 = __builtin_bit_cast(bf16x8, b);
}

#define EX2(x) __builtin_amdgcn_exp2f(x)
#define PHASE(S, PA, PB, M1, M2, M3, M4)                                              \
  do {                                                                                \
    M1; SBAR(); PA[0] = pk2(EX2(S[0]), EX2(S[1])); PA[1] = pk2(EX2(S[2]), EX2(S[3])); SBAR();  \
    M2; SBAR(); PA[2] = pk2(EX2(S[4]), EX2(S[5])); PA[3] = pk2(EX2(S[6]), EX2(S[7])); SBAR();  \
    M3; SBAR(); PB[0] = pk2(EX2(S[8]), EX2(S[9])); PB[1] = pk2(EX2(S[10]), EX2(S[11])); SBAR(); \
    M4; SBAR(); PB[2] = pk2(EX2(S[12]), EX2(S[13])); PB[3] = pk2(EX2(S[14]), EX2(S[15])); SBAR(); \
  } while (0)
#define BC8(x) __builtin_bit_cast(bf16x8, x)
DI void attn_fast(const u16* __restrict__ Kh, const u16* __restrict__ Vh, const bf16x8 (&qf)[2][2], char* lds,
                  f32x16 (&o)[2], int wv, int lane) {
  const int r32 = lane & 31, h = lane >> 5;
  const int koff0 = r32 * 64 + ((h ^ ((r32 >> 2) & 3)) << 4);
  const int koff1 = r32 * 64 + (((2 + h) ^ ((r32 >> 2) & 3)) << 4);
  const int voff = 8192 + lane * 16;
  const unsigned l0 = lds_addr(lds);
  f32x16 negm[2], sa, sb;
#pragma unroll
  for (int a = 0; a < 2; ++a)
#pragma unroll
    for (int r = 0; r < 16; ++r) { o[a][r] = 0.f; negm[a][r] = 0.f; }
  waitv<8>();
  BARRIER();
  bf16x8 kc0 = *(const bf16x8*)(lds + koff0), kc1 = *(const bf16x8*)(lds + koff1);
  {
#pragma unroll
    for (int a = 0; a < 2; ++a) {
      f32x16 s = MFMA32(kc0, qf[a][0], negm[a]);
      s = MFMA32(kc1, qf[a][1], s);
      float mx = max16(s);
#pragma unroll
      for (int r = 0; r < 16; ++r) { negm[a][r] = -mx; if (a == 0) sa[r] = s[r] - mx; }
    }
  }
  u32x4 pa0, pa1, pb0 = {0u, 0u, 0u, 0u}, pb1 = {0u, 0u, 0u, 0u};
  bf16x8 vp0 = {0, 0, 0, 0, 0, 0, 0, 0}, vp1 = {0, 0, 0, 0, 0, 0, 0, 0};
  for (int c = 0; c < 16; ++c) {
    if (c + 2 < 16) waitv<4>(); else waitv<0>();
    BARRIER();
    if (c + 3 < 16) attn_stage(Kh, Vh, c + 3, l0 + ((c + 3) & 3) * 16384, wv, lane);
    const char* buf = lds + (c & 3) * 16384;
    const char* bufn = lds + ((c + 1) & 3) * 16384;
#pragma unroll
    for (int t = 0; t < 4; ++t) {
      const char* kb = t < 3 ? buf + (t + 1) * 2048 : bufn;
      const char* vb = buf + t * 2048 + voff;
      bf16x8 kn0 = *(const bf16x8*)(kb + koff0), kn1 = *(const bf16x8*)(kb + koff1);
      bf16x8 vc0 = *(const bf16x8*)vb, vc1 = *(const bf16x8*)(vb + 1024);
      SBAR();
      PHASE(sa, pa0, pa1, sb = MFMA32(kc0, qf[1][0], negm[1]), o[1] = MFMA32(vp0, BC8(pb0), o[1]),
            sb = MFMA32(kc1, qf[1][1], sb), o[1] = MFMA32(vp1, BC8(pb1), o[1]));
      PHASE(sb, pb0, pb1, sa = MFMA32(kn0, qf[0][0], negm[0]), o[0] = MFMA32(vc0, BC8(pa0), o[0]),
            sa = MFMA32(kn1, qf[0][1], sa), o[0] = MFMA32(vc1, BC8(pa1), o[0]));
      kc0 = kn0; kc1 = kn1; vp0 = vc0; vp1 = vc1;
    }
  }
  o[1] = MFMA32(vp0, BC8(pb0), o[1]);
  o[1] = MFMA32(vp1, BC8(pb1), o[1]);
}

DI void attn_safe(const u16* __restrict__ Kh, const u16* __restrict__ Vh, const bf16x8 (&qf)[2][2], char* lds,
                  f32x16 (&o)[2], int wv, int lane) {
  const int r32 = lane & 31, h = lane >> 5;
  const int koff0 = r32 * 64 + ((h ^ ((r32 >> 2) & 3)) << 4);
  const int koff1 = r32 * 64 + (((2 + h) ^ ((r32 >> 2) & 3)) << 4);
  const int voff = 8192 + lane * 16;
  const unsigned l0 = lds_addr(lds);
  f32x16 zero;
  float m[2] = {-INFINITY, -INFINITY};
#pragma unroll
  for (int r = 0; r < 16; ++r) { o[0][r] = 0.f; o[1][r] = 0.f; zero[r] = 0.f; }
  for (int c = 0; c < 16; ++c) {
    BARRIER();
    attn_stage(Kh, Vh, c, l0, wv, lane);
    waitv<0>();
    BARRIER();
    for (int t = 0; t < 4; ++t) {
      const char* kb = lds + t * 2048;
      bf16x8 k0 = *(const bf16x8*)(kb + koff0), k1 = *(const bf16x8*)(kb + koff1);
      bf16x8 va = *(const bf16x8*)(kb + voff), vb = *(const bf16x8*)(kb + voff + 1024);
#pragma unroll
      for (int a = 0; a < 2; ++a) {
        f32x16 s = MFMA32(k0, qf[a][0], zero);
        s = MFMA32(k1, qf[a][1], s);
        float mn = fmaxf(m[a], max16(s));
        float al = __builtin_amdgcn_exp2f(m[a] - mn);
        m[a] = mn;
#pragma unroll
        for (int r = 0; r < 16; ++r) { o[a][r] *= al; s[r] -= mn; }
        bf16x8 p0, p1;
        exp_pack(s, p0, p1);
        o[a] = MFMA32(va, p0, o[a]);
        o[a] = MFMA32(vb, p1, o[a]);
      }
    }
  }
}

DI void attn_block(int bid, const u16* __restrict__ qkvp, const float* __restrict__ gate, u16* __restrict__ Ao,
                   char* lds) {
  const int tid = threadIdx.x, lane = tid & 63, r32 = lane & 31, h = lane >> 5;
  const int wv = __builtin_amdgcn_readfirstlane(tid >> 6);
  const int head = (bid & 7) * 4 + ((bid >> 3) & 3), qb = bid >> 5;
  const u16* Qh = qkvp + (size_t)head * (S_ * 32);
  const u16* Kh = qkvp + (size_t)(H_ + head) * (S_ * 32);
  const u16* Vh = qkvp + (size_t)(2 * H_ + head) * (S_ * 32);
  const int qrow0 = qb * 256 + wv * 64 + r32;
  {
    const unsigned l0 = lds_addr(lds);
    attn_stage(Kh, Vh, 0, l0, wv, lane);
    attn_stage(Kh, Vh, 1, l0 + 16384, wv, lane);
    attn_stage(Kh, Vh, 2, l0 + 32768, wv, lane);
  }
  bf16x8 qf[2][2];
#pragma unroll
  for (int a = 0; a < 2; ++a) {
    qf[a][0] = *(const bf16x8*)(Qh + (size_t)(qrow0 + a * 32) * 32 + h * 8);
    qf[a][1] = *(const bf16x8*)(Qh + (size_t)(qrow0 + a * 32) * 32 + 16 + h * 8);
  }
  float gq[2] = {gate[qrow0], gate[qrow0 + 32]};
  asm volatile("" ::"v"(qf[0][0]), "v"(qf[0][1]), "v"(qf[1][0]), "v"(qf[1][1]), "v"(gq[0]), "v"(gq[1]));
  f32x16 o[2];
  attn_fast(Kh, Vh, qf, lds, o, wv, lane);
  bool bad = false;
#pragma unroll
  for (int a = 0; a < 2; ++a)
#pragma unroll
    for (int r = 0; r < 11; ++r) bad |= (r < 8 || h == 0) && !(fabsf(o[a][r]) < 1e30f);
  if (__syncthreads_or(bad)) attn_safe(Kh, Vh, qf, lds, o, wv, lane);
  unsigned pw[2][5];
#pragma unroll
  for (int a = 0; a < 2; ++a) {
    float l = __shfl(o[a][10], r32);
    float sc = gq[a] / l;
#pragma unroll
    for (int i = 0; i < 5; ++i) pw[a][i] = pk2(o[a][2 * i] * sc, o[a][2 * i + 1] * sc);
  }
  unsigned xs[5], ys[5];
#pragma unroll
  for (int i = 0; i < 5; ++i) {
    auto r = __builtin_amdgcn_permlane32_swap(pw[0][i], pw[1][i], false, false);
    xs[i] = r[0];
    ys[i] = r[1];
  }
  unsigned* dst = (unsigned*)(Ao + (size_t)(qrow0 + h * 32) * D_ + head * DH_);
  typedef __attribute__((ext_vector_type(4), aligned(4))) unsigned u32x4a;
  *(u32x4a*)dst = (u32x4a){xs[0], xs[1], ys[0], ys[1]};
  *(u32x4a*)(dst + 4) = (u32x4a){xs[2], xs[3], ys[2], ys[3]};
  dst[8] = xs[4];
}

constexpr int K2_STAGE = 16384 + 18432;
DI void down_block(int bid, const u16* __restrict__ Ah, const u16* __restrict__ wd, u16* __restrict__ part,
                   char* lds) {
  const int tid = threadIdx.x, lane = tid & 63;
  const int wv = __builtin_amdgcn_readfirstlane(tid >> 6);
  const int mt_blk = (bid & 7) * 2 + ((bid >> 3) & 1), rest = bid >> 4;
  const int nt_blk = rest & 3, sp = rest >> 2;
  const int m0 = mt_blk * 128, n0 = nt_blk * 144;
  f32x4 acc[9][2] = {};
  const char* pa = (const char*)Ah + ((size_t)(mt_blk * 8) * 144 + sp * 36) * 1024;
  const u16* pb = wd + (size_t)sp * 1152 * D_ + n0;
  const unsigned l0 = lds_addr(lds);
  const unsigned aof = (unsigned)(wv * 2 * 144 * 1024 + lane * 16);
  unsigned bof[5];
#pragma unroll
  for (int i = 0; i < 5; ++i) {
    int u = (i < 4 ? i * 4 + wv : 16 + (wv & 1)) * 64 + lane;
    int st = u >> 4, kblk = st / 9, nblk = st - kblk * 9;
    int rr = ((u >> 1) & 7) ^ ((kblk & 1) << 2);
    int k = (kblk >> 2) * 32 + (rr >> 2) * 16 + (kblk & 3) * 4 + (rr & 3);
    bof[i] = (unsigned)(k * D_ + nblk * 16 + (u & 1) * 8) * 2u;
  }
  auto piece = [&](int i, int kt, unsigned buf) {
    if (i < 4) glds16(pa + kt * 2048 + ((i >> 1) * 144 + (i & 1)) * 1024, aof, buf + (wv * 4 + i) * 1024);
    else if (i < 8) glds16(pb + (size_t)kt * 64 * D_, bof[i - 4], buf + 16384 + ((i - 4) * 4 + wv) * 1024);
    else if (wv < 2) glds16(pb + (size_t)kt * 64 * D_, bof[4], buf + 16384 + (16 + wv) * 1024);
  };
  const int g = lane >> 4;
  const int aoff0 = wv * 4096 + lane * 16, aoff1 = aoff0 + 1024;
  const int boff = 16384 + g * 9 * 256 + ((lane & 15) >> 2) * 32 + (lane & 3) * 8;
  const int hlo = (g & 1) << 7, hhi = hlo ^ 128;
#pragma unroll
  for (int i = 0; i < 9; ++i) piece(i, 0, l0);
  for (int kt = 0; kt < 18; ++kt) {
    waitv<0>();
    BARRIER();
    const char* cur = lds + (kt & 1) * K2_STAGE;
    const unsigned dst = l0 + ((kt + 1) & 1) * K2_STAGE;
    const bool more = kt + 1 < 18;
    bf16x8 af[2][2];
#pragma unroll
    for (int mt = 0; mt < 2; ++mt) {
      af[0][mt] = *(const bf16x8*)(cur + aoff0 + mt * 2048);
      af[1][mt] = *(const bf16x8*)(cur + aoff1 + mt * 2048);
    }
    bf16x8 bw0 = bfrag2(cur + boff + hlo, cur + boff + hhi);
    bf16x8 bw1 = bfrag2(cur + boff + 256 + hlo, cur + boff + 256 + hhi);
#pragma unroll
    for (int q = 0; q < 18; ++q) {
      bf16x8 b = (q & 1) ? bw1 : bw0;
      if (q + 2 < 18) {
        const int q2 = q + 2, o2 = ((q2 / 9) * 36 + q2 % 9) * 256;
        bf16x8 nb = bfrag2(cur + boff + o2 + hlo, cur + boff + o2 + hhi);
        if (q & 1) bw1 = nb; else bw0 = nb;
      }
      if (q < 9 && more) piece(q, kt + 1, dst);
#pragma unroll
      for (int mt = 0; mt < 2; ++mt)
        acc[q % 9][mt] = __builtin_amdgcn_mfma_f32_16x16x32_bf16(b, af[q / 9][mt], acc[q % 9][mt], 0, 0, 0);
      SBAR();
    }
  }
  u16* dst = part + ((size_t)((sp * 64 + mt_blk * 4 + wv) * 36 + nt_blk * 9) * 64 + lane) * 8;
#pragma unroll
  for (int nt = 0; nt < 9; ++nt)
    *(u32x4*)(dst + nt * 512) = (u32x4){pk2(acc[nt][0][0], acc[nt][0][1]), pk2(acc[nt][0][2], acc[nt][0][3]),
                                        pk2(acc[nt][1][0], acc[nt][1][1]), pk2(acc[nt][1][2], acc[nt][1][3])};
}

__global__ __launch_bounds__(256, 2) void k_mid(const u16* __restrict__ qkvp, const float* __restrict__ gate,
                                                u16* __restrict__ Ao, const u16* __restrict__ Ah,
                                                const u16* __restrict__ wd, u16* __restrict__ part) {
  __shared__ __attribute__((aligned(16))) char lds[2 * K2_STAGE];
  const int bid = blockIdx.x;
  if (bid < 256) attn_block(bid, qkvp, gate, Ao, lds);
  else down_block(bid - 256, Ah, wd, part, lds);
}

constexpr int K3_STAGE = 4096 + 6144;
__global__ __launch_bounds__(256) void k_out(const u16* __restrict__ Ao, const u16* __restrict__ wo,
                                             const u16* __restrict__ part, u16* __restrict__ out) {
  __shared__ __attribute__((aligned(16))) char lds[15 * K3_STAGE];
  const int tid = threadIdx.x, lane = tid & 63;
  const int wv = __builtin_amdgcn_readfirstlane(tid >> 6), wm = wv >> 1, wn = wv & 1;
  const int nt_blk = blockIdx.x % 6, mt_blk = blockIdx.x / 6;
  const int m0 = mt_blk * 64, n0 = nt_blk * 96;
  f32x4 acc[3][2] = {};
  const u16* pa = Ao + (size_t)m0 * D_;
  const u16* pb = wo + n0;
  const unsigned aof0 = aseg_off(wv * 64 + lane, D_);
  const unsigned bof0 = bseg_off<6>(wv * 64 + lane, D_), bof1 = bseg_off<6>((4 + (wv & 1)) * 64 + lane, D_);
  auto piece = [&](int i, int kt, unsigned buf) {
    if (i == 0) glds16(pa + kt * 32, aof0, buf + wv * 1024);
    else if (i == 1) glds16(pb + (size_t)kt * 32 * D_, bof0, buf + 4096 + wv * 1024);
    else if (wv < 2) glds16(pb + (size_t)kt * 32 * D_, bof1, buf + 4096 + (4 + wv) * 1024);
  };
  auto waitL = [&](auto n) { if (wv < 2) waitv<9 * decltype(n)::value>(); else waitv<6 * decltype(n)::value>(); };
  auto bar = [&]() { BARRIER(); };
  const int g = lane >> 4;
  const int aoff = (wm * 32 + (lane & 15)) * 64 + ((g ^ ((-(lane >> 2)) & 3)) << 4);
  const int boff = 4096 + (g * 6 + wn * 3) * 256 + ((lane & 15) >> 2) * 32 + (lane & 3) * 8;
  const int hlo = (g & 1) << 7, hhi = hlo ^ 128;
  auto ldA = [&](const char* st, int mt) { return *(const bf16x8*)(st + aoff + mt * 1024); };
  auto ldB = [&](const char* st, int nt) { return bfrag2(st + boff + nt * 256 + hlo, st + boff + nt * 256 + hhi); };
  auto extra = [&](int, int, unsigned) {};
  u32x4 pv[3][4];
  auto extra0 = [&]() {
#pragma unroll
    for (int nt = 0; nt < 3; ++nt)
#pragma unroll
      for (int sp = 0; sp < 4; ++sp)
        pv[nt][sp] = __builtin_nontemporal_load(
            (const u32x4*)(part + ((size_t)((sp * 64 + mt_blk * 2 + wm) * 36 + nt_blk * 6 + wn * 3 + nt) * 64 + lane) * 8));
  };
  gemm_loop<2, 3, 3, K3_STAGE, 3, 5>(6, lds, acc, ldA, ldB, piece, waitL, extra, bar, extra0);
#pragma unroll
  for (int nt = 0; nt < 3; ++nt)
#pragma unroll
    for (int mt = 0; mt < 2; ++mt) {
      size_t off = (size_t)(m0 + wm * 32 + mt * 16 + (lane & 15)) * D_ + n0 + wn * 48 + nt * 16 + 4 * (lane >> 4);
#pragma unroll
      for (int sp = 0; sp < 4; ++sp) {
        u32x2 w = {pv[nt][sp][mt * 2], pv[nt][sp][mt * 2 + 1]};
        acc[nt][mt] += (f32x4){__uint_as_float(w[0] << 16), __uint_as_float(w[0] & 0xffff0000u), __uint_as_float(w[1] << 16),
                               __uint_as_float(w[1] & 0xffff0000u)};
      }
      u32x2 v = {pk2(acc[nt][mt][0], acc[nt][mt][1]), pk2(acc[nt][mt][2], acc[nt][mt][3])};
      *(u32x2*)(out + off) = v;
    }
}

extern "C" void kernel_launch(void* const* d_in, const int* in_sizes, int n_in, void* d_out, int out_size,
                              void* d_ws, size_t ws_size, hipStream_t stream) {
  const u16* x = (const u16*)d_in[0];
  const u16* wqkv = (const u16*)d_in[1];
  const u16* wo = (const u16*)d_in[2];
  const u16* wg = (const u16*)d_in[4];
  const u16* wu = (const u16*)d_in[5];
  const u16* wd = (const u16*)d_in[6];
  const u16* gw = (const u16*)d_in[7];
  char* ws = (char*)d_ws;
  u16* qkvp = (u16*)ws;
  float* gate = (float*)(ws + (size_t)16 * 1024 * 1024);
  u16* Ao = (u16*)(ws + (size_t)17 * 1024 * 1024);
  u16* Ah = (u16*)(ws + (size_t)20 * 1024 * 1024);
  u16* part = (u16*)(ws + (size_t)48 * 1024 * 1024);
  k_proj<<<256, 512, 0, stream>>>(x, wqkv, wg, wu, gw, qkvp, gate, Ah);
  k_mid<<<512, 256, 0, stream>>>(qkvp, gate, Ao, Ah, wd, part);
  k_out<<<192, 256, 0, stream>>>(Ao, wo, part, (u16*)d_out);
}
```

```cpp
#include <hip/hip_runtime.h>
#include <hip/hip_bf16.h>
#include <type_traits>

typedef __attribute__((ext_vector_type(8))) short bf16x8;
typedef __attribute__((ext_vector_type(4))) short s16x4;
typedef __attribute__((ext_vector_type(4))) float f32x4;
typedef __attribute__((ext_vector_type(2))) float f32x2;
typedef __attribute__((ext_vector_type(16))) float f32x16;
typedef __attribute__((ext_vector_type(4))) unsigned u32x4;
typedef __attribute__((ext_vector_type(2))) unsigned u32x2;
typedef __attribute__((ext_vector_type(2))) __bf16 bf16x2_t;
typedef unsigned short u16;

#define DI __device__ __forceinline__
#define LDSP(T) __attribute__((address_space(3))) T

constexpr int S_ = 2048, D_ = 576, H_ = 32, DH_ = 18, F_ = 4608;

DI unsigned pk2(float lo, float hi) {
  f32x2 v = {lo, hi};
  bf16x2_t b = __builtin_convertvector(v, bf16x2_t);
  return __builtin_bit_cast(unsigned, b);
}
DI float rbf(float x) { return __uint_as_float(pk2(x, 0.f) << 16); }
DI float dot2bf(unsigned a, unsigned b, float c) {
  return __builtin_amdgcn_fdot2_f32_bf16(__builtin_bit_cast(bf16x2_t, a), __builtin_bit_cast(bf16x2_t, b), c, false);
}

DI void glds16(const void* sbase, unsigned voff, unsigned lds_base) {
  unsigned sv;
  asm volatile("s_mov_b32 %0, m0\n\ts_mov_b32 m0, %3\n\ts_nop 0\n\tglobal_load_lds_dwordx4 %1, %2\n\ts_mov_b32 m0, %0"
               : "=&s"(sv) : "v"(voff), "s"(sbase), "s"(__builtin_amdgcn_readfirstlane(lds_base)) : "memory");
}
DI unsigned lds_addr(const char* p) { return (unsigned)(size_t)(LDSP(const char)*)p; }
DI s16x4 trread(const char* p) { return __builtin_amdgcn_ds_read_tr16_b64_v4i16((LDSP(s16x4)*)p); }
template <int N> DI void waitv() { asm volatile("s_waitcnt vmcnt(%0)" ::"n"(N) : "memory"); }
#define BARRIER() do { asm volatile("s_waitcnt lgkmcnt(0)" ::: "memory"); __builtin_amdgcn_s_barrier(); asm volatile("" ::: "memory"); } while (0)

#define SBAR() __builtin_amdgcn_sched_barrier(0)
template <int NBs>
DI unsigned bseg_off(int u, int ld) {
  int st = u >> 4, kblk = st / NBs, nblk = st - kblk * NBs;
  int rr = (u >> 1) & 7, hf = u & 1;
  int k = kblk * 8 + (rr ^ ((kblk & 1) << 2));
  return (unsigned)(k * ld + nblk * 16 + hf * 8) * 2u;
}
DI unsigned aseg_off(int u, int lda) {
  int row = u >> 2, ch = (u & 3) ^ ((-(row >> 2)) & 3);
  return (unsigned)(row * lda + ch * 8) * 2u;
}
DI bf16x8 bfrag2(const char* lo, const char* hi) {
  s16x4 a = trread(lo), b = trread(hi);
  return __builtin_shufflevector(a, b, 0, 1, 2, 3, 4, 5, 6, 7);
}

template <int N> using IC = std::integral_constant<int, N>;
template <int MT, int NT, int LMAX, int STAGE, int KS, int NR, class LdA, class LdB, class Piece, class WaitL, class Extra, class Bar, class Pre>
DI void gemm_loop(int nsup, char* lds, f32x4 (&acc)[NT][MT], LdA ldA, LdB ldB, Piece piece, WaitL waitL, Extra extra, Bar bar, Pre pre) {
  static_assert(NT >= MT + 1 && NT >= LMAX, "schedule");
  constexpr int SUP = KS * STAGE;
  const unsigned l0 = lds_addr(lds);
#pragma unroll
  for (int s = 0; s < (NR - 1) * KS; ++s)
#pragma unroll
    for (int i = 0; i < LMAX; ++i) piece(i, s, l0 + s * STAGE);
  pre();
  waitL(IC<NR - 2>{});
  bar();
  bf16x8 af[MT], bw0, bw1;
#pragma unroll
  for (int mt = 0; mt < MT; ++mt) af[mt] = ldA(lds, mt);
  bw0 = ldB(lds, 0);
  bw1 = ldB(lds, 1);
  int cb = 0;
  auto body = [&](int j, auto wn_, auto more_) {
    constexpr bool more = decltype(more_)::value;
    waitL(wn_);
    bar();
    const int nb_ = cb + 1 == NR ? 0 : cb + 1, pb_ = cb == 0 ? NR - 1 : cb - 1;
    const char* base = lds + cb * SUP;
    const char* nbase = lds + nb_ * SUP;
    const unsigned dst = l0 + pb_ * SUP;
#pragma unroll
    for (int ks = 0; ks < KS; ++ks) {
      const char* cur = base + ks * STAGE;
      const char* nx = ks + 1 < KS ? base + (ks + 1) * STAGE : nbase;
      bf16x8 an[MT];
#pragma unroll
      for (int nt = 0; nt < NT; ++nt) {
        bf16x8 b = (nt & 1) ? bw1 : bw0;
        bf16x8 nb = nt + 2 < NT ? ldB(cur, nt + 2) : ldB(nx, nt + 2 - NT);
        if (nt & 1) bw1 = nb; else bw0 = nb;
        if (nt >= NT - 1 - MT && nt < NT - 1) an[nt - (NT - 1 - MT)] = ldA(nx, nt - (NT - 1 - MT));
        if (nt < LMAX && more) piece(nt, (j + NR - 1) * KS + ks, dst + ks * STAGE);
#pragma unroll
        for (int mt = 0; mt < MT; ++mt) acc[nt][mt] = __builtin_amdgcn_mfma_f32_16x16x32_bf16(b, af[mt], acc[nt][mt], 0, 0, 0);
        if (more) extra(nt, (j + NR - 1) * KS + ks, dst + ks * STAGE - l0);
        SBAR();
      }
#pragma unroll
      for (int mt = 0; mt < MT; ++mt) af[mt] = an[mt];
      if (NT & 1) { bf16x8 t = bw0; bw0 = bw1; bw1 = t; }
    }
    cb = nb_;
  };
  for (int j = 0; j < nsup - (NR - 1); ++j) body(j, IC<NR - 3>{}, IC<1>{});
#pragma unroll 1
  for (int j = nsup - (NR - 1); j < nsup; ++j) body(j, IC<0>{}, IC<0>{});
}

constexpr float QSCALE = 0.23570226039551584f * 1.4426950408889634f;
constexpr int K1_STAGE = 16384 + 9216 + 9216 + 4096;

__global__ __launch_bounds__(512) void k_proj(const u16* __restrict__ x, const u16* __restrict__ wqkv,
                                              const u16* __restrict__ wg, const u16* __restrict__ wu,
                                              const u16* __restrict__ gate_w, u16* __restrict__ qkvp,
                                              float* __restrict__ gate, u16* __restrict__ Ah) {
  __shared__ __attribute__((aligned(16))) char lds[4 * K1_STAGE + 1152];
  const int tid = threadIdx.x, lane = tid & 63;
  const int wv = __builtin_amdgcn_readfirstlane(tid >> 6), wm = wv >> 1, wn = wv & 1;
  const int bid = blockIdx.x;
  const int nt_blk = (bid & 7) * 4 + ((bid >> 3) & 3), mt_blk = bid >> 5;
  const int m0 = mt_blk * 256, f0 = nt_blk * 144;
  const int coff = nt_blk == 31 ? 2 : 0;
  const int c0 = nt_blk * 54 - coff;
  char* gwl = lds + 4 * K1_STAGE;

  f32x4 acc[11][4] = {};
  const u16* xa = x + (size_t)m0 * D_;
  const unsigned aof0 = aseg_off(wv * 64 + lane, D_), aof1 = aseg_off((8 + wv) * 64 + lane, D_);
  unsigned bof[3];
  const u16* bsrc[3];
  int bld[3];
#pragma unroll
  for (int i = 0; i < 3; ++i) {
    int jj = i * 8 + wv;
    if (jj >= 22) jj -= 6;
    if (jj < 18) {
      int j = jj < 9 ? jj : jj - 9;
      bof[i] = bseg_off<9>(j * 64 + lane, F_);
      bsrc[i] = (jj < 9 ? wg : wu) + f0;
      bld[i] = F_;
    } else {
      bof[i] = bseg_off<4>(((jj - 18) & 3) * 64 + lane, 3 * D_);
      bsrc[i] = wqkv + c0;
      bld[i] = 3 * D_;
    }
  }
  const bool padlane = wv >= 2 && wv < 6 && (lane & 1) && (lane >> 4) == 3;
  auto piece = [&](int i, int kt, unsigned buf) {
    if (i == 0) glds16(xa + kt * 32, aof0, buf + wv * 1024);
    else if (i == 1) glds16(xa + kt * 32, aof1, buf + (8 + wv) * 1024);
    else if (i < 4) glds16(bsrc[i - 2] + (size_t)kt * 32 * bld[i - 2], bof[i - 2], buf + 16384 + ((i - 2) * 8 + wv) * 1024);
    else if (!padlane) glds16(bsrc[2] + (size_t)kt * 32 * bld[2], bof[2], buf + 16384 + (16 + (wv < 6 ? wv : wv - 6)) * 1024);
  };
  const int gk = lane & 31, gkb = gk >> 3;
  const int gdst = 16384 + 18432 + (gkb * 4 + 3) * 256 + ((gk & 7) ^ ((gkb & 1) << 2)) * 32 + 16;
  auto gfill = [&](int kt, unsigned off) {
    if (wv == 6 && lane < 32) {
      u32x4 v = {(unsigned)*(const u16*)(gwl + (kt * 32 + gk) * 2), 0u, 0u, 0u};
      *(u32x4*)(lds + off + gdst) = v;
    }
  };
  auto pre = [&]() {
    if (wv == 6) {
      waitv<15>();
      gfill(0, 0); gfill(1, K1_STAGE); gfill(2, 2 * K1_STAGE);
    }
  };
  if (wv == 6) {
    glds16(gate_w, lane * 16u, lds_addr(gwl));
    if (lane < 8) glds16(gate_w + 512, lane * 16u, lds_addr(gwl) + 1024);
  }
  auto waitL = [&](auto n) { waitv<5 * decltype(n)::value>(); };
  auto bar = [&]() { BARRIER(); };
  const int g = lane >> 4;
  const int bl = ((lane & 15) >> 2) * 32 + (lane & 3) * 8;
  const int aoff = (wm * 64 + (lane & 15)) * 64 + ((g ^ ((-(lane >> 2)) & 3)) << 4);
  const int bgu = 16384 + wn * 9216 + g * 9 * 256 + bl;
  const int bqq = 16384 + 18432 + (g * 4 + wn * 2) * 256 + bl;
  const int hlo = (g & 1) << 7, hhi = hlo ^ 128;
  auto ldA = [&](const char* st, int mt) { return *(const bf16x8*)(st + aoff + mt * 1024); };
  auto ldB = [&](const char* st, int nt) {
    const char* bp = st + (nt < 9 ? bgu + nt * 256 : bqq + (nt - 9) * 256);
    return bfrag2(bp + hlo, bp + hhi);
  };
  auto extra = [&](int nt, int kt, unsigned off) { if (nt == 5) gfill(kt, off); };
  gemm_loop<4, 11, 5, K1_STAGE, 1, 4>(18, lds, acc, ldA, ldB, piece, waitL, extra, bar, pre);
  BARRIER();
  int tz = threadIdx.x;
  asm volatile("" : "+v"(tz));
  const int lz = tz & 63, gz = lz >> 4, tk = lz & 15;

  float* G = (float*)(lds + 73728);
  const int hg0 = nt_blk * 3;
  {
    char* xs = lds + wv * 9216 + lz * 8;
    if (wn == 1) {
#pragma unroll
      for (int nt = 0; nt < 9; ++nt)
#pragma unroll
        for (int m2 = 0; m2 < 2; ++m2) { f32x4 t = acc[nt][m2]; acc[nt][m2] = acc[nt][2 + m2]; acc[nt][2 + m2] = t; }
    }
#pragma unroll
    for (int nt = 0; nt < 9; ++nt)
#pragma unroll
      for (int m2 = 0; m2 < 2; ++m2) {
        f32x4 v = acc[nt][2 + m2];
        *(u32x2*)(xs + (nt * 2 + m2) * 512) = (u32x2){pk2(v[0], v[1]), pk2(v[2], v[3])};
      }
    if (wn == 1 && gz == 2) {
#pragma unroll
      for (int mt = 0; mt < 4; ++mt) {
        float gv = rbf(1.f / (1.f + __expf(-rbf(acc[10][mt][0]))));
        G[wm * 64 + mt * 16 + tk] = gv;
        if (nt_blk == 0) gate[m0 + wm * 64 + mt * 16 + tk] = gv;
      }
    }
  }
  __syncthreads();
  {
    const char* xr = lds + (wv ^ 1) * 9216 + lz * 8;
    u32x2 pv[9][2];
#pragma unroll
    for (int nt = 0; nt < 9; ++nt)
#pragma unroll
      for (int m2 = 0; m2 < 2; ++m2) pv[nt][m2] = *(const u32x2*)(xr + (nt * 2 + m2) * 512);
    float og[2];
#pragma unroll
    for (int m2 = 0; m2 < 2; ++m2) og[m2] = rbf(1.f - G[wm * 64 + (wn * 2 + m2) * 16 + tk]);
    asm volatile("s_waitcnt lgkmcnt(0)" ::: "memory");
    __builtin_amdgcn_s_barrier();
#pragma unroll
    for (int nt = 0; nt < 9; ++nt)
#pragma unroll
      for (int m2 = 0; m2 < 2; ++m2) {
        f32x4 v = acc[nt][m2];
        u32x2 uv = pv[nt][m2];
        float h0 = v[0] * og[m2] * __uint_as_float(uv[0] << 16);
        float h1 = v[1] * og[m2] * __uint_as_float(uv[0] & 0xffff0000u);
        float h2 = v[2] * og[m2] * __uint_as_float(uv[1] << 16);
        float h3 = v[3] * og[m2] * __uint_as_float(uv[1] & 0xffff0000u);
        pv[nt][m2] = (u32x2){pk2(h0, h1), pk2(h2, h3)};
      }
#pragma unroll
    for (int m2 = 0; m2 < 2; ++m2) {
      char* hb = (char*)Ah + ((size_t)((mt_blk * 16 + wm * 4 + wn * 2 + m2) * 144 + ((nt_blk * 9) >> 1)) * 64 + lz) * 16;
      if ((nt_blk & 1) == 0) {
#pragma unroll
        for (int i = 0; i < 4; ++i)
          *(u32x4*)(hb + i * 1024) = (u32x4){pv[2 * i][m2][0], pv[2 * i][m2][1], pv[2 * i + 1][m2][0], pv[2 * i + 1][m2][1]};
        *(u32x2*)(hb + 4096) = pv[8][m2];
      } else {
        *(u32x2*)(hb + 8) = pv[0][m2];
#pragma unroll
        for (int i = 0; i < 4; ++i)
          *(u32x4*)(hb + (i + 1) * 1024) =
              (u32x4){pv[2 * i + 1][m2][0], pv[2 * i + 1][m2][1], pv[2 * i + 2][m2][0], pv[2 * i + 2][m2][1]};
      }
    }
  }
  {
    char* img = lds;
#pragma unroll
    for (int t2 = 0; t2 < 2; ++t2)
#pragma unroll
      for (int r = 0; r < 4; r += 2) {
        int c = wn * 32 + t2 * 16 + 4 * gz + r - coff;
        if (c >= 0 && c < 54) {
          int hl = c / 18, d = c - hl * 18;
          int which = (hg0 + hl) >> 5;
          float sc = which == 0 ? QSCALE : 1.f;
#pragma unroll
          for (int mt = 0; mt < 4; ++mt) {
            int tok = wm * 64 + mt * 16 + tk;
            unsigned v = pk2(acc[9 + t2][mt][r] * sc, acc[9 + t2][mt][r + 1] * sc);
            if (which == 2) {
              char* q = img + hl * 16384 + (tok >> 5) * 2048 + ((((tok >> 4) & 1) * 2 + ((tok >> 2) & 1)) * 32 + d) * 16 +
                        ((tok & 3) + 4 * ((tok >> 3) & 1)) * 2;
              *(u16*)q = (u16)v;
              *(u16*)(q + 16) = (u16)(v >> 16);
            } else {
              int ch = d >> 3;
              if (which == 1) ch ^= (tok >> 2) & 3;
              *(unsigned*)(img + (hl * 256 + tok) * 64 + ch * 16 + (d & 7) * 2) = v;
            }
          }
        }
      }
    for (int i = tz; i < 768; i += 512) {
      int which = (hg0 + (i >> 8)) >> 5;
      if (which == 2) {
        char* grp = img + (i >> 8) * 16384 + ((i & 255) >> 3) * 512;
        int d0 = 18 + (i & 7) * 2;
        unsigned one = d0 == 18 ? 0x3f803f80u : 0u;
        if (d0 < 32) {
          *(u32x4*)(grp + d0 * 16) = (u32x4){one, one, one, one};
          *(u32x4*)(grp + d0 * 16 + 16) = (u32x4){0u, 0u, 0u, 0u};
        }
      } else {
        int sw = which == 1 ? (i >> 2) & 3 : 0;
        char* row = img + i * 64;
        *(unsigned*)(row + (2 ^ sw) * 16 + 4) = 0u;
        *(u32x2*)(row + (2 ^ sw) * 16 + 8) = (u32x2){0u, 0u};
        *(u32x4*)(row + (3 ^ sw) * 16) = (u32x4){0u, 0u, 0u, 0u};
      }
    }
  }
  __syncthreads();
#pragma unroll
  for (int i = 0; i < 6; ++i) {
    int u = i * 512 + tz;
    int hl = u >> 10, rem = u & 1023;
    u32x4 v = *(const u32x4*)(lds + u * 16);
    *(u32x4*)(qkvp + ((size_t)(nt_blk * 3 + hl) * S_ + m0) * 32 + rem * 8) = v;
  }
}

DI void attn_stage(const u16* __restrict__ Kh, const u16* __restrict__ Vh, int c, unsigned buf, int wv, int lane) {
  glds16(Kh + (size_t)c * 4096, (wv * 64 + lane) * 16u, buf + wv * 1024);
  glds16(Vh + (size_t)c * 4096, (wv * 64 + lane) * 16u, buf + 8192 + wv * 1024);
}

DI float halfswap_max(float m) {
  auto rr = __builtin_amdgcn_permlane32_swap(__float_as_uint(m), __float_as_uint(m), false, false);
  return fmaxf(__uint_as_float(rr[0]), __uint_as_float(rr[1]));
}
DI float max16(const f32x16& s) {
  float mx = s[0];
#pragma unroll
  for (int r = 1; r < 16; ++r) mx = fmaxf(mx, s[r]);
  return halfswap_max(mx);
}
#define MFMA32(a, b, c) __builtin_amdgcn_mfma_f32_32x32x16_bf16((a), (b), (c), 0, 0, 0)

DI void exp_pack(const f32x16& s, bf16x8& p0, bf16x8& p1) {
  u32x4 a, b;
#pragma unroll
  for (int r = 0; r < 4; ++r) a[r] = pk2(__builtin_amdgcn_exp2f(s[2 * r]), __builtin_amdgcn_exp2f(s[2 * r + 1]));
#pragma unroll
  for (int r = 0; r < 4; ++r) b[r] = pk2(__builtin_amdgcn_exp2f(s[8 + 2 * r]), __builtin_amdgcn_exp2f(s[9 + 2 * r]));
  p0 = __builtin_bit_cast(bf16x8, a);
  p1 = __builtin_bit_cast(bf16x8, b);
}

#define EX2(x) __builtin_amdgcn_exp2f(x)
#define PHASE(S, PA, PB, M1, M2, M3, M4)                                                      \
  do {                                                                                        \
    M1; SBAR(); PA[0] = pk2(EX2(S[0]), EX2(S[1])); PA[1] = pk2(EX2(S[2]), EX2(S[3])); SBAR();  \
    M2; SBAR(); PA[2] = pk2(EX2(S[4]), EX2(S[5])); PA[3] = pk2(EX2(S[6]), EX2(S[7])); SBAR();  \
    M3; SBAR(); PB[0] = pk2(EX2(S[8]), EX2(S[9])); PB[1] = pk2(EX2(S[10]), EX2(S[11])); SBAR(); \
    M4; SBAR(); PB[2] = pk2(EX2(S[12]), EX2(S[13])); PB[3] = pk2(EX2(S[14]), EX2(S[15])); SBAR(); \
  } while (0)
#define BC8(x) __builtin_bit_cast(bf16x8, x)
DI void attn_fast(const u16* __restrict__ Kh, const u16* __restrict__ Vh, bf16x8 q0, bf16x8 q1, char* lds, f32x16& o,
                  int wv, int lane) {
  const int r32 = lane & 31, h = lane >> 5;
  const int koff0 = r32 * 64 + ((h ^ ((r32 >> 2) & 3)) << 4);
  const int koff1 = r32 * 64 + (((2 + h) ^ ((r32 >> 2) & 3)) << 4);
  const int voff = 8192 + lane * 16;
  const unsigned l0 = lds_addr(lds);
  f32x16 negm, sa, sb;
#pragma unroll
  for (int r = 0; r < 16; ++r) { o[r] = 0.f; negm[r] = 0.f; }
  waitv<4>();
  BARRIER();
  {
    bf16x8 k0 = *(const bf16x8*)(lds + koff0), k1 = *(const bf16x8*)(lds + koff1);
    f32x16 s = MFMA32(k0, q0, negm);
    s = MFMA32(k1, q1, s);
    float mx = max16(s);
#pragma unroll
    for (int r = 0; r < 16; ++r) { negm[r] = -mx; sa[r] = s[r] - mx; }
  }
  u32x4 pa0, pa1, pb0 = {0u, 0u, 0u, 0u}, pb1 = {0u, 0u, 0u, 0u};
  bf16x8 vb0 = {0, 0, 0, 0, 0, 0, 0, 0}, vb1 = {0, 0, 0, 0, 0, 0, 0, 0};
  for (int c = 0; c < 16; ++c) {
    if (c + 2 < 16) waitv<2>(); else waitv<0>();
    BARRIER();
    if (c + 3 < 16) attn_stage(Kh, Vh, c + 3, l0 + ((c + 3) & 3) * 16384, wv, lane);
    const char* buf = lds + (c & 3) * 16384;
    const char* bufn = lds + ((c + 1) & 3) * 16384;
#pragma unroll
    for (int t = 0; t < 4; t += 2) {
      bf16x8 k0 = *(const bf16x8*)(buf + (t + 1) * 2048 + koff0), k1 = *(const bf16x8*)(buf + (t + 1) * 2048 + koff1);
      bf16x8 va0 = *(const bf16x8*)(buf + t * 2048 + voff), va1 = *(const bf16x8*)(buf + t * 2048 + voff + 1024);
      SBAR();
      PHASE(sa, pa0, pa1, o = MFMA32(vb0, BC8(pb0), o), sb = MFMA32(k0, q0, negm), o = MFMA32(vb1, BC8(pb1), o),
            sb = MFMA32(k1, q1, sb));
      const char* kb = t + 2 < 4 ? buf + (t + 2) * 2048 : bufn;
      k0 = *(const bf16x8*)(kb + koff0);
      k1 = *(const bf16x8*)(kb + koff1);
      vb0 = *(const bf16x8*)(buf + (t + 1) * 2048 + voff);
      vb1 = *(const bf16x8*)(buf + (t + 1) * 2048 + voff + 1024);
      SBAR();
      PHASE(sb, pb0, pb1, o = MFMA32(va0, BC8(pa0), o), sa = MFMA32(k0, q0, negm), o = MFMA32(va1, BC8(pa1), o),
            sa = MFMA32(k1, q1, sa));
    }
  }
  o = MFMA32(vb0, BC8(pb0), o);
  o = MFMA32(vb1, BC8(pb1), o);
}

DI void attn_safe(const u16* __restrict__ Kh, const u16* __restrict__ Vh, bf16x8 q0, bf16x8 q1, char* lds, f32x16& o,
                  int wv, int lane) {
  const int r32 = lane & 31, h = lane >> 5;
  const int koff0 = r32 * 64 + ((h ^ ((r32 >> 2) & 3)) << 4);
  const int koff1 = r32 * 64 + (((2 + h) ^ ((r32 >> 2) & 3)) << 4);
  const int voff = 8192 + lane * 16;
  const unsigned l0 = lds_addr(lds);
  f32x16 zero;
  float m = -INFINITY;
#pragma unroll
  for (int r = 0; r < 16; ++r) { o[r] = 0.f; zero[r] = 0.f; }
  for (int c = 0; c < 16; ++c) {
    BARRIER();
    attn_stage(Kh, Vh, c, l0, wv, lane);
    waitv<0>();
    BARRIER();
    for (int t = 0; t < 4; ++t) {
      const char* kb = lds + t * 2048;
      bf16x8 k0 = *(const bf16x8*)(kb + koff0), k1 = *(const bf16x8*)(kb + koff1);
      bf16x8 va = *(const bf16x8*)(kb + voff), vb = *(const bf16x8*)(kb + voff + 1024);
      f32x16 s = MFMA32(k0, q0, zero);
      s = MFMA32(k1, q1, s);
      float mn = fmaxf(m, max16(s));
      float al = __builtin_amdgcn_exp2f(m - mn);
      m = mn;
#pragma unroll
      for (int r = 0; r < 16; ++r) { o[r] *= al; s[r] -= mn; }
      bf16x8 p0, p1;
      exp_pack(s, p0, p1);
      o = MFMA32(va, p0, o);
      o = MFMA32(vb, p1, o);
    }
  }
}

DI void attn_block(int bid, const u16* __restrict__ qkvp, const float* __restrict__ gate, u16* __restrict__ Ao,
                   char* lds) {
  const int tid = threadIdx.x, lane = tid & 63, r32 = lane & 31, h = lane >> 5;
  const int wv = __builtin_amdgcn_readfirstlane(tid >> 6);
  const int head = (bid & 7) * 4 + ((bid >> 3) & 3), qb = bid >> 5;
  const u16* Qh = qkvp + (size_t)head * (S_ * 32);
  const u16* Kh = qkvp + (size_t)(H_ + head) * (S_ * 32);
  const u16* Vh = qkvp + (size_t)(2 * H_ + head) * (S_ * 32);
  const int qrow = qb * 256 + wv * 32 + r32;
  {
    const unsigned l0 = lds_addr(lds);
    attn_stage(Kh, Vh, 0, l0, wv, lane);
    attn_stage(Kh, Vh, 1, l0 + 16384, wv, lane);
    attn_stage(Kh, Vh, 2, l0 + 32768, wv, lane);
  }
  bf16x8 q0 = *(const bf16x8*)(Qh + (size_t)qrow * 32 + h * 8);
  bf16x8 q1 = *(const bf16x8*)(Qh + (size_t)qrow * 32 + 16 + h * 8);
  float gq = gate[qrow];
  asm volatile("" ::"v"(q0), "v"(q1), "v"(gq));
  f32x16 o;
  attn_fast(Kh, Vh, q0, q1, lds, o, wv, lane);
  bool bad = false;
#pragma unroll
  for (int r = 0; r < 11; ++r) bad |= (r < 8 || h == 0) && !(fabsf(o[r]) < 1e30f);
  if (__syncthreads_or(bad)) attn_safe(Kh, Vh, q0, q1, lds, o, wv, lane);
  float l = __shfl(o[10], r32);
  float sc = gq / l;
  unsigned pw[5];
#pragma unroll
  for (int i = 0; i < 5; ++i) pw[i] = pk2(o[2 * i] * sc, o[2 * i + 1] * sc);
  auto r0 = __builtin_amdgcn_permlane32_swap(pw[0], pw[2], false, false);
  auto r1 = __builtin_amdgcn_permlane32_swap(pw[1], pw[3], false, false);
  unsigned* dst = (unsigned*)(Ao + (size_t)qrow * D_ + head * DH_);
  typedef __attribute__((ext_vector_type(4), aligned(4))) unsigned u32x4a;
  *(u32x4a*)(dst + h * 4) = (u32x4a){r0[0], r1[0], r0[1], r1[1]};
  if (h == 0) dst[8] = pw[4];
}

constexpr int K2_STAGE = 16384 + 18432;
DI void down_block(int bid, const u16* __restrict__ Ah, const u16* __restrict__ wd, u16* __restrict__ part,
                   char* lds) {
  const int tid = threadIdx.x, lane = tid & 63;
  const int wv = __builtin_amdgcn_readfirstlane(tid >> 6), wm = wv & 3, ks = wv >> 2;
  const int mt_blk = (bid & 7) * 2 + ((bid >> 3) & 1), rest = bid >> 4;
  const int nt_blk = rest & 3, sp = rest >> 2;
  const int n0 = nt_blk * 144;
  f32x4 acc[9][2] = {};
  const char* pa = (const char*)Ah + ((size_t)(mt_blk * 8) * 144 + sp * 36) * 1024;
  const u16* pb = wd + (size_t)sp * 1152 * D_ + n0;
  const unsigned l0 = lds_addr(lds);
  const unsigned aof = (unsigned)((wm * 2 * 144 + ks) * 1024 + lane * 16);
  unsigned bof[3];
#pragma unroll
  for (int i = 0; i < 3; ++i) {
    int u = (i < 2 ? i * 8 + wv : 16 + (wv & 1)) * 64 + lane;
    int st = u >> 4, kblk = st / 9, nblk = st - kblk * 9;
    int rr = ((u >> 1) & 7) ^ ((kblk & 1) << 2);
    int k = (kblk >> 2) * 32 + (rr >> 2) * 16 + (kblk & 3) * 4 + (rr & 3);
    bof[i] = (unsigned)(k * D_ + nblk * 16 + (u & 1) * 8) * 2u;
  }
  auto piece = [&](int i, int kt, unsigned buf) {
    if (i < 2) glds16(pa + kt * 2048 + i * 144 * 1024, aof, buf + (wm * 4 + i * 2 + ks) * 1024);
    else if (i < 4) glds16(pb + (size_t)kt * 64 * D_, bof[i - 2], buf + 16384 + ((i - 2) * 8 + wv) * 1024);
    else if (wv < 2) glds16(pb + (size_t)kt * 64 * D_, bof[2], buf + 16384 + (16 + wv) * 1024);
  };
  const int g = lane >> 4;
  const int aoff = wm * 4096 + ks * 1024 + lane * 16;
  const int boff = 16384 + (ks * 36 + g * 9) * 256 + ((lane & 15) >> 2) * 32 + (lane & 3) * 8;
  const int hlo = (g & 1) << 7, hhi = hlo ^ 128;
#pragma unroll
  for (int i = 0; i < 5; ++i) piece(i, 0, l0);
  for (int kt = 0; kt < 18; ++kt) {
    waitv<0>();
    BARRIER();
    const char* cur = lds + (kt & 1) * K2_STAGE;
    const unsigned dst = l0 + ((kt + 1) & 1) * K2_STAGE;
    const bool more = kt + 1 < 18;
    bf16x8 af0 = *(const bf16x8*)(cur + aoff), af1 = *(const bf16x8*)(cur + aoff + 2048);
    bf16x8 bw0 = bfrag2(cur + boff + hlo, cur + boff + hhi);
    bf16x8 bw1 = bfrag2(cur + boff + 256 + hlo, cur + boff + 256 + hhi);
#pragma unroll
    for (int q = 0; q < 9; ++q) {
      bf16x8 b = (q & 1) ? bw1 : bw0;
      if (q + 2 < 9) {
        bf16x8 nb = bfrag2(cur + boff + (q + 2) * 256 + hlo, cur + boff + (q + 2) * 256 + hhi);
        if (q & 1) bw1 = nb; else bw0 = nb;
      }
      if (q < 5 && more) piece(q, kt + 1, dst);
      acc[q][0] = __builtin_amdgcn_mfma_f32_16x16x32_bf16(b, af0, acc[q][0], 0, 0, 0);
      acc[q][1] = __builtin_amdgcn_mfma_f32_16x16x32_bf16(b, af1, acc[q][1], 0, 0, 0);
      SBAR();
    }
  }
  BARRIER();
  char* xch = lds + wm * 18432 + lane * 16;
  if (ks == 1) {
#pragma unroll
    for (int nt = 0; nt < 9; ++nt) {
      *(f32x4*)(xch + nt * 2048) = acc[nt][0];
      *(f32x4*)(xch + nt * 2048 + 1024) = acc[nt][1];
    }
  }
  __syncthreads();
  if (ks == 0) {
    u16* dst = part + ((size_t)((sp * 64 + mt_blk * 4 + wm) * 36 + nt_blk * 9) * 64 + lane) * 8;
#pragma unroll
    for (int nt = 0; nt < 9; ++nt) {
      f32x4 a0 = acc[nt][0] + *(const f32x4*)(xch + nt * 2048);
      f32x4 a1 = acc[nt][1] + *(const f32x4*)(xch + nt * 2048 + 1024);
      *(u32x4*)(dst + nt * 512) = (u32x4){pk2(a0[0], a0[1]), pk2(a0[2], a0[3]), pk2(a1[0], a1[1]), pk2(a1[2], a1[3])};
    }
  }
}

__global__ __launch_bounds__(512, 4) void k_mid(const u16* __restrict__ qkvp, const float* __restrict__ gate,
                                                u16* __restrict__ Ao, const u16* __restrict__ Ah,
                                                const u16* __restrict__ wd, u16* __restrict__ part) {
  __shared__ __attribute__((aligned(16))) char lds[73728];
  const int bid = blockIdx.x;
  if (bid < 256) attn_block(bid, qkvp, gate, Ao, lds);
  else down_block(bid - 256, Ah, wd, part, lds);
}

constexpr int K3_STAGE = 4096 + 6144;
__global__ __launch_bounds__(256) void k_out(const u16* __restrict__ Ao, const u16* __restrict__ wo,
                                             const u16* __restrict__ part, u16* __restrict__ out) {
  __shared__ __attribute__((aligned(16))) char lds[15 * K3_STAGE];
  const int tid = threadIdx.x, lane = tid & 63;
  const int wv = __builtin_amdgcn_readfirstlane(tid >> 6), wm = wv >> 1, wn = wv & 1;
  const int nt_blk = blockIdx.x % 6, mt_blk = blockIdx.x / 6;
  const int m0 = mt_blk * 64, n0 = nt_blk * 96;
  f32x4 acc[3][2] = {};
  const u16* pa = Ao + (size_t)m0 * D_;
  const u16* pb = wo + n0;
  const unsigned aof0 = aseg_off(wv * 64 + lane, D_);
  const unsigned bof0 = bseg_off<6>(wv * 64 + lane, D_), bof1 = bseg_off<6>((4 + (wv & 1)) * 64 + lane, D_);
  auto piece = [&](int i, int kt, unsigned buf) {
    if (i == 0) glds16(pa + kt * 32, aof0, buf + wv * 1024);
    else if (i == 1) glds16(pb + (size_t)kt * 32 * D_, bof0, buf + 4096 + wv * 1024);
    else if (wv < 2) glds16(pb + (size_t)kt * 32 * D_, bof1, buf + 4096 + (4 + wv) * 1024);
  };
  auto waitL = [&](auto n) { if (wv < 2) waitv<9 * decltype(n)::value>(); else waitv<6 * decltype(n)::value>(); };
  auto bar = [&]() { BARRIER(); };
  const int g = lane >> 4;
  const int aoff = (wm * 32 + (lane & 15)) * 64 + ((g ^ ((-(lane >> 2)) & 3)) << 4);
  const int boff = 4096 + (g * 6 + wn * 3) * 256 + ((lane & 15) >> 2) * 32 + (lane & 3) * 8;
  const int hlo = (g & 1) << 7, hhi = hlo ^ 128;
  auto ldA = [&](const char* st, int mt) { return *(const bf16x8*)(st + aoff + mt * 1024); };
  auto ldB = [&](const char* st, int nt) { return bfrag2(st + boff + nt * 256 + hlo, st + boff + nt * 256 + hhi); };
  auto extra = [&](int, int, unsigned) {};
  u32x4 pv[3][4];
  auto extra0 = [&]() {
#pragma unroll
    for (int nt = 0; nt < 3; ++nt)
#pragma unroll
      for (int sp = 0; sp < 4; ++sp)
        pv[nt][sp] = __builtin_nontemporal_load(
            (const u32x4*)(part + ((size_t)((sp * 64 + mt_blk * 2 + wm) * 36 + nt_blk * 6 + wn * 3 + nt) * 64 + lane) * 8));
  };
  gemm_loop<2, 3, 3, K3_STAGE, 3, 5>(6, lds, acc, ldA, ldB, piece, waitL, extra, bar, extra0);
#pragma unroll
  for (int nt = 0; nt < 3; ++nt)
#pragma unroll
    for (int mt = 0; mt < 2; ++mt) {
      size_t off = (size_t)(m0 + wm * 32 + mt * 16 + (lane & 15)) * D_ + n0 + wn * 48 + nt * 16 + 4 * (lane >> 4);
#pragma unroll
      for (int sp = 0; sp < 4; ++sp) {
        u32x2 w = {pv[nt][sp][mt * 2], pv[nt][sp][mt * 2 + 1]};
        acc[nt][mt] += (f32x4){__uint_as_float(w[0] << 16), __uint_as_float(w[0] & 0xffff0000u), __uint_as_float(w[1] << 16),
                               __uint_as_float(w[1] & 0xffff0000u)};
      }
      u32x2 v = {pk2(acc[nt][mt][0], acc[nt][mt][1]), pk2(acc[nt][mt][2], acc[nt][mt][3])};
      *(u32x2*)(out + off) = v;
    }
}

extern "C" void kernel_launch(void* const* d_in, const int* in_sizes, int n_in, void* d_out, int out_size,
                              void* d_ws, size_t ws_size, hipStream_t stream) {
  const u16* x = (const u16*)d_in[0];
  const u16* wqkv = (const u16*)d_in[1];
  const u16* wo = (const u16*)d_in[2];
  const u16* wg = (const u16*)d_in[4];
  const u16* wu = (const u16*)d_in[5];
  const u16* wd = (const u16*)d_in[6];
  const u16* gw = (const u16*)d_in[7];
  char* ws = (char*)d_ws;
  u16* qkvp = (u16*)ws;
  float* gate = (float*)(ws + (size_t)16 * 1024 * 1024);
  u16* Ao = (u16*)(ws + (size_t)17 * 1024 * 1024);
  u16* Ah = (u16*)(ws + (size_t)20 * 1024 * 1024);
  u16* part = (u16*)(ws + (size_t)48 * 1024 * 1024);
  k_proj<<<256, 512, 0, stream>>>(x, wqkv, wg, wu, gw, qkvp, gate, Ah);
  k_mid<<<512, 512, 0, stream>>>(qkvp, gate, Ao, Ah, wd, part);
  k_out<<<192, 256, 0, stream>>>(Ao, wo, part, (u16*)d_out);
}
```

```cpp
#include <hip/hip_runtime.h>
#include <hip/hip_bf16.h>
#include <type_traits>

typedef __attribute__((ext_vector_type(8))) short bf16x8;
typedef __attribute__((ext_vector_type(4))) short s16x4;
typedef __attribute__((ext_vector_type(4))) float f32x4;
typedef __attribute__((ext_vector_type(2))) float f32x2;
typedef __attribute__((ext_vector_type(16))) float f32x16;
typedef __attribute__((ext_vector_type(4))) unsigned u32x4;
typedef __attribute__((ext_vector_type(2))) unsigned u32x2;
typedef __attribute__((ext_vector_type(2))) __bf16 bf16x2_t;
typedef unsigned short u16;

#define DI __device__ __forceinline__
#define LDSP(T) __attribute__((address_space(3))) T

constexpr int S_ = 2048, D_ = 576, H_ = 32, DH_ = 18, F_ = 4608;

DI unsigned pk2(float lo, float hi) {
  f32x2 v = {lo, hi};
  bf16x2_t b = __builtin_convertvector(v, bf16x2_t);
  return __builtin_bit_cast(unsigned, b);
}
DI float rbf(float x) { return __uint_as_float(pk2(x, 0.f) << 16); }
DI float dot2bf(unsigned a, unsigned b, float c) {
  return __builtin_amdgcn_fdot2_f32_bf16(__builtin_bit_cast(bf16x2_t, a), __builtin_bit_cast(bf16x2_t, b), c, false);
}

DI void glds16(const void* sbase, unsigned voff, unsigned lds_base) {
  unsigned sv;
  asm volatile("s_mov_b32 %0, m0\n\ts_mov_b32 m0, %3\n\ts_nop 0\n\tglobal_load_lds_dwordx4 %1, %2\n\ts_mov_b32 m0, %0"
               : "=&s"(sv) : "v"(voff), "s"(sbase), "s"(__builtin_amdgcn_readfirstlane(lds_base)) : "memory");
}
DI unsigned lds_addr(const char* p) { return (unsigned)(size_t)(LDSP(const char)*)p; }
DI s16x4 trread(const char* p) { return __builtin_amdgcn_ds_read_tr16_b64_v4i16((LDSP(s16x4)*)p); }
template <int N> DI void waitv() { asm volatile("s_waitcnt vmcnt(%0)" ::"n"(N) : "memory"); }
#define BARRIER() do { asm volatile("s_waitcnt lgkmcnt(0)" ::: "memory"); __builtin_amdgcn_s_barrier(); asm volatile("" ::: "memory"); } while (0)

#define SBAR() __builtin_amdgcn_sched_barrier(0)
template <int NBs>
DI unsigned bseg_off(int u, int ld) {
  int st = u >> 4, kblk = st / NBs, nblk = st - kblk * NBs;
  int rr = (u >> 1) & 7, hf = u & 1;
  int k = kblk * 8 + (rr ^ ((kblk & 1) << 2));
  return (unsigned)(k * ld + nblk * 16 + hf * 8) * 2u;
}
DI unsigned aseg_off(int u, int lda) {
  int row = u >> 2, ch = (u & 3) ^ ((-(row >> 2)) & 3);
  return (unsigned)(row * lda + ch * 8) * 2u;
}
DI bf16x8 bfrag2(const char* lo, const char* hi) {
  s16x4 a = trread(lo), b = trread(hi);
  return __builtin_shufflevector(a, b, 0, 1, 2, 3, 4, 5, 6, 7);
}

template <int N> using IC = std::integral_constant<int, N>;
template <int MT, int NT, int LMAX, int STAGE, int KS, int NR, class LdA, class LdB, class Piece, class WaitL, class Extra, class Bar, class Pre>
DI void gemm_loop(int nsup, char* lds, f32x4 (&acc)[NT][MT], LdA ldA, LdB ldB, Piece piece, WaitL waitL, Extra extra, Bar bar, Pre pre) {
  static_assert(NT >= MT + 1 && NT >= LMAX, "schedule");
  constexpr int SUP = KS * STAGE;
  const unsigned l0 = lds_addr(lds);
#pragma unroll
  for (int s = 0; s < (NR - 1) * KS; ++s)
#pragma unroll
    for (int i = 0; i < LMAX; ++i) piece(i, s, l0 + s * STAGE);
  pre();
  waitL(IC<NR - 2>{});
  bar();
  bf16x8 af[MT], bw0, bw1;
#pragma unroll
  for (int mt = 0; mt < MT; ++mt) af[mt] = ldA(lds, mt);
  bw0 = ldB(lds, 0);
  bw1 = ldB(lds, 1);
  int cb = 0;
  auto body = [&](int j, auto wn_, auto more_) {
    constexpr bool more = decltype(more_)::value;
    waitL(wn_);
    bar();
    const int nb_ = cb + 1 == NR ? 0 : cb + 1, pb_ = cb == 0 ? NR - 1 : cb - 1;
    const char* base = lds + cb * SUP;
    const char* nbase = lds + nb_ * SUP;
    const unsigned dst = l0 + pb_ * SUP;
#pragma unroll
    for (int ks = 0; ks < KS; ++ks) {
      const char* cur = base + ks * STAGE;
      const char* nx = ks + 1 < KS ? base + (ks + 1) * STAGE : nbase;
      bf16x8 an[MT];
#pragma unroll
      for (int nt = 0; nt < NT; ++nt) {
        bf16x8 b = (nt & 1) ? bw1 : bw0;
        bf16x8 nb = nt + 2 < NT ? ldB(cur, nt + 2) : ldB(nx, nt + 2 - NT);
        if (nt & 1) bw1 = nb; else bw0 = nb;
        if (nt >= NT - 1 - MT && nt < NT - 1) an[nt - (NT - 1 - MT)] = ldA(nx, nt - (NT - 1 - MT));
        if (nt < LMAX && more) piece(nt, (j + NR - 1) * KS + ks, dst + ks * STAGE);
#pragma unroll
        for (int mt = 0; mt < MT; ++mt) acc[nt][mt] = __builtin_amdgcn_mfma_f32_16x16x32_bf16(b, af[mt], acc[nt][mt], 0, 0, 0);
        if (more) extra(nt, (j + NR - 1) * KS + ks, dst + ks * STAGE - l0);
        SBAR();
      }
#pragma unroll
      for (int mt = 0; mt < MT; ++mt) af[mt] = an[mt];
      if (NT & 1) { bf16x8 t = bw0; bw0 = bw1; bw1 = t; }
    }
    cb = nb_;
  };
  for (int j = 0; j < nsup - (NR - 1); ++j) body(j, IC<NR - 3>{}, IC<1>{});
#pragma unroll 1
  for (int j = nsup - (NR - 1); j < nsup; ++j) body(j, IC<0>{}, IC<0>{});
}

constexpr float QSCALE = 0.23570226039551584f * 1.4426950408889634f;
constexpr int K1_STAGE = 16384 + 9216 + 9216 + 4096;

__global__ __launch_bounds__(512) void k_proj(const u16* __restrict__ x, const u16* __restrict__ wqkv,
                                              const u16* __restrict__ wg, const u16* __restrict__ wu,
                                              const u16* __restrict__ gate_w, u16* __restrict__ qkvp,
                                              float* __restrict__ gate, u16* __restrict__ Ah,
                                              const u16* __restrict__ wd, const u16* __restrict__ wo) {
  __shared__ __attribute__((aligned(16))) char lds[4 * K1_STAGE + 1152 + 1024];
  const int tid = threadIdx.x, lane = tid & 63;
  const int wv = __builtin_amdgcn_readfirstlane(tid >> 6), wm = wv >> 1, wn = wv & 1;
  const int bid = blockIdx.x;
  const int nt_blk = (bid & 7) * 4 + ((bid >> 3) & 3), mt_blk = bid >> 5;
  const int m0 = mt_blk * 256, f0 = nt_blk * 144;
  const int coff = nt_blk == 31 ? 2 : 0;
  const int c0 = nt_blk * 54 - coff;
  char* gwl = lds + 4 * K1_STAGE;

  f32x4 acc[11][4] = {};
  const u16* xa = x + (size_t)m0 * D_;
  const unsigned aof0 = aseg_off(wv * 64 + lane, D_), aof1 = aseg_off((8 + wv) * 64 + lane, D_);
  unsigned bof[3];
  const u16* bsrc[3];
  int bld[3];
#pragma unroll
  for (int i = 0; i < 3; ++i) {
    int jj = i * 8 + wv;
    if (jj >= 22) jj -= 6;
    if (jj < 18) {
      int j = jj < 9 ? jj : jj - 9;
      bof[i] = bseg_off<9>(j * 64 + lane, F_);
      bsrc[i] = (jj < 9 ? wg : wu) + f0;
      bld[i] = F_;
    } else {
      bof[i] = bseg_off<4>(((jj - 18) & 3) * 64 + lane, 3 * D_);
      bsrc[i] = wqkv + c0;
      bld[i] = 3 * D_;
    }
  }
  const bool padlane = wv >= 2 && wv < 6 && (lane & 1) && (lane >> 4) == 3;
  auto piece = [&](int i, int kt, unsigned buf) {
    if (i == 0) glds16(xa + kt * 32, aof0, buf + wv * 1024);
    else if (i == 1) glds16(xa + kt * 32, aof1, buf + (8 + wv) * 1024);
    else if (i < 4) glds16(bsrc[i - 2] + (size_t)kt * 32 * bld[i - 2], bof[i - 2], buf + 16384 + ((i - 2) * 8 + wv) * 1024);
    else if (!padlane) glds16(bsrc[2] + (size_t)kt * 32 * bld[2], bof[2], buf + 16384 + (16 + (wv < 6 ? wv : wv - 6)) * 1024);
  };
  const int gk = lane & 31, gkb = gk >> 3;
  const int gdst = 16384 + 18432 + (gkb * 4 + 3) * 256 + ((gk & 7) ^ ((gkb & 1) << 2)) * 32 + 16;
  auto gfill = [&](int kt, unsigned off) {
    if (wv == 6 && lane < 32) {
      u32x4 v = {(unsigned)*(const u16*)(gwl + (kt * 32 + gk) * 2), 0u, 0u, 0u};
      *(u32x4*)(lds + off + gdst) = v;
    }
  };
  auto pre = [&]() {
    if (wv == 6) {
      waitv<15>();
      gfill(0, 0); gfill(1, K1_STAGE); gfill(2, 2 * K1_STAGE);
    }
  };
  if (wv == 6) {
    glds16(gate_w, lane * 16u, lds_addr(gwl));
    if (lane < 8) glds16(gate_w + 512, lane * 16u, lds_addr(gwl) + 1024);
  }
  auto waitL = [&](auto n) { waitv<5 * decltype(n)::value>(); };
  auto bar = [&]() { BARRIER(); };
  const int g = lane >> 4;
  const int bl = ((lane & 15) >> 2) * 32 + (lane & 3) * 8;
  const int aoff = (wm * 64 + (lane & 15)) * 64 + ((g ^ ((-(lane >> 2)) & 3)) << 4);
  const int bgu = 16384 + wn * 9216 + g * 9 * 256 + bl;
  const int bqq = 16384 + 18432 + (g * 4 + wn * 2) * 256 + bl;
  const int hlo = (g & 1) << 7, hhi = hlo ^ 128;
  auto ldA = [&](const char* st, int mt) { return *(const bf16x8*)(st + aoff + mt * 1024); };
  auto ldB = [&](const char* st, int nt) {
    const char* bp = st + (nt < 9 ? bgu + nt * 256 : bqq + (nt - 9) * 256);
    return bfrag2(bp + hlo, bp + hhi);
  };
  auto extra = [&](int nt, int kt, unsigned off) {
    if (nt == 5) gfill(kt, off);
    if (nt == 7 && kt >= 8 && kt < 11) {
      const unsigned i = (unsigned)(kt - 8);
      if (wv < 7) {
        unsigned o = (unsigned)bid * 20736u + (wv * 3 + i) * 1024u + lane * 16u;
        glds16(wd, o < 5308416u ? o : 0u, lds_addr(gwl) + 1152);
      } else {
        unsigned o = (unsigned)bid * 2592u + i * 1024u + lane * 16u;
        glds16(wo, o < 663552u ? o : 0u, lds_addr(gwl) + 1152);
      }
    }
  };
  gemm_loop<4, 11, 5, K1_STAGE, 1, 4>(18, lds, acc, ldA, ldB, piece, waitL, extra, bar, pre);
  BARRIER();
  int tz = threadIdx.x;
  asm volatile("" : "+v"(tz));
  const int lz = tz & 63, gz = lz >> 4, tk = lz & 15;

  float* G = (float*)(lds + 73728);
  const int hg0 = nt_blk * 3;
  {
    char* xs = lds + wv * 9216 + lz * 8;
    if (wn == 1) {
#pragma unroll
      for (int nt = 0; nt < 9; ++nt)
#pragma unroll
        for (int m2 = 0; m2 < 2; ++m2) { f32x4 t = acc[nt][m2]; acc[nt][m2] = acc[nt][2 + m2]; acc[nt][2 + m2] = t; }
    }
#pragma unroll
    for (int nt = 0; nt < 9; ++nt)
#pragma unroll
      for (int m2 = 0; m2 < 2; ++m2) {
        f32x4 v = acc[nt][2 + m2];
        *(u32x2*)(xs + (nt * 2 + m2) * 512) = (u32x2){pk2(v[0], v[1]), pk2(v[2], v[3])};
      }
    if (wn == 1 && gz == 2) {
#pragma unroll
      for (int mt = 0; mt < 4; ++mt) {
        float gv = rbf(1.f / (1.f + __expf(-rbf(acc[10][mt][0]))));
        G[wm * 64 + mt * 16 + tk] = gv;
        if (nt_blk == 0) gate[m0 + wm * 64 + mt * 16 + tk] = gv;
      }
    }
  }
  __syncthreads();
  {
    const char* xr = lds + (wv ^ 1) * 9216 + lz * 8;
    u32x2 pv[9][2];
#pragma unroll
    for (int nt = 0; nt < 9; ++nt)
#pragma unroll
      for (int m2 = 0; m2 < 2; ++m2) pv[nt][m2] = *(const u32x2*)(xr + (nt * 2 + m2) * 512);
    float og[2];
#pragma unroll
    for (int m2 = 0; m2 < 2; ++m2) og[m2] = rbf(1.f - G[wm * 64 + (wn * 2 + m2) * 16 + tk]);
    asm volatile("s_waitcnt lgkmcnt(0)" ::: "memory");
    __builtin_amdgcn_s_barrier();
#pragma unroll
    for (int nt = 0; nt < 9; ++nt)
#pragma unroll
      for (int m2 = 0; m2 < 2; ++m2) {
        f32x4 v = acc[nt][m2];
        u32x2 uv = pv[nt][m2];
        float h0 = v[0] * og[m2] * __uint_as_float(uv[0] << 16);
        float h1 = v[1] * og[m2] * __uint_as_float(uv[0] & 0xffff0000u);
        float h2 = v[2] * og[m2] * __uint_as_float(uv[1] << 16);
        float h3 = v[3] * og[m2] * __uint_as_float(uv[1] & 0xffff0000u);
        pv[nt][m2] = (u32x2){pk2(h0, h1), pk2(h2, h3)};
      }
#pragma unroll
    for (int m2 = 0; m2 < 2; ++m2) {
      char* hb = (char*)Ah + ((size_t)((mt_blk * 16 + wm * 4 + wn * 2 + m2) * 144 + ((nt_blk * 9) >> 1)) * 64 + lz) * 16;
      if ((nt_blk & 1) == 0) {
#pragma unroll
        for (int i = 0; i < 4; ++i)
          *(u32x4*)(hb + i * 1024) = (u32x4){pv[2 * i][m2][0], pv[2 * i][m2][1], pv[2 * i + 1][m2][0], pv[2 * i + 1][m2][1]};
        *(u32x2*)(hb + 4096) = pv[8][m2];
      } else {
        *(u32x2*)(hb + 8) = pv[0][m2];
#pragma unroll
        for (int i = 0; i < 4; ++i)
          *(u32x4*)(hb + (i + 1) * 1024) =
              (u32x4){pv[2 * i + 1][m2][0], pv[2 * i + 1][m2][1], pv[2 * i + 2][m2][0], pv[2 * i + 2][m2][1]};
      }
    }
  }
  {
    char* img = lds;
#pragma unroll
    for (int t2 = 0; t2 < 2; ++t2)
#pragma unroll
      for (int r = 0; r < 4; r += 2) {
        int c = wn * 32 + t2 * 16 + 4 * gz + r - coff;
        if (c >= 0 && c < 54) {
          int hl = c / 18, d = c - hl * 18;
          int which = (hg0 + hl) >> 5;
          float sc = which == 0 ? QSCALE : 1.f;
#pragma unroll
          for (int mt = 0; mt < 4; ++mt) {
            int tok = wm * 64 + mt * 16 + tk;
            unsigned v = pk2(acc[9 + t2][mt][r] * sc, acc[9 + t2][mt][r + 1] * sc);
            if (which == 2) {
              char* q = img + hl * 16384 + (tok >> 5) * 2048 + ((((tok >> 4) & 1) * 2 + ((tok >> 2) & 1)) * 32 + d) * 16 +
                        ((tok & 3) + 4 * ((tok >> 3) & 1)) * 2;
              *(u16*)q = (u16)v;
              *(u16*)(q + 16) = (u16)(v >> 16);
            } else {
              int ch = d >> 3;
              if (which == 1) ch ^= (tok >> 2) & 3;
              *(unsigned*)(img + (hl * 256 + tok) * 64 + ch * 16 + (d & 7) * 2) = v;
            }
          }
        }
      }
    for (int i = tz; i < 768; i += 512) {
      int which = (hg0 + (i >> 8)) >> 5;
      if (which == 2) {
        char* grp = img + (i >> 8) * 16384 + ((i & 255) >> 3) * 512;
        int d0 = 18 + (i & 7) * 2;
        unsigned one = d0 == 18 ? 0x3f803f80u : 0u;
        if (d0 < 32) {
          *(u32x4*)(grp + d0 * 16) = (u32x4){one, one, one, one};
          *(u32x4*)(grp + d0 * 16 + 16) = (u32x4){0u, 0u, 0u, 0u};
        }
      } else {
        int sw = which == 1 ? (i >> 2) & 3 : 0;
        char* row = img + i * 64;
        *(unsigned*)(row + (2 ^ sw) * 16 + 4) = 0u;
        *(u32x2*)(row + (2 ^ sw) * 16 + 8) = (u32x2){0u, 0u};
        *(u32x4*)(row + (3 ^ sw) * 16) = (u32x4){0u, 0u, 0u, 0u};
      }
    }
  }
  __syncthreads();
#pragma unroll
  for (int i = 0; i < 6; ++i) {
    int u = i * 512 + tz;
    int hl = u >> 10, rem = u & 1023;
    u32x4 v = *(const u32x4*)(lds + u * 16);
    *(u32x4*)(qkvp + ((size_t)(nt_blk * 3 + hl) * S_ + m0) * 32 + rem * 8) = v;
  }
}

DI void attn_stage(const u16* __restrict__ Kh, const u16* __restrict__ Vh, int c, unsigned buf, int wv, int lane) {
#pragma unroll
  for (int i = 0; i < 2; ++i) glds16(Kh + (size_t)c * 4096, ((i * 4 + wv) * 64 + lane) * 16u, buf + (i * 4 + wv) * 1024);
#pragma unroll
  for (int i = 0; i < 2; ++i)
    glds16(Vh + (size_t)c * 4096, ((i * 4 + wv) * 64 + lane) * 16u, buf + 8192 + (i * 4 + wv) * 1024);
}

DI float halfswap_max(float m) {
  auto rr = __builtin_amdgcn_permlane32_swap(__float_as_uint(m), __float_as_uint(m), false, false);
  return fmaxf(__uint_as_float(rr[0]), __uint_as_float(rr[1]));
}
DI float max16(const f32x16& s) {
  float mx = s[0];
#pragma unroll
  for (int r = 1; r < 16; ++r) mx = fmaxf(mx, s[r]);
  return halfswap_max(mx);
}
#define MFMA32(a, b, c) __builtin_amdgcn_mfma_f32_32x32x16_bf16((a), (b), (c), 0, 0, 0)

DI void exp_pack(const f32x16& s, bf16x8& p0, bf16x8& p1) {
  u32x4 a, b;
#pragma unroll
  for (int r = 0; r < 4; ++r) a[r] = pk2(__builtin_amdgcn_exp2f(s[2 * r]), __builtin_amdgcn_exp2f(s[2 * r + 1]));
#pragma unroll
  for (int r = 0; r < 4; ++r) b[r] = pk2(__builtin_amdgcn_exp2f(s[8 + 2 * r]), __builtin_amdgcn_exp2f(s[9 + 2 * r]));
  p0 = __builtin_bit_cast(bf16x8, a);
  p1 = __builtin_bit_cast(bf16x8, b);
}

#define EX2(x) __builtin_amdgcn_exp2f(x)
#define PHASE(S, PA, PB, M1, M2, M3, M4)                                              \
  do {                                                                                \
    M1; SBAR(); PA[0] = pk2(EX2(S[0]), EX2(S[1])); PA[1] = pk2(EX2(S[2]), EX2(S[3])); SBAR();  \
    M2; SBAR(); PA[2] = pk2(EX2(S[4]), EX2(S[5])); PA[3] = pk2(EX2(S[6]), EX2(S[7])); SBAR();  \
    M3; SBAR(); PB[0] = pk2(EX2(S[8]), EX2(S[9])); PB[1] = pk2(EX2(S[10]), EX2(S[11])); SBAR(); \
    M4; SBAR(); PB[2] = pk2(EX2(S[12]), EX2(S[13])); PB[3] = pk2(EX2(S[14]), EX2(S[15])); SBAR(); \
  } while (0)
#define BC8(x) __builtin_bit_cast(bf16x8, x)
DI void attn_fast(const u16* __restrict__ Kh, const u16* __restrict__ Vh, const bf16x8 (&qf)[2][2], char* lds,
                  f32x16 (&o)[2], int wv, int lane) {
  const int r32 = lane & 31, h = lane >> 5;
  const int koff0 = r32 * 64 + ((h ^ ((r32 >> 2) & 3)) << 4);
  const int koff1 = r32 * 64 + (((2 + h) ^ ((r32 >> 2) & 3)) << 4);
  const int voff = 8192 + lane * 16;
  const unsigned l0 = lds_addr(lds);
  f32x16 negm[2], sa, sb;
#pragma unroll
  for (int a = 0; a < 2; ++a)
#pragma unroll
    for (int r = 0; r < 16; ++r) { o[a][r] = 0.f; negm[a][r] = 0.f; }
  waitv<8>();
  BARRIER();
  bf16x8 kc0 = *(const bf16x8*)(lds + koff0), kc1 = *(const bf16x8*)(lds + koff1);
  {
#pragma unroll
    for (int a = 0; a < 2; ++a) {
      f32x16 s = MFMA32(kc0, qf[a][0], negm[a]);
      s = MFMA32(kc1, qf[a][1], s);
      float mx = max16(s);
#pragma unroll
      for (int r = 0; r < 16; ++r) { negm[a][r] = -mx; if (a == 0) sa[r] = s[r] - mx; }
    }
  }
  u32x4 pa0, pa1, pb0 = {0u, 0u, 0u, 0u}, pb1 = {0u, 0u, 0u, 0u};
  bf16x8 vp0 = {0, 0, 0, 0, 0, 0, 0, 0}, vp1 = {0, 0, 0, 0, 0, 0, 0, 0};
  for (int c = 0; c < 16; ++c) {
    if (c + 2 < 16) waitv<4>(); else waitv<0>();
    BARRIER();
    if (c + 3 < 16) attn_stage(Kh, Vh, c + 3, l0 + ((c + 3) & 3) * 16384, wv, lane);
    const char* buf = lds + (c & 3) * 16384;
    const char* bufn = lds + ((c + 1) & 3) * 16384;
#pragma unroll
    for (int t = 0; t < 4; ++t) {
      const char* kb = t < 3 ? buf + (t + 1) * 2048 : bufn;
      const char* vb = buf + t * 2048 + voff;
      bf16x8 kn0 = *(const bf16x8*)(kb + koff0), kn1 = *(const bf16x8*)(kb + koff1);
      bf16x8 vc0 = *(const bf16x8*)vb, vc1 = *(const bf16x8*)(vb + 1024);
      SBAR();
      PHASE(sa, pa0, pa1, sb = MFMA32(kc0, qf[1][0], negm[1]), o[1] = MFMA32(vp0, BC8(pb0), o[1]),
            sb = MFMA32(kc1, qf[1][1], sb), o[1] = MFMA32(vp1, BC8(pb1), o[1]));
      PHASE(sb, pb0, pb1, sa = MFMA32(kn0, qf[0][0], negm[0]), o[0] = MFMA32(vc0, BC8(pa0), o[0]),
            sa = MFMA32(kn1, qf[0][1], sa), o[0] = MFMA32(vc1, BC8(pa1), o[0]));
      kc0 = kn0; kc1 = kn1; vp0 = vc0; vp1 = vc1;
    }
  }
  o[1] = MFMA32(vp0, BC8(pb0), o[1]);
  o[1] = MFMA32(vp1, BC8(pb1), o[1]);
}

DI void attn_safe(const u16* __restrict__ Kh, const u16* __restrict__ Vh, const bf16x8 (&qf)[2][2], char* lds,
                  f32x16 (&o)[2], int wv, int lane) {
  const int r32 = lane & 31, h = lane >> 5;
  const int koff0 = r32 * 64 + ((h ^ ((r32 >> 2) & 3)) << 4);
  const int koff1 = r32 * 64 + (((2 + h) ^ ((r32 >> 2) & 3)) << 4);
  const int voff = 8192 + lane * 16;
  const unsigned l0 = lds_addr(lds);
  f32x16 zero;
  float m[2] = {-INFINITY, -INFINITY};
#pragma unroll
  for (int r = 0; r < 16; ++r) { o[0][r] = 0.f; o[1][r] = 0.f; zero[r] = 0.f; }
  for (int c = 0; c < 16; ++c) {
    BARRIER();
    attn_stage(Kh, Vh, c, l0, wv, lane);
    waitv<0>();
    BARRIER();
    for (int t = 0; t < 4; ++t) {
      const char* kb = lds + t * 2048;
      bf16x8 k0 = *(const bf16x8*)(kb + koff0), k1 = *(const bf16x8*)(kb + koff1);
      bf16x8 va = *(const bf16x8*)(kb + voff), vb = *(const bf16x8*)(kb + voff + 1024);
#pragma unroll
      for (int a = 0; a < 2; ++a) {
        f32x16 s = MFMA32(k0, qf[a][0], zero);
        s = MFMA32(k1, qf[a][1], s);
        float mn = fmaxf(m[a], max16(s));
        float al = __builtin_amdgcn_exp2f(m[a] - mn);
        m[a] = mn;
#pragma unroll
        for (int r = 0; r < 16; ++r) { o[a][r] *= al; s[r] -= mn; }
        bf16x8 p0, p1;
        exp_pack(s, p0, p1);
        o[a] = MFMA32(va, p0, o[a]);
        o[a] = MFMA32(vb, p1, o[a]);
      }
    }
  }
}

DI void attn_block(int bid, const u16* __restrict__ qkvp, const float* __restrict__ gate, u16* __restrict__ Ao,
                   char* lds) {
  const int tid = threadIdx.x, lane = tid & 63, r32 = lane & 31, h = lane >> 5;
  const int wv = __builtin_amdgcn_readfirstlane(tid >> 6);
  const int head = (bid & 7) * 4 + ((bid >> 3) & 3), qb = bid >> 5;
  const u16* Qh = qkvp + (size_t)head * (S_ * 32);
  const u16* Kh = qkvp + (size_t)(H_ + head) * (S_ * 32);
  const u16* Vh = qkvp + (size_t)(2 * H_ + head) * (S_ * 32);
  const int qrow0 = qb * 256 + wv * 64 + r32;
  {
    const unsigned l0 = lds_addr(lds);
    attn_stage(Kh, Vh, 0, l0, wv, lane);
    attn_stage(Kh, Vh, 1, l0 + 16384, wv, lane);
    attn_stage(Kh, Vh, 2, l0 + 32768, wv, lane);
  }
  bf16x8 qf[2][2];
#pragma unroll
  for (int a = 0; a < 2; ++a) {
    qf[a][0] = *(const bf16x8*)(Qh + (size_t)(qrow0 + a * 32) * 32 + h * 8);
    qf[a][1] = *(const bf16x8*)(Qh + (size_t)(qrow0 + a * 32) * 32 + 16 + h * 8);
  }
  float gq[2] = {gate[qrow0], gate[qrow0 + 32]};
  asm volatile("" ::"v"(qf[0][0]), "v"(qf[0][1]), "v"(qf[1][0]), "v"(qf[1][1]), "v"(gq[0]), "v"(gq[1]));
  f32x16 o[2];
  attn_fast(Kh, Vh, qf, lds, o, wv, lane);
  bool bad = false;
#pragma unroll
  for (int a = 0; a < 2; ++a)
#pragma unroll
    for (int r = 0; r < 11; ++r) bad |= (r < 8 || h == 0) && !(fabsf(o[a][r]) < 1e30f);
  if (__syncthreads_or(bad)) attn_safe(Kh, Vh, qf, lds, o, wv, lane);
  unsigned pw[2][5];
#pragma unroll
  for (int a = 0; a < 2; ++a) {
    float l = __shfl(o[a][10], r32);
    float sc = gq[a] / l;
#pragma unroll
    for (int i = 0; i < 5; ++i) pw[a][i] = pk2(o[a][2 * i] * sc, o[a][2 * i + 1] * sc);
  }
  unsigned xs[5], ys[5];
#pragma unroll
  for (int i = 0; i < 5; ++i) {
    auto r = __builtin_amdgcn_permlane32_swap(pw[0][i], pw[1][i], false, false);
    xs[i] = r[0];
    ys[i] = r[1];
  }
  unsigned* dst = (unsigned*)(Ao + (size_t)(qrow0 + h * 32) * D_ + head * DH_);
  typedef __attribute__((ext_vector_type(4), aligned(4))) unsigned u32x4a;
  *(u32x4a*)dst = (u32x4a){xs[0], xs[1], ys[0], ys[1]};
  *(u32x4a*)(dst + 4) = (u32x4a){xs[2], xs[3], ys[2], ys[3]};
  dst[8] = xs[4];
}

constexpr int K2_STAGE = 16384 + 18432;
DI void down_block(int bid, const u16* __restrict__ Ah, const u16* __restrict__ wd, u16* __restrict__ part,
                   char* lds) {
  const int tid = threadIdx.x, lane = tid & 63;
  const int wv = __builtin_amdgcn_readfirstlane(tid >> 6);
  const int mt_blk = (bid & 7) * 2 + ((bid >> 3) & 1), rest = bid >> 4;
  const int nt_blk = rest & 3, sp = rest >> 2;
  const int m0 = mt_blk * 128, n0 = nt_blk * 144;
  f32x4 acc[9][2] = {};
  const char* pa = (const char*)Ah + ((size_t)(mt_blk * 8) * 144 + sp * 36) * 1024;
  const u16* pb = wd + (size_t)sp * 1152 * D_ + n0;
  const unsigned l0 = lds_addr(lds);
  const unsigned aof = (unsigned)(wv * 2 * 144 * 1024 + lane * 16);
  unsigned bof[5];
#pragma unroll
  for (int i = 0; i < 5; ++i) {
    int u = (i < 4 ? i * 4 + wv : 16 + (wv & 1)) * 64 + lane;
    int st = u >> 4, kblk = st / 9, nblk = st - kblk * 9;
    int rr = ((u >> 1) & 7) ^ ((kblk & 1) << 2);
    int k = (kblk >> 2) * 32 + (rr >> 2) * 16 + (kblk & 3) * 4 + (rr & 3);
    bof[i] = (unsigned)(k * D_ + nblk * 16 + (u & 1) * 8) * 2u;
  }
  auto piece = [&](int i, int kt, unsigned buf) {
    if (i < 4) glds16(pa + kt * 2048 + ((i >> 1) * 144 + (i & 1)) * 1024, aof, buf + (wv * 4 + i) * 1024);
    else if (i < 8) glds16(pb + (size_t)kt * 64 * D_, bof[i - 4], buf + 16384 + ((i - 4) * 4 + wv) * 1024);
    else if (wv < 2) glds16(pb + (size_t)kt * 64 * D_, bof[4], buf + 16384 + (16 + wv) * 1024);
  };
  const int g = lane >> 4;
  const int aoff0 = wv * 4096 + lane * 16, aoff1 = aoff0 + 1024;
  const int boff = 16384 + g * 9 * 256 + ((lane & 15) >> 2) * 32 + (lane & 3) * 8;
  const int hlo = (g & 1) << 7, hhi = hlo ^ 128;
#pragma unroll
  for (int i = 0; i < 9; ++i) piece(i, 0, l0);
  for (int kt = 0; kt < 18; ++kt) {
    waitv<0>();
    BARRIER();
    const char* cur = lds + (kt & 1) * K2_STAGE;
    const unsigned dst = l0 + ((kt + 1) & 1) * K2_STAGE;
    const bool more = kt + 1 < 18;
    bf16x8 af[2][2];
#pragma unroll
    for (int mt = 0; mt < 2; ++mt) {
      af[0][mt] = *(const bf16x8*)(cur + aoff0 + mt * 2048);
      af[1][mt] = *(const bf16x8*)(cur + aoff1 + mt * 2048);
    }
    bf16x8 bw0 = bfrag2(cur + boff + hlo, cur + boff + hhi);
    bf16x8 bw1 = bfrag2(cur + boff + 256 + hlo, cur + boff + 256 + hhi);
#pragma unroll
    for (int q = 0; q < 18; ++q) {
      bf16x8 b = (q & 1) ? bw1 : bw0;
      if (q + 2 < 18) {
        const int q2 = q + 2, o2 = ((q2 / 9) * 36 + q2 % 9) * 256;
        bf16x8 nb = bfrag2(cur + boff + o2 + hlo, cur + boff + o2 + hhi);
        if (q & 1) bw1 = nb; else bw0 = nb;
      }
      if (q < 9 && more) piece(q, kt + 1, dst);
#pragma unroll
      for (int mt = 0; mt < 2; ++mt)
        acc[q % 9][mt] = __builtin_amdgcn_mfma_f32_16x16x32_bf16(b, af[q / 9][mt], acc[q % 9][mt], 0, 0, 0);
      SBAR();
    }
  }
  u16* dst = part + ((size_t)((sp * 64 + mt_blk * 4 + wv) * 36 + nt_blk * 9) * 64 + lane) * 8;
#pragma unroll
  for (int nt = 0; nt < 9; ++nt)
    *(u32x4*)(dst + nt * 512) = (u32x4){pk2(acc[nt][0][0], acc[nt][0][1]), pk2(acc[nt][0][2], acc[nt][0][3]),
                                        pk2(acc[nt][1][0], acc[nt][1][1]), pk2(acc[nt][1][2], acc[nt][1][3])};
}

__global__ __launch_bounds__(256, 2) void k_mid(const u16* __restrict__ qkvp, const float* __restrict__ gate,
                                                u16* __restrict__ Ao, const u16* __restrict__ Ah,
                                                const u16* __restrict__ wd, u16* __restrict__ part) {
  __shared__ __attribute__((aligned(16))) char lds[2 * K2_STAGE];
  const int bid = blockIdx.x;
  if (bid < 256) attn_block(bid, qkvp, gate, Ao, lds);
  else down_block(bid - 256, Ah, wd, part, lds);
}

constexpr int K3_STAGE = 4096 + 6144;
__global__ __launch_bounds__(256) void k_out(const u16* __restrict__ Ao, const u16* __restrict__ wo,
                                             const u16* __restrict__ part, u16* __restrict__ out) {
  __shared__ __attribute__((aligned(16))) char lds[15 * K3_STAGE];
  const int tid = threadIdx.x, lane = tid & 63;
  const int wv = __builtin_amdgcn_readfirstlane(tid >> 6), wm = wv >> 1, wn = wv & 1;
  const int nt_blk = blockIdx.x % 6, mt_blk = blockIdx.x / 6;
  const int m0 = mt_blk * 64, n0 = nt_blk * 96;
  f32x4 acc[3][2] = {};
  const u16* pa = Ao + (size_t)m0 * D_;
  const u16* pb = wo + n0;
  const unsigned aof0 = aseg_off(wv * 64 + lane, D_);
  const unsigned bof0 = bseg_off<6>(wv * 64 + lane, D_), bof1 = bseg_off<6>((4 + (wv & 1)) * 64 + lane, D_);
  auto piece = [&](int i, int kt, unsigned buf) {
    if (i == 0) glds16(pa + kt * 32, aof0, buf + wv * 1024);
    else if (i == 1) glds16(pb + (size_t)kt * 32 * D_, bof0, buf + 4096 + wv * 1024);
    else if (wv < 2) glds16(pb + (size_t)kt * 32 * D_, bof1, buf + 4096 + (4 + wv) * 1024);
  };
  auto waitL = [&](auto n) { if (wv < 2) waitv<9 * decltype(n)::value>(); else waitv<6 * decltype(n)::value>(); };
  auto bar = [&]() { BARRIER(); };
  const int g = lane >> 4;
  const int aoff = (wm * 32 + (lane & 15)) * 64 + ((g ^ ((-(lane >> 2)) & 3)) << 4);
  const int boff = 4096 + (g * 6 + wn * 3) * 256 + ((lane & 15) >> 2) * 32 + (lane & 3) * 8;
  const int hlo = (g & 1) << 7, hhi = hlo ^ 128;
  auto ldA = [&](const char* st, int mt) { return *(const bf16x8*)(st + aoff + mt * 1024); };
  auto ldB = [&](const char* st, int nt) { return bfrag2(st + boff + nt * 256 + hlo, st + boff + nt * 256 + hhi); };
  auto extra = [&](int, int, unsigned) {};
  u32x4 pv[3][4];
  auto extra0 = [&]() {
#pragma unroll
    for (int nt = 0; nt < 3; ++nt)
#pragma unroll
      for (int sp = 0; sp < 4; ++sp)
        pv[nt][sp] = __builtin_nontemporal_load(
            (const u32x4*)(part + ((size_t)((sp * 64 + mt_blk * 2 + wm) * 36 + nt_blk * 6 + wn * 3 + nt) * 64 + lane) * 8));
  };
  gemm_loop<2, 3, 3, K3_STAGE, 3, 5>(6, lds, acc, ldA, ldB, piece, waitL, extra, bar, extra0);
#pragma unroll
  for (int nt = 0; nt < 3; ++nt)
#pragma unroll
    for (int mt = 0; mt < 2; ++mt) {
      size_t off = (size_t)(m0 + wm * 32 + mt * 16 + (lane & 15)) * D_ + n0 + wn * 48 + nt * 16 + 4 * (lane >> 4);
#pragma unroll
      for (int sp = 0; sp < 4; ++sp) {
        u32x2 w = {pv[nt][sp][mt * 2], pv[nt][sp][mt * 2 + 1]};
        acc[nt][mt] += (f32x4){__uint_as_float(w[0] << 16), __uint_as_float(w[0] & 0xffff0000u), __uint_as_float(w[1] << 16),
                               __uint_as_float(w[1] & 0xffff0000u)};
      }
      u32x2 v = {pk2(acc[nt][mt][0], acc[nt][mt][1]), pk2(acc[nt][mt][2], acc[nt][mt][3])};
      *(u32x2*)(out + off) = v;
    }
}

extern "C" void kernel_launch(void* const* d_in, const int* in_sizes, int n_in, void* d_out, int out_size,
                              void* d_ws, size_t ws_size, hipStream_t stream) {
  const u16* x = (const u16*)d_in[0];
  const u16* wqkv = (const u16*)d_in[1];
  const u16* wo = (const u16*)d_in[2];
  const u16* wg = (const u16*)d_in[4];
  const u16* wu = (const u16*)d_in[5];
  const u16* wd = (const u16*)d_in[6];
  const u16* gw = (const u16*)d_in[7];
  char* ws = (char*)d_ws;
  u16* qkvp = (u16*)ws;
  float* gate = (float*)(ws + (size_t)16 * 1024 * 1024);
  u16* Ao = (u16*)(ws + (size_t)17 * 1024 * 1024);
  u16* Ah = (u16*)(ws + (size_t)20 * 1024 * 1024);
  u16* part = (u16*)(ws + (size_t)48 * 1024 * 1024);
  k_proj<<<256, 512, 0, stream>>>(x, wqkv, wg, wu, gw, qkvp, gate, Ah, wd, wo);
  k_mid<<<512, 256, 0, stream>>>(qkvp, gate, Ao, Ah, wd, part);
  k_out<<<192, 256, 0, stream>>>(Ao, wo, part, (u16*)d_out);
}
```

```cpp
#include <hip/hip_runtime.h>
#include <hip/hip_bf16.h>
#include <type_traits>

typedef __attribute__((ext_vector_type(8))) short bf16x8;
typedef __attribute__((ext_vector_type(4))) short s16x4;
typedef __attribute__((ext_vector_type(4))) float f32x4;
typedef __attribute__((ext_vector_type(2))) float f32x2;
typedef __attribute__((ext_vector_type(16))) float f32x16;
typedef __attribute__((ext_vector_type(4))) unsigned u32x4;
typedef __attribute__((ext_vector_type(2))) unsigned u32x2;
typedef __attribute__((ext_vector_type(2))) __bf16 bf16x2_t;
typedef unsigned short u16;

#define DI __device__ __forceinline__
#define LDSP(T) __attribute__((address_space(3))) T

constexpr int S_ = 2048, D_ = 576, H_ = 32, DH_ = 18, F_ = 4608;

DI unsigned pk2(float lo, float hi) {
  f32x2 v = {lo, hi};
  bf16x2_t b = __builtin_convertvector(v, bf16x2_t);
  return __builtin_bit_cast(unsigned, b);
}
DI float rbf(float x) { return __uint_as_float(pk2(x, 0.f) << 16); }
DI float dot2bf(unsigned a, unsigned b, float c) {
  return __builtin_amdgcn_fdot2_f32_bf16(__builtin_bit_cast(bf16x2_t, a), __builtin_bit_cast(bf16x2_t, b), c, false);
}

DI void glds16(const void* sbase, unsigned voff, unsigned lds_base) {
  unsigned sv;
  asm volatile("s_mov_b32 %0, m0\n\ts_mov_b32 m0, %3\n\ts_nop 0\n\tglobal_load_lds_dwordx4 %1, %2\n\ts_mov_b32 m0, %0"
               : "=&s"(sv) : "v"(voff), "s"(sbase), "s"(__builtin_amdgcn_readfirstlane(lds_base)) : "memory");
}
DI unsigned lds_addr(const char* p) { return (unsigned)(size_t)(LDSP(const char)*)p; }
DI s16x4 trread(const char* p) { return __builtin_amdgcn_ds_read_tr16_b64_v4i16((LDSP(s16x4)*)p); }
template <int N> DI void waitv() { asm volatile("s_waitcnt vmcnt(%0)" ::"n"(N) : "memory"); }
#define BARRIER() do { asm volatile("s_waitcnt lgkmcnt(0)" ::: "memory"); __builtin_amdgcn_s_barrier(); asm volatile("" ::: "memory"); } while (0)

#define SBAR() __builtin_amdgcn_sched_barrier(0)
template <int NBs>
DI unsigned bseg_off(int u, int ld) {
  int st = u >> 4, kblk = st / NBs, nblk = st - kblk * NBs;
  int rr = (u >> 1) & 7, hf = u & 1;
  int k = kblk * 8 + (rr ^ ((kblk & 1) << 2));
  return (unsigned)(k * ld + nblk * 16 + hf * 8) * 2u;
}
DI unsigned aseg_off(int u, int lda) {
  int row = u >> 2, ch = (u & 3) ^ ((-(row >> 2)) & 3);
  return (unsigned)(row * lda + ch * 8) * 2u;
}
DI bf16x8 bfrag2(const char* lo, const char* hi) {
  s16x4 a = trread(lo), b = trread(hi);
  return __builtin_shufflevector(a, b, 0, 1, 2, 3, 4, 5, 6, 7);
}

template <int N> using IC = std::integral_constant<int, N>;
template <int MT, int NT, int LMAX, int STAGE, int KS, int NR, class LdA, class LdB, class Piece, class WaitL, class Extra, class Bar, class Pre>
DI void gemm_loop(int nsup, char* lds, f32x4 (&acc)[NT][MT], LdA ldA, LdB ldB, Piece piece, WaitL waitL, Extra extra, Bar bar, Pre pre) {
  static_assert(NT >= MT + 1 && NT >= LMAX, "schedule");
  constexpr int SUP = KS * STAGE;
  const unsigned l0 = lds_addr(lds);
#pragma unroll
  for (int s = 0; s < (NR - 1) * KS; ++s)
#pragma unroll
    for (int i = 0; i < LMAX; ++i) piece(i, s, l0 + s * STAGE);
  pre();
  waitL(IC<NR - 2>{});
  bar();
  bf16x8 af[MT], bw0, bw1;
#pragma unroll
  for (int mt = 0; mt < MT; ++mt) af[mt] = ldA(lds, mt);
  bw0 = ldB(lds, 0);
  bw1 = ldB(lds, 1);
  int cb = 0;
  auto body = [&](int j, auto wn_, auto more_) {
    constexpr bool more = decltype(more_)::value;
    waitL(wn_);
    bar();
    const int nb_ = cb + 1 == NR ? 0 : cb + 1, pb_ = cb == 0 ? NR - 1 : cb - 1;
    const char* base = lds + cb * SUP;
    const char* nbase = lds + nb_ * SUP;
    const unsigned dst = l0 + pb_ * SUP;
#pragma unroll
    for (int ks = 0; ks < KS; ++ks) {
      const char* cur = base + ks * STAGE;
      const char* nx = ks + 1 < KS ? base + (ks + 1) * STAGE : nbase;
      bf16x8 an[MT];
#pragma unroll
      for (int nt = 0; nt < NT; ++nt) {
        bf16x8 b = (nt & 1) ? bw1 : bw0;
        bf16x8 nb = nt + 2 < NT ? ldB(cur, nt + 2) : ldB(nx, nt + 2 - NT);
        if (nt & 1) bw1 = nb; else bw0 = nb;
        if (nt >= NT - 1 - MT && nt < NT - 1) an[nt - (NT - 1 - MT)] = ldA(nx, nt - (NT - 1 - MT));
        if (nt < LMAX && more) piece(nt, (j + NR - 1) * KS + ks, dst + ks * STAGE);
#pragma unroll
        for (int mt = 0; mt < MT; ++mt) acc[nt][mt] = __builtin_amdgcn_mfma_f32_16x16x32_bf16(b, af[mt], acc[nt][mt], 0, 0, 0);
        if (more) extra(nt, (j + NR - 1) * KS + ks, dst + ks * STAGE - l0);
        SBAR();
      }
#pragma unroll
      for (int mt = 0; mt < MT; ++mt) af[mt] = an[mt];
      if (NT & 1) { bf16x8 t = bw0; bw0 = bw1; bw1 = t; }
    }
    cb = nb_;
  };
  for (int j = 0; j < nsup - (NR - 1); ++j) body(j, IC<NR - 3>{}, IC<1>{});
#pragma unroll 1
  for (int j = nsup - (NR - 1); j < nsup; ++j) body(j, IC<0>{}, IC<0>{});
}

constexpr float QSCALE = 0.23570226039551584f * 1.4426950408889634f;
constexpr int K1_STAGE = 16384 + 9216 + 9216 + 4096;

__global__ __launch_bounds__(512) void k_proj(const u16* __restrict__ x, const u16* __restrict__ wqkv,
                                              const u16* __restrict__ wg, const u16* __restrict__ wu,
                                              const u16* __restrict__ gate_w, u16* __restrict__ qkvp,
                                              float* __restrict__ gate, u16* __restrict__ Ah,
                                              const u16* __restrict__ wd, const u16* __restrict__ wo) {
  __shared__ __attribute__((aligned(16))) char lds[4 * K1_STAGE + 1152 + 1024];
  const int tid = threadIdx.x, lane = tid & 63;
  const int wv = __builtin_amdgcn_readfirstlane(tid >> 6), wm = wv >> 1, wn = wv & 1;
  const int bid = blockIdx.x;
  const int nt_blk = (bid & 7) * 4 + ((bid >> 3) & 3), mt_blk = bid >> 5;
  const int m0 = mt_blk * 256, f0 = nt_blk * 144;
  const int coff = nt_blk == 31 ? 2 : 0;
  const int c0 = nt_blk * 54 - coff;
  char* gwl = lds + 4 * K1_STAGE;

  f32x4 acc[11][4] = {};
  const u16* xa = x + (size_t)m0 * D_;
  const unsigned aof0 = aseg_off(wv * 64 + lane, D_), aof1 = aseg_off((8 + wv) * 64 + lane, D_);
  unsigned bof[3];
  const u16* bsrc[3];
  int bld[3];
#pragma unroll
  for (int i = 0; i < 3; ++i) {
    int jj = i * 8 + wv;
    if (jj >= 22) jj -= 6;
    if (jj < 18) {
      int j = jj < 9 ? jj : jj - 9;
      bof[i] = bseg_off<9>(j * 64 + lane, F_);
      bsrc[i] = (jj < 9 ? wg : wu) + f0;
      bld[i] = F_;
    } else {
      bof[i] = bseg_off<4>(((jj - 18) & 3) * 64 + lane, 3 * D_);
      bsrc[i] = wqkv + c0;
      bld[i] = 3 * D_;
    }
  }
  const bool padlane = wv >= 2 && wv < 6 && (lane & 1) && (lane >> 4) == 3;
  auto piece = [&](int i, int kt, unsigned buf) {
    if (i == 0) glds16(xa + kt * 32, aof0, buf + wv * 1024);
    else if (i == 1) glds16(xa + kt * 32, aof1, buf + (8 + wv) * 1024);
    else if (i < 4) glds16(bsrc[i - 2] + (size_t)kt * 32 * bld[i - 2], bof[i - 2], buf + 16384 + ((i - 2) * 8 + wv) * 1024);
    else if (!padlane) glds16(bsrc[2] + (size_t)kt * 32 * bld[2], bof[2], buf + 16384 + (16 + (wv < 6 ? wv : wv - 6)) * 1024);
  };
  const int gk = lane & 31, gkb = gk >> 3;
  const int gdst = 16384 + 18432 + (gkb * 4 + 3) * 256 + ((gk & 7) ^ ((gkb & 1) << 2)) * 32 + 16;
  auto gfill = [&](int kt, unsigned off) {
    if (wv == 6 && lane < 32) {
      u32x4 v = {(unsigned)*(const u16*)(gwl + (kt * 32 + gk) * 2), 0u, 0u, 0u};
      *(u32x4*)(lds + off + gdst) = v;
    }
  };
  auto pre = [&]() {
    if (wv == 6) {
      waitv<15>();
      gfill(0, 0); gfill(1, K1_STAGE); gfill(2, 2 * K1_STAGE);
    }
  };
  if (wv == 6) {
    glds16(gate_w, lane * 16u, lds_addr(gwl));
    if (lane < 8) glds16(gate_w + 512, lane * 16u, lds_addr(gwl) + 1024);
  }
  auto waitL = [&](auto n) { waitv<5 * decltype(n)::value>(); };
  auto bar = [&]() { BARRIER(); };
  const int g = lane >> 4;
  const int bl = ((lane & 15) >> 2) * 32 + (lane & 3) * 8;
  const int aoff = (wm * 64 + (lane & 15)) * 64 + ((g ^ ((-(lane >> 2)) & 3)) << 4);
  const int bgu = 16384 + wn * 9216 + g * 9 * 256 + bl;
  const int bqq = 16384 + 18432 + (g * 4 + wn * 2) * 256 + bl;
  const int hlo = (g & 1) << 7, hhi = hlo ^ 128;
  auto ldA = [&](const char* st, int mt) { return *(const bf16x8*)(st + aoff + mt * 1024); };
  auto ldB = [&](const char* st, int nt) {
    const char* bp = st + (nt < 9 ? bgu + nt * 256 : bqq + (nt - 9) * 256);
    return bfrag2(bp + hlo, bp + hhi);
  };
  auto extra = [&](int nt, int kt, unsigned off) {
    if (nt == 5) gfill(kt, off);
    if (nt == 7 && kt >= 8 && kt < 11) {
      const unsigned i = (unsigned)(kt - 8);
      if (wv < 7) {
        unsigned o = (unsigned)bid * 20736u + (wv * 3 + i) * 1024u + lane * 16u;
        glds16(wd, o < 5308416u ? o : 0u, lds_addr(gwl) + 1152);
      } else {
        unsigned o = (unsigned)bid * 2592u + i * 1024u + lane * 16u;
        glds16(wo, o < 663552u ? o : 0u, lds_addr(gwl) + 1152);
      }
    }
  };
  gemm_loop<4, 11, 5, K1_STAGE, 1, 4>(18, lds, acc, ldA, ldB, piece, waitL, extra, bar, pre);
  BARRIER();
  int tz = threadIdx.x;
  asm volatile("" : "+v"(tz));
  const int lz = tz & 63, gz = lz >> 4, tk = lz & 15;

  float* G = (float*)(lds + 73728);
  const int hg0 = nt_blk * 3;
  {
    char* xs = lds + wv * 9216 + lz * 8;
    if (wn == 1) {
#pragma unroll
      for (int nt = 0; nt < 9; ++nt)
#pragma unroll
        for (int m2 = 0; m2 < 2; ++m2) { f32x4 t = acc[nt][m2]; acc[nt][m2] = acc[nt][2 + m2]; acc[nt][2 + m2] = t; }
    }
#pragma unroll
    for (int nt = 0; nt < 9; ++nt)
#pragma unroll
      for (int m2 = 0; m2 < 2; ++m2) {
        f32x4 v = acc[nt][2 + m2];
        *(u32x2*)(xs + (nt * 2 + m2) * 512) = (u32x2){pk2(v[0], v[1]), pk2(v[2], v[3])};
      }
    if (wn == 1 && gz == 2) {
#pragma unroll
      for (int mt = 0; mt < 4; ++mt) {
        float gv = rbf(1.f / (1.f + __expf(-rbf(acc[10][mt][0]))));
        G[wm * 64 + mt * 16 + tk] = gv;
        if (nt_blk == 0) gate[m0 + wm * 64 + mt * 16 + tk] = gv;
      }
    }
  }
  __syncthreads();
  {
    const char* xr = lds + (wv ^ 1) * 9216 + lz * 8;
    u32x2 pv[9][2];
#pragma unroll
    for (int nt = 0; nt < 9; ++nt)
#pragma unroll
      for (int m2 = 0; m2 < 2; ++m2) pv[nt][m2] = *(const u32x2*)(xr + (nt * 2 + m2) * 512);
    float og[2];
#pragma unroll
    for (int m2 = 0; m2 < 2; ++m2) og[m2] = rbf(1.f - G[wm * 64 + (wn * 2 + m2) * 16 + tk]);
    asm volatile("s_waitcnt lgkmcnt(0)" ::: "memory");
    __builtin_amdgcn_s_barrier();
#pragma unroll
    for (int nt = 0; nt < 9; ++nt)
#pragma unroll
      for (int m2 = 0; m2 < 2; ++m2) {
        f32x4 v = acc[nt][m2];
        u32x2 uv = pv[nt][m2];
        float h0 = v[0] * og[m2] * __uint_as_float(uv[0] << 16);
        float h1 = v[1] * og[m2] * __uint_as_float(uv[0] & 0xffff0000u);
        float h2 = v[2] * og[m2] * __uint_as_float(uv[1] << 16);
        float h3 = v[3] * og[m2] * __uint_as_float(uv[1] & 0xffff0000u);
        pv[nt][m2] = (u32x2){pk2(h0, h1), pk2(h2, h3)};
      }
#pragma unroll
    for (int m2 = 0; m2 < 2; ++m2) {
      char* hb = (char*)Ah + ((size_t)((mt_blk * 16 + wm * 4 + wn * 2 + m2) * 144 + ((nt_blk * 9) >> 1)) * 64 + lz) * 16;
      if ((nt_blk & 1) == 0) {
#pragma unroll
        for (int i = 0; i < 4; ++i)
          *(u32x4*)(hb + i * 1024) = (u32x4){pv[2 * i][m2][0], pv[2 * i][m2][1], pv[2 * i + 1][m2][0], pv[2 * i + 1][m2][1]};
        *(u32x2*)(hb + 4096) = pv[8][m2];
      } else {
        *(u32x2*)(hb + 8) = pv[0][m2];
#pragma unroll
        for (int i = 0; i < 4; ++i)
          *(u32x4*)(hb + (i + 1) * 1024) =
              (u32x4){pv[2 * i + 1][m2][0], pv[2 * i + 1][m2][1], pv[2 * i + 2][m2][0], pv[2 * i + 2][m2][1]};
      }
    }
  }
  {
    char* img = lds;
#pragma unroll
    for (int t2 = 0; t2 < 2; ++t2)
#pragma unroll
      for (int r = 0; r < 4; r += 2) {
        int c = wn * 32 + t2 * 16 + 4 * gz + r - coff;
        if (c >= 0 && c < 54) {
          int hl = c / 18, d = c - hl * 18;
          int which = (hg0 + hl) >> 5;
          float sc = which == 0 ? QSCALE : 1.f;
#pragma unroll
          for (int mt = 0; mt < 4; ++mt) {
            int tok = wm * 64 + mt * 16 + tk;
            unsigned v = pk2(acc[9 + t2][mt][r] * sc, acc[9 + t2][mt][r + 1] * sc);
            if (which == 2) {
              char* q = img + hl * 16384 + (tok >> 5) * 2048 + ((((tok >> 4) & 1) * 2 + ((tok >> 2) & 1)) * 32 + d) * 16 +
                        ((tok & 3) + 4 * ((tok >> 3) & 1)) * 2;
              *(u16*)q = (u16)v;
              *(u16*)(q + 16) = (u16)(v >> 16);
            } else {
              int ch = d >> 3;
              if (which == 1) ch ^= (tok >> 2) & 3;
              *(unsigned*)(img + (hl * 256 + tok) * 64 + ch * 16 + (d & 7) * 2) = v;
            }
          }
        }
      }
    for (int i = tz; i < 768; i += 512) {
      int which = (hg0 + (i >> 8)) >> 5;
      if (which == 2) {
        char* grp = img + (i >> 8) * 16384 + ((i & 255) >> 3) * 512;
        int d0 = 18 + (i & 7) * 2;
        unsigned one = d0 == 18 ? 0x3f803f80u : 0u;
        if (d0 < 32) {
          *(u32x4*)(grp + d0 * 16) = (u32x4){one, one, one, one};
          *(u32x4*)(grp + d0 * 16 + 16) = (u32x4){0u, 0u, 0u, 0u};
        }
      } else {
        int sw = which == 1 ? (i >> 2) & 3 : 0;
        char* row = img + i * 64;
        *(unsigned*)(row + (2 ^ sw) * 16 + 4) = 0u;
        *(u32x2*)(row + (2 ^ sw) * 16 + 8) = (u32x2){0u, 0u};
        *(u32x4*)(row + (3 ^ sw) * 16) = (u32x4){0u, 0u, 0u, 0u};
      }
    }
  }
  __syncthreads();
#pragma unroll
  for (int i = 0; i < 6; ++i) {
    int u = i * 512 + tz;
    int hl = u >> 10, rem = u & 1023;
    u32x4 v = *(const u32x4*)(lds + u * 16);
    *(u32x4*)(qkvp + ((size_t)(nt_blk * 3 + hl) * S_ + m0) * 32 + rem * 8) = v;
  }
}

DI void attn_stage(const u16* __restrict__ Kh, const u16* __restrict__ Vh, int c, unsigned buf, int wv, int lane) {
#pragma unroll
  for (int i = 0; i < 2; ++i) glds16(Kh + (size_t)c * 4096, ((i * 4 + wv) * 64 + lane) * 16u, buf + (i * 4 + wv) * 1024);
#pragma unroll
  for (int i = 0; i < 2; ++i)
    glds16(Vh + (size_t)c * 4096, ((i * 4 + wv) * 64 + lane) * 16u, buf + 8192 + (i * 4 + wv) * 1024);
}

DI float halfswap_max(float m) {
  auto rr = __builtin_amdgcn_permlane32_swap(__float_as_uint(m), __float_as_uint(m), false, false);
  return fmaxf(__uint_as_float(rr[0]), __uint_as_float(rr[1]));
}
DI float max16(const f32x16& s) {
  float mx = s[0];
#pragma unroll
  for (int r = 1; r < 16; ++r) mx = fmaxf(mx, s[r]);
  return halfswap_max(mx);
}
#define MFMA32(a, b, c) __builtin_amdgcn_mfma_f32_32x32x16_bf16((a), (b), (c), 0, 0, 0)

DI void exp_pack(const f32x16& s, bf16x8& p0, bf16x8& p1) {
  u32x4 a, b;
#pragma unroll
  for (int r = 0; r < 4; ++r) a[r] = pk2(__builtin_amdgcn_exp2f(s[2 * r]), __builtin_amdgcn_exp2f(s[2 * r + 1]));
#pragma unroll
  for (int r = 0; r < 4; ++r) b[r] = pk2(__builtin_amdgcn_exp2f(s[8 + 2 * r]), __builtin_amdgcn_exp2f(s[9 + 2 * r]));
  p0 = __builtin_bit_cast(bf16x8, a);
  p1 = __builtin_bit_cast(bf16x8, b);
}

#define EX2(x) __builtin_amdgcn_exp2f(x)
#define PHASE(S, PA, PB, M1, M2, M3, M4)                                              \
  do {                                                                                \
    M1; SBAR(); PA[0] = pk2(EX2(S[0]), EX2(S[1])); PA[1] = pk2(EX2(S[2]), EX2(S[3])); SBAR();  \
    M2; SBAR(); PA[2] = pk2(EX2(S[4]), EX2(S[5])); PA[3] = pk2(EX2(S[6]), EX2(S[7])); SBAR();  \
    M3; SBAR(); PB[0] = pk2(EX2(S[8]), EX2(S[9])); PB[1] = pk2(EX2(S[10]), EX2(S[11])); SBAR(); \
    M4; SBAR(); PB[2] = pk2(EX2(S[12]), EX2(S[13])); PB[3] = pk2(EX2(S[14]), EX2(S[15])); SBAR(); \
  } while (0)
#define BC8(x) __builtin_bit_cast(bf16x8, x)
DI void attn_fast(const u16* __restrict__ Kh, const u16* __restrict__ Vh, const bf16x8 (&qf)[2][2], char* lds,
                  f32x16 (&o)[2], int wv, int lane) {
  const int r32 = lane & 31, h = lane >> 5;
  const int koff0 = r32 * 64 + ((h ^ ((r32 >> 2) & 3)) << 4);
  const int koff1 = r32 * 64 + (((2 + h) ^ ((r32 >> 2) & 3)) << 4);
  const int voff = 8192 + lane * 16;
  const unsigned l0 = lds_addr(lds);
  f32x16 negm[2], sa, sb;
#pragma unroll
  for (int a = 0; a < 2; ++a)
#pragma unroll
    for (int r = 0; r < 16; ++r) { o[a][r] = 0.f; negm[a][r] = 0.f; }
  waitv<8>();
  BARRIER();
  bf16x8 kc0 = *(const bf16x8*)(lds + koff0), kc1 = *(const bf16x8*)(lds + koff1);
  {
#pragma unroll
    for (int a = 0; a < 2; ++a) {
      f32x16 s = MFMA32(kc0, qf[a][0], negm[a]);
      s = MFMA32(kc1, qf[a][1], s);
      float mx = max16(s);
#pragma unroll
      for (int r = 0; r < 16; ++r) { negm[a][r] = -mx; if (a == 0) sa[r] = s[r] - mx; }
    }
  }
  u32x4 pa0, pa1, pb0 = {0u, 0u, 0u, 0u}, pb1 = {0u, 0u, 0u, 0u};
  bf16x8 vp0 = {0, 0, 0, 0, 0, 0, 0, 0}, vp1 = {0, 0, 0, 0, 0, 0, 0, 0};
  for (int c = 0; c < 16; ++c) {
    if (c + 2 < 16) waitv<4>(); else waitv<0>();
    BARRIER();
    if (c + 3 < 16) attn_stage(Kh, Vh, c + 3, l0 + ((c + 3) & 3) * 16384, wv, lane);
    const char* buf = lds + (c & 3) * 16384;
    const char* bufn = lds + ((c + 1) & 3) * 16384;
#pragma unroll
    for (int t = 0; t < 4; ++t) {
      const char* kb = t < 3 ? buf + (t + 1) * 2048 : bufn;
      const char* vb = buf + t * 2048 + voff;
      bf16x8 kn0 = *(const bf16x8*)(kb + koff0), kn1 = *(const bf16x8*)(kb + koff1);
      bf16x8 vc0 = *(const bf16x8*)vb, vc1 = *(const bf16x8*)(vb + 1024);
      SBAR();
      PHASE(sa, pa0, pa1, sb = MFMA32(kc0, qf[1][0], negm[1]), o[1] = MFMA32(vp0, BC8(pb0), o[1]),
            sb = MFMA32(kc1, qf[1][1], sb), o[1] = MFMA32(vp1, BC8(pb1), o[1]));
      PHASE(sb, pb0, pb1, sa = MFMA32(kn0, qf[0][0], negm[0]), o[0] = MFMA32(vc0, BC8(pa0), o[0]),
            sa = MFMA32(kn1, qf[0][1], sa), o[0] = MFMA32(vc1, BC8(pa1), o[0]));
      kc0 = kn0; kc1 = kn1; vp0 = vc0; vp1 = vc1;
    }
  }
  o[1] = MFMA32(vp0, BC8(pb0), o[1]);
  o[1] = MFMA32(vp1, BC8(pb1), o[1]);
}

DI void attn_safe(const u16* __restrict__ Kh, const u16* __restrict__ Vh, const bf16x8 (&qf)[2][2], char* lds,
                  f32x16 (&o)[2], int wv, int lane) {
  const int r32 = lane & 31, h = lane >> 5;
  const int koff0 = r32 * 64 + ((h ^ ((r32 >> 2) & 3)) << 4);
  const int koff1 = r32 * 64 + (((2 + h) ^ ((r32 >> 2) & 3)) << 4);
  const int voff = 8192 + lane * 16;
  const unsigned l0 = lds_addr(lds);
  f32x16 zero;
  float m[2] = {-INFINITY, -INFINITY};
#pragma unroll
  for (int r = 0; r < 16; ++r) { o[0][r] = 0.f; o[1][r] = 0.f; zero[r] = 0.f; }
  for (int c = 0; c < 16; ++c) {
    BARRIER();
    attn_stage(Kh, Vh, c, l0, wv, lane);
    waitv<0>();
    BARRIER();
    for (int t = 0; t < 4; ++t) {
      const char* kb = lds + t * 2048;
      bf16x8 k0 = *(const bf16x8*)(kb + koff0), k1 = *(const bf16x8*)(kb + koff1);
      bf16x8 va = *(const bf16x8*)(kb + voff), vb = *(const bf16x8*)(kb + voff + 1024);
#pragma unroll
      for (int a = 0; a < 2; ++a) {
        f32x16 s = MFMA32(k0, qf[a][0], zero);
        s = MFMA32(k1, qf[a][1], s);
        float mn = fmaxf(m[a], max16(s));
        float al = __builtin_amdgcn_exp2f(m[a] - mn);
        m[a] = mn;
#pragma unroll
        for (int r = 0; r < 16; ++r) { o[a][r] *= al; s[r] -= mn; }
        bf16x8 p0, p1;
        exp_pack(s, p0, p1);
        o[a] = MFMA32(va, p0, o[a]);
        o[a] = MFMA32(vb, p1, o[a]);
      }
    }
  }
}

DI void attn_block(int bid, const u16* __restrict__ qkvp, const float* __restrict__ gate, u16* __restrict__ Ao,
                   char* lds) {
  const int tid = threadIdx.x, lane = tid & 63, r32 = lane & 31, h = lane >> 5;
  const int wv = __builtin_amdgcn_readfirstlane(tid >> 6);
  const int head = (bid & 7) * 4 + ((bid >> 3) & 3), qb = bid >> 5;
  const u16* Qh = qkvp + (size_t)head * (S_ * 32);
  const u16* Kh = qkvp + (size_t)(H_ + head) * (S_ * 32);
  const u16* Vh = qkvp + (size_t)(2 * H_ + head) * (S_ * 32);
  const int qrow0 = qb * 256 + wv * 64 + r32;
  {
    const unsigned l0 = lds_addr(lds);
    attn_stage(Kh, Vh, 0, l0, wv, lane);
    attn_stage(Kh, Vh, 1, l0 + 16384, wv, lane);
    attn_stage(Kh, Vh, 2, l0 + 32768, wv, lane);
  }
  bf16x8 qf[2][2];
#pragma unroll
  for (int a = 0; a < 2; ++a) {
    qf[a][0] = *(const bf16x8*)(Qh + (size_t)(qrow0 + a * 32) * 32 + h * 8);
    qf[a][1] = *(const bf16x8*)(Qh + (size_t)(qrow0 + a * 32) * 32 + 16 + h * 8);
  }
  float gq[2] = {gate[qrow0], gate[qrow0 + 32]};
  asm volatile("" ::"v"(qf[0][0]), "v"(qf[0][1]), "v"(qf[1][0]), "v"(qf[1][1]), "v"(gq[0]), "v"(gq[1]));
  f32x16 o[2];
  attn_fast(Kh, Vh, qf, lds, o, wv, lane);
  bool bad = false;
#pragma unroll
  for (int a = 0; a < 2; ++a)
#pragma unroll
    for (int r = 0; r < 11; ++r) bad |= (r < 8 || h == 0) && !(fabsf(o[a][r]) < 1e30f);
  if (__syncthreads_or(bad)) attn_safe(Kh, Vh, qf, lds, o, wv, lane);
#pragma unroll
  for (int a = 0; a < 2; ++a) {
    float l = __shfl(o[a][10], r32);
    float sc = gq[a] / l;
    const int q = qrow0 + a * 32;
    char* rowb = (char*)Ao + (size_t)(q >> 4) * (18 * 1024) + (q & 15) * 16;
    const int U = head * 2 + h;
    *(u32x4*)(rowb + (U >> 2) * 1024 + (U & 3) * 256) = (u32x4){pk2(o[a][0] * sc, o[a][1] * sc), pk2(o[a][2] * sc, o[a][3] * sc),
                                                                pk2(o[a][4] * sc, o[a][5] * sc), pk2(o[a][6] * sc, o[a][7] * sc)};
    const int U2 = 64 + (head >> 2);
    if (h == 0) *(unsigned*)(rowb + (U2 >> 2) * 1024 + (U2 & 3) * 256 + (head & 3) * 4) = pk2(o[a][8] * sc, o[a][9] * sc);
  }
}

constexpr int K2_STAGE = 16384 + 18432;
DI void down_block(int bid, const u16* __restrict__ Ah, const u16* __restrict__ wd, u16* __restrict__ part,
                   char* lds) {
  const int tid = threadIdx.x, lane = tid & 63;
  const int wv = __builtin_amdgcn_readfirstlane(tid >> 6);
  const int mt_blk = (bid & 7) * 2 + ((bid >> 3) & 1), rest = bid >> 4;
  const int nt_blk = rest & 3, sp = rest >> 2;
  const int m0 = mt_blk * 128, n0 = nt_blk * 144;
  f32x4 acc[9][2] = {};
  const char* pa = (const char*)Ah + ((size_t)(mt_blk * 8) * 144 + sp * 36) * 1024;
  const u16* pb = wd + (size_t)sp * 1152 * D_ + n0;
  const unsigned l0 = lds_addr(lds);
  const unsigned aof = (unsigned)(wv * 2 * 144 * 1024 + lane * 16);
  unsigned bof[5];
#pragma unroll
  for (int i = 0; i < 5; ++i) {
    int u = (i < 4 ? i * 4 + wv : 16 + (wv & 1)) * 64 + lane;
    int st = u >> 4, kblk = st / 9, nblk = st - kblk * 9;
    int rr = ((u >> 1) & 7) ^ ((kblk & 1) << 2);
    int k = (kblk >> 2) * 32 + (rr >> 2) * 16 + (kblk & 3) * 4 + (rr & 3);
    bof[i] = (unsigned)(k * D_ + nblk * 16 + (u & 1) * 8) * 2u;
  }
  auto piece = [&](int i, int kt, unsigned buf) {
    if (i < 4) glds16(pa + kt * 2048 + ((i >> 1) * 144 + (i & 1)) * 1024, aof, buf + (wv * 4 + i) * 1024);
    else if (i < 8) glds16(pb + (size_t)kt * 64 * D_, bof[i - 4], buf + 16384 + ((i - 4) * 4 + wv) * 1024);
    else if (wv < 2) glds16(pb + (size_t)kt * 64 * D_, bof[4], buf + 16384 + (16 + wv) * 1024);
  };
  const int g = lane >> 4;
  const int aoff0 = wv * 4096 + lane * 16, aoff1 = aoff0 + 1024;
  const int boff = 16384 + g * 9 * 256 + ((lane & 15) >> 2) * 32 + (lane & 3) * 8;
  const int hlo = (g & 1) << 7, hhi = hlo ^ 128;
#pragma unroll
  for (int i = 0; i < 9; ++i) piece(i, 0, l0);
  for (int kt = 0; kt < 18; ++kt) {
    waitv<0>();
    BARRIER();
    const char* cur = lds + (kt & 1) * K2_STAGE;
    const unsigned dst = l0 + ((kt + 1) & 1) * K2_STAGE;
    const bool more = kt + 1 < 18;
    bf16x8 af[2][2];
#pragma unroll
    for (int mt = 0; mt < 2; ++mt) {
      af[0][mt] = *(const bf16x8*)(cur + aoff0 + mt * 2048);
      af[1][mt] = *(const bf16x8*)(cur + aoff1 + mt * 2048);
    }
    bf16x8 bw0 = bfrag2(cur + boff + hlo, cur + boff + hhi);
    bf16x8 bw1 = bfrag2(cur + boff + 256 + hlo, cur + boff + 256 + hhi);
#pragma unroll
    for (int q = 0; q < 18; ++q) {
      bf16x8 b = (q & 1) ? bw1 : bw0;
      if (q + 2 < 18) {
        const int q2 = q + 2, o2 = ((q2 / 9) * 36 + q2 % 9) * 256;
        bf16x8 nb = bfrag2(cur + boff + o2 + hlo, cur + boff + o2 + hhi);
        if (q & 1) bw1 = nb; else bw0 = nb;
      }
      if (q < 9 && more) piece(q, kt + 1, dst);
#pragma unroll
      for (int mt = 0; mt < 2; ++mt)
        acc[q % 9][mt] = __builtin_amdgcn_mfma_f32_16x16x32_bf16(b, af[q / 9][mt], acc[q % 9][mt], 0, 0, 0);
      SBAR();
    }
  }
  u16* dst = part + ((size_t)((sp * 64 + mt_blk * 4 + wv) * 36 + nt_blk * 9) * 64 + lane) * 8;
#pragma unroll
  for (int nt = 0; nt < 9; ++nt)
    *(u32x4*)(dst + nt * 512) = (u32x4){pk2(acc[nt][0][0], acc[nt][0][1]), pk2(acc[nt][0][2], acc[nt][0][3]),
                                        pk2(acc[nt][1][0], acc[nt][1][1]), pk2(acc[nt][1][2], acc[nt][1][3])};
}

__global__ __launch_bounds__(256, 2) void k_mid(const u16* __restrict__ qkvp, const float* __restrict__ gate,
                                                u16* __restrict__ Ao, const u16* __restrict__ Ah,
                                                const u16* __restrict__ wd, u16* __restrict__ part) {
  __shared__ __attribute__((aligned(16))) char lds[2 * K2_STAGE];
  const int bid = blockIdx.x;
  if (bid < 256) attn_block(bid, qkvp, gate, Ao, lds);
  else down_block(bid - 256, Ah, wd, part, lds);
}

constexpr int K3_STAGE = 4096 + 6144;
__global__ __launch_bounds__(256) void k_out(const u16* __restrict__ Ao, const u16* __restrict__ wo,
                                             const u16* __restrict__ part, u16* __restrict__ out) {
  __shared__ __attribute__((aligned(16))) char lds[15 * K3_STAGE];
  const int tid = threadIdx.x, lane = tid & 63;
  const int wv = __builtin_amdgcn_readfirstlane(tid >> 6), wm = wv >> 1, wn = wv & 1;
  const int nt_blk = blockIdx.x % 6, mt_blk = blockIdx.x / 6;
  const int m0 = mt_blk * 64, n0 = nt_blk * 96;
  f32x4 acc[3][2] = {};
  const char* pa = (const char*)Ao + (size_t)(mt_blk * 4 + wv) * (18 * 1024);
  const u16* pb = wo + n0;
  unsigned bofA[2], bofB[2];
#pragma unroll
  for (int i = 0; i < 2; ++i) {
    int u = (i == 0 ? wv : 4 + (wv & 1)) * 64 + lane;
    int st = u >> 4, kb = st / 6, nblk = st - kb * 6;
    int j = ((u >> 1) & 7) ^ ((kb & 1) << 2);
    int col = nblk * 16 + (u & 1) * 8;
    bofA[i] = (unsigned)(((kb >> 1) * 18 + 4 * (kb & 1) + (j & 3) + 8 * (j >> 2)) * D_ + col) * 2u;
    bofB[i] = (unsigned)(((4 * kb + (j >> 1)) * 18 + 16 + (j & 1)) * D_ + col) * 2u;
  }
  auto piece = [&](int i, int kt, unsigned buf) {
    if (i == 0) glds16(pa + kt * 1024, lane * 16u, buf + wv * 1024);
    else if (i == 1 || wv < 2) {
      const u16* bb = kt < 16 ? pb + (size_t)kt * 36 * D_ : pb + (size_t)(kt - 16) * 288 * D_;
      unsigned o = kt < 16 ? bofA[i - 1] : bofB[i - 1];
      glds16(bb, o, buf + 4096 + (i == 1 ? wv : 4 + wv) * 1024);
    }
  };
  auto waitL = [&](auto n) { if (wv < 2) waitv<9 * decltype(n)::value>(); else waitv<6 * decltype(n)::value>(); };
  auto bar = [&]() { BARRIER(); };
  const int g = lane >> 4;
  const int aoff = wm * 2048 + lane * 16;
  const int boff = 4096 + (g * 6 + wn * 3) * 256 + ((lane & 15) >> 2) * 32 + (lane & 3) * 8;
  const int hlo = (g & 1) << 7, hhi = hlo ^ 128;
  auto ldA = [&](const char* st, int mt) { return *(const bf16x8*)(st + aoff + mt * 1024); };
  auto ldB = [&](const char* st, int nt) { return bfrag2(st + boff + nt * 256 + hlo, st + boff + nt * 256 + hhi); };
  auto extra = [&](int, int, unsigned) {};
  u32x4 pv[3][4];
  auto extra0 = [&]() {
#pragma unroll
    for (int nt = 0; nt < 3; ++nt)
#pragma unroll
      for (int sp = 0; sp < 4; ++sp)
        pv[nt][sp] = __builtin_nontemporal_load(
            (const u32x4*)(part + ((size_t)((sp * 64 + mt_blk * 2 + wm) * 36 + nt_blk * 6 + wn * 3 + nt) * 64 + lane) * 8));
  };
  gemm_loop<2, 3, 3, K3_STAGE, 3, 5>(6, lds, acc, ldA, ldB, piece, waitL, extra, bar, extra0);
#pragma unroll
  for (int nt = 0; nt < 3; ++nt)
#pragma unroll
    for (int mt = 0; mt < 2; ++mt) {
      size_t off = (size_t)(m0 + wm * 32 + mt * 16 + (lane & 15)) * D_ + n0 + wn * 48 + nt * 16 + 4 * (lane >> 4);
#pragma unroll
      for (int sp = 0; sp < 4; ++sp) {
        u32x2 w = {pv[nt][sp][mt * 2], pv[nt][sp][mt * 2 + 1]};
        acc[nt][mt] += (f32x4){__uint_as_float(w[0] << 16), __uint_as_float(w[0] & 0xffff0000u), __uint_as_float(w[1] << 16),
                               __uint_as_float(w[1] & 0xffff0000u)};
      }
      u32x2 v = {pk2(acc[nt][mt][0], acc[nt][mt][1]), pk2(acc[nt][mt][2], acc[nt][mt][3])};
      *(u32x2*)(out + off) = v;
    }
}

extern "C" void kernel_launch(void* const* d_in, const int* in_sizes, int n_in, void* d_out, int out_size,
                              void* d_ws, size_t ws_size, hipStream_t stream) {
  const u16* x = (const u16*)d_in[0];
  const u16* wqkv = (const u16*)d_in[1];
  const u16* wo = (const u16*)d_in[2];
  const u16* wg = (const u16*)d_in[4];
  const u16* wu = (const u16*)d_in[5];
  const u16* wd = (const u16*)d_in[6];
  const u16* gw = (const u16*)d_in[7];
  char* ws = (char*)d_ws;
  u16* qkvp = (u16*)ws;
  float* gate = (float*)(ws + (size_t)16 * 1024 * 1024);
  u16* Ao = (u16*)(ws + (size_t)17 * 1024 * 1024);
  u16* Ah = (u16*)(ws + (size_t)20 * 1024 * 1024);
  u16* part = (u16*)(ws + (size_t)48 * 1024 * 1024);
  k_proj<<<256, 512, 0, stream>>>(x, wqkv, wg, wu, gw, qkvp, gate, Ah, wd, wo);
  k_mid<<<512, 256, 0, stream>>>(qkvp, gate, Ao, Ah, wd, part);
  k_out<<<192, 256, 0, stream>>>(Ao, wo, part, (u16*)d_out);
}
```

```cpp
#include <hip/hip_runtime.h>
#include <hip/hip_bf16.h>
#include <type_traits>

typedef __attribute__((ext_vector_type(8))) short bf16x8;
typedef __attribute__((ext_vector_type(4))) short s16x4;
typedef __attribute__((ext_vector_type(4))) float f32x4;
typedef __attribute__((ext_vector_type(2))) float f32x2;
typedef __attribute__((ext_vector_type(16))) float f32x16;
typedef __attribute__((ext_vector_type(4))) unsigned u32x4;
typedef __attribute__((ext_vector_type(2))) unsigned u32x2;
typedef __attribute__((ext_vector_type(2))) __bf16 bf16x2_t;
typedef unsigned short u16;

#define DI __device__ __forceinline__
#define LDSP(T) __attribute__((address_space(3))) T

constexpr int S_ = 2048, D_ = 576, H_ = 32, DH_ = 18, F_ = 4608;

DI unsigned pk2(float lo, float hi) {
  f32x2 v = {lo, hi};
  bf16x2_t b = __builtin_convertvector(v, bf16x2_t);
  return __builtin_bit_cast(unsigned, b);
}
DI float rbf(float x) { return __uint_as_float(pk2(x, 0.f) << 16); }

DI void glds16(const void* sbase, unsigned voff, unsigned lds_base) {
  unsigned sv;
  asm volatile("s_mov_b32 %0, m0\n\ts_mov_b32 m0, %3\n\ts_nop 0\n\tglobal_load_lds_dwordx4 %1, %2\n\ts_mov_b32 m0, %0"
               : "=&s"(sv) : "v"(voff), "s"(sbase), "s"(__builtin_amdgcn_readfirstlane(lds_base)) : "memory");
}
DI unsigned lds_addr(const char* p) { return (unsigned)(size_t)(LDSP(const char)*)p; }
DI s16x4 trread(const char* p) { return __builtin_amdgcn_ds_read_tr16_b64_v4i16((LDSP(s16x4)*)p); }
template <int N> DI void waitv() { asm volatile("s_waitcnt vmcnt(%0)" ::"n"(N) : "memory"); }
#define BARRIER() do { asm volatile("s_waitcnt lgkmcnt(0)" ::: "memory"); __builtin_amdgcn_s_barrier(); asm volatile("" ::: "memory"); } while (0)

#define SBAR() __builtin_amdgcn_sched_barrier(0)
template <int NBs>
DI unsigned bseg_off(int u, int ld) {
  int st = u >> 4, kblk = st / NBs, nblk = st - kblk * NBs;
  int rr = (u >> 1) & 7, hf = u & 1;
  int k = kblk * 8 + (rr ^ ((kblk & 1) << 2));
  return (unsigned)(k * ld + nblk * 16 + hf * 8) * 2u;
}
DI unsigned aseg_off(int u, int lda) {
  int row = u >> 2, ch = (u & 3) ^ ((-(row >> 2)) & 3);
  return (unsigned)(row * lda + ch * 8) * 2u;
}
DI bf16x8 bfrag2(const char* lo, const char* hi) {
  s16x4 a = trread(lo), b = trread(hi);
  return __builtin_shufflevector(a, b, 0, 1, 2, 3, 4, 5, 6, 7);
}

template <int N> using IC = std::integral_constant<int, N>;
template <int MT, int NT, int LMAX, int STAGE, int KS, int NR, class LdA, class LdB, class Piece, class WaitL, class Extra, class Bar, class Pre>
DI void gemm_loop(int nsup, char* lds, f32x4 (&acc)[NT][MT], LdA ldA, LdB ldB, Piece piece, WaitL waitL, Extra extra, Bar bar, Pre pre) {
  static_assert(NT >= MT + 1 && NT >= LMAX, "schedule");
  constexpr int SUP = KS * STAGE;
  const unsigned l0 = lds_addr(lds);
#pragma unroll
  for (int s = 0; s < (NR - 1) * KS; ++s)
#pragma unroll
    for (int i = 0; i < LMAX; ++i) piece(i, s, l0 + s * STAGE);
  pre();
  waitL(IC<NR - 2>{});
  bar();
  bf16x8 af[MT], bw0, bw1;
#pragma unroll
  for (int mt = 0; mt < MT; ++mt) af[mt] = ldA(lds, mt);
  bw0 = ldB(lds, 0);
  bw1 = ldB(lds, 1);
  int cb = 0;
  auto body = [&](int j, auto wn_, auto more_) {
    constexpr bool more = decltype(more_)::value;
    waitL(wn_);
    bar();
    const int nb_ = cb + 1 == NR ? 0 : cb + 1, pb_ = cb == 0 ? NR - 1 : cb - 1;
    const char* base = lds + cb * SUP;
    const char* nbase = lds + nb_ * SUP;
    const unsigned dst = l0 + pb_ * SUP;
#pragma unroll
    for (int ks = 0; ks < KS; ++ks) {
      const char* cur = base + ks * STAGE;
      const char* nx = ks + 1 < KS ? base + (ks + 1) * STAGE : nbase;
      bf16x8 an[MT];
#pragma unroll
      for (int nt = 0; nt < NT; ++nt) {
        bf16x8 b = (nt & 1) ? bw1 : bw0;
        bf16x8 nb = nt + 2 < NT ? ldB(cur, nt + 2) : ldB(nx, nt + 2 - NT);
        if (nt & 1) bw1 = nb; else bw0 = nb;
        if (nt >= NT - 1 - MT && nt < NT - 1) an[nt - (NT - 1 - MT)] = ldA(nx, nt - (NT - 1 - MT));
        if (nt < LMAX && more) piece(nt, (j + NR - 1) * KS + ks, dst + ks * STAGE);
#pragma unroll
        for (int mt = 0; mt < MT; ++mt) acc[nt][mt] = __builtin_amdgcn_mfma_f32_16x16x32_bf16(b, af[mt], acc[nt][mt], 0, 0, 0);
        if (more) extra(nt, (j + NR - 1) * KS + ks, dst + ks * STAGE - l0);
        SBAR();
      }
#pragma unroll
      for (int mt = 0; mt < MT; ++mt) af[mt] = an[mt];
      if (NT & 1) { bf16x8 t = bw0; bw0 = bw1; bw1 = t; }
    }
    cb = nb_;
  };
  for (int j = 0; j < nsup - (NR - 1); ++j) body(j, IC<NR - 3>{}, IC<1>{});
#pragma unroll 1
  for (int j = nsup - (NR - 1); j < nsup; ++j) body(j, IC<0>{}, IC<0>{});
}

constexpr float QSCALE = 0.23570226039551584f * 1.4426950408889634f;
constexpr int K1_STAGE = 16384 + 9216 + 9216 + 4096;

__global__ __launch_bounds__(512) void k_proj(const u16* __restrict__ x, const u16* __restrict__ wqkv,
                                              const u16* __restrict__ wg, const u16* __restrict__ wu,
                                              const u16* __restrict__ gate_w, u16* __restrict__ qkvp,
                                              float* __restrict__ gate, u16* __restrict__ Ah,
                                              const u16* __restrict__ wd, const u16* __restrict__ wo) {
  __shared__ __attribute__((aligned(16))) char lds[4 * K1_STAGE + 1152 + 1024];
  const int tid = threadIdx.x, lane = tid & 63;
  const int wv = __builtin_amdgcn_readfirstlane(tid >> 6), wm = wv >> 1, wn = wv & 1;
  const int bid = blockIdx.x;
  const int nt_blk = (bid & 7) * 4 + ((bid >> 3) & 3), mt_blk = bid >> 5;
  const int m0 = mt_blk * 256, f0 = nt_blk * 144;
  const int coff = nt_blk == 31 ? 2 : 0;
  const int c0 = nt_blk * 54 - coff;
  char* gwl = lds + 4 * K1_STAGE;

  f32x4 acc[22][2] = {};
  const u16* xa = x + (size_t)m0 * D_;
  const unsigned aof0 = aseg_off(wv * 64 + lane, D_), aof1 = aseg_off((8 + wv) * 64 + lane, D_);
  unsigned bof[3];
  const u16* bsrc[3];
  int bld[3];
#pragma unroll
  for (int i = 0; i < 3; ++i) {
    int jj = i * 8 + wv;
    if (jj >= 22) jj -= 6;
    if (jj < 18) {
      int j = jj < 9 ? jj : jj - 9;
      bof[i] = bseg_off<9>(j * 64 + lane, F_);
      bsrc[i] = (jj < 9 ? wg : wu) + f0;
      bld[i] = F_;
    } else {
      bof[i] = bseg_off<4>(((jj - 18) & 3) * 64 + lane, 3 * D_);
      bsrc[i] = wqkv + c0;
      bld[i] = 3 * D_;
    }
  }
  const bool padlane = wv >= 2 && wv < 6 && (lane & 1) && (lane >> 4) == 3;
  auto piece = [&](int i, int kt, unsigned buf) {
    if (i == 0) glds16(xa + kt * 32, aof0, buf + wv * 1024);
    else if (i == 1) glds16(xa + kt * 32, aof1, buf + (8 + wv) * 1024);
    else if (i < 4) glds16(bsrc[i - 2] + (size_t)kt * 32 * bld[i - 2], bof[i - 2], buf + 16384 + ((i - 2) * 8 + wv) * 1024);
    else if (!padlane) glds16(bsrc[2] + (size_t)kt * 32 * bld[2], bof[2], buf + 16384 + (16 + (wv < 6 ? wv : wv - 6)) * 1024);
  };
  const int gk = lane & 31, gkb = gk >> 3;
  const int gdst = 16384 + 18432 + (gkb * 4 + 3) * 256 + ((gk & 7) ^ ((gkb & 1) << 2)) * 32 + 16;
  auto gfill = [&](int kt, unsigned off) {
    if (wv == 6 && lane < 32) {
      u32x4 v = {(unsigned)*(const u16*)(gwl + (kt * 32 + gk) * 2), 0u, 0u, 0u};
      *(u32x4*)(lds + off + gdst) = v;
    }
  };
  auto pre = [&]() {
    if (wv == 6) {
      waitv<15>();
      gfill(0, 0); gfill(1, K1_STAGE); gfill(2, 2 * K1_STAGE);
    }
  };
  if (wv == 6) {
    glds16(gate_w, lane * 16u, lds_addr(gwl));
    if (lane < 8) glds16(gate_w + 512, lane * 16u, lds_addr(gwl) + 1024);
  }
  auto waitL = [&](auto n) { waitv<5 * decltype(n)::value>(); };
  auto bar = [&]() { BARRIER(); };
  const int g = lane >> 4;
  const int bl = ((lane & 15) >> 2) * 32 + (lane & 3) * 8;
  const int aoff = (wv * 32 + (lane & 15)) * 64 + ((g ^ ((-(lane >> 2)) & 3)) << 4);
  const int bgu = 16384 + g * 9 * 256 + bl;
  const int bqq = 16384 + 18432 + g * 4 * 256 + bl;
  const int hlo = (g & 1) << 7, hhi = hlo ^ 128;
  auto ldA = [&](const char* st, int mt) { return *(const bf16x8*)(st + aoff + mt * 1024); };
  auto ldB = [&](const char* st, int nt) {
    const char* bp = st + (nt < 9 ? bgu + nt * 256 : nt < 18 ? bgu + 9216 + (nt - 9) * 256 : bqq + (nt - 18) * 256);
    return bfrag2(bp + hlo, bp + hhi);
  };
  auto extra = [&](int nt, int kt, unsigned off) {
    if (nt == 5) gfill(kt, off);
    if (nt == 7 && kt >= 8 && kt < 11) {
      const unsigned i = (unsigned)(kt - 8);
      if (wv < 7) {
        unsigned o = (unsigned)bid * 20736u + (wv * 3 + i) * 1024u + lane * 16u;
        glds16(wd, o < 5308416u ? o : 0u, lds_addr(gwl) + 1152);
      } else {
        unsigned o = (unsigned)bid * 2592u + i * 1024u + lane * 16u;
        glds16(wo, o < 663552u ? o : 0u, lds_addr(gwl) + 1152);
      }
    }
  };
  gemm_loop<2, 22, 5, K1_STAGE, 1, 4>(18, lds, acc, ldA, ldB, piece, waitL, extra, bar, pre);
  BARRIER();
  int tz = threadIdx.x;
  asm volatile("" : "+v"(tz));
  const int lz = tz & 63, gz = lz >> 4, tk = lz & 15;

  const int hg0 = nt_blk * 3;
#pragma unroll
  for (int mt = 0; mt < 2; ++mt) {
    float z = __shfl(acc[21][mt][0], 32 + tk);
    float gv = rbf(1.f / (1.f + __expf(-rbf(z))));
    if (nt_blk == 0 && gz == 0) gate[m0 + wv * 32 + mt * 16 + tk] = gv;
    const float og = rbf(1.f - gv);
    u32x2 hv[9];
#pragma unroll
    for (int nt = 0; nt < 9; ++nt) {
      f32x4 a = acc[nt][mt], b = acc[9 + nt][mt];
      hv[nt] = (u32x2){pk2(a[0] * og * b[0], a[1] * og * b[1]), pk2(a[2] * og * b[2], a[3] * og * b[3])};
    }
    char* hb = (char*)Ah + ((size_t)((mt_blk * 16 + wv * 2 + mt) * 144 + ((nt_blk * 9) >> 1)) * 64 + lz) * 16;
    if ((nt_blk & 1) == 0) {
#pragma unroll
      for (int i = 0; i < 4; ++i) *(u32x4*)(hb + i * 1024) = (u32x4){hv[2 * i][0], hv[2 * i][1], hv[2 * i + 1][0], hv[2 * i + 1][1]};
      *(u32x2*)(hb + 4096) = hv[8];
    } else {
      *(u32x2*)(hb + 8) = hv[0];
#pragma unroll
      for (int i = 0; i < 4; ++i)
        *(u32x4*)(hb + (i + 1) * 1024) = (u32x4){hv[2 * i + 1][0], hv[2 * i + 1][1], hv[2 * i + 2][0], hv[2 * i + 2][1]};
    }
  }
  {
    char* img = lds;
#pragma unroll
    for (int t2 = 0; t2 < 4; ++t2)
#pragma unroll
      for (int r = 0; r < 4; r += 2) {
        int c = t2 * 16 + 4 * gz + r - coff;
        if (c >= 0 && c < 54) {
          int hl = c / 18, d = c - hl * 18;
          int which = (hg0 + hl) >> 5;
          float sc = which == 0 ? QSCALE : 1.f;
#pragma unroll
          for (int mt = 0; mt < 2; ++mt) {
            int tok = wv * 32 + mt * 16 + tk;
            unsigned v = pk2(acc[18 + t2][mt][r] * sc, acc[18 + t2][mt][r + 1] * sc);
            if (which == 2) {
              char* q = img + hl * 16384 + (tok >> 5) * 2048 + ((((tok >> 4) & 1) * 2 + ((tok >> 2) & 1)) * 32 + d) * 16 +
                        ((tok & 3) + 4 * ((tok >> 3) & 1)) * 2;
              *(u16*)q = (u16)v;
              *(u16*)(q + 16) = (u16)(v >> 16);
            } else {
              int ch = d >> 3;
              if (which == 1) ch ^= (tok >> 2) & 3;
              *(unsigned*)(img + (hl * 256 + tok) * 64 + ch * 16 + (d & 7) * 2) = v;
            }
          }
        }
      }
    for (int i = tz; i < 768; i += 512) {
      int which = (hg0 + (i >> 8)) >> 5;
      if (which == 2) {
        char* grp = img + (i >> 8) * 16384 + ((i & 255) >> 3) * 512;
        int d0 = 18 + (i & 7) * 2;
        unsigned one = d0 == 18 ? 0x3f803f80u : 0u;
        if (d0 < 32) {
          *(u32x4*)(grp + d0 * 16) = (u32x4){one, one, one, one};
          *(u32x4*)(grp + d0 * 16 + 16) = (u32x4){0u, 0u, 0u, 0u};
        }
      } else {
        int sw = which == 1 ? (i >> 2) & 3 : 0;
        char* row = img + i * 64;
        *(unsigned*)(row + (2 ^ sw) * 16 + 4) = 0u;
        *(u32x2*)(row + (2 ^ sw) * 16 + 8) = (u32x2){0u, 0u};
        *(u32x4*)(row + (3 ^ sw) * 16) = (u32x4){0u, 0u, 0u, 0u};
      }
    }
  }
  __syncthreads();
#pragma unroll
  for (int i = 0; i < 6; ++i) {
    int u = i * 512 + tz;
    int hl = u >> 10, rem = u & 1023;
    u32x4 v = *(const u32x4*)(lds + u * 16);
    *(u32x4*)(qkvp + ((size_t)(nt_blk * 3 + hl) * S_ + m0) * 32 + rem * 8) = v;
  }
}

DI void attn_stage(const u16* __restrict__ Kh, const u16* __restrict__ Vh, int c, unsigned buf, int wv, int lane) {
#pragma unroll
  for (int i = 0; i < 2; ++i) glds16(Kh + (size_t)c * 4096, ((i * 4 + wv) * 64 + lane) * 16u, buf + (i * 4 + wv) * 1024);
#pragma unroll
  for (int i = 0; i < 2; ++i)
    glds16(Vh + (size_t)c * 4096, ((i * 4 + wv) * 64 + lane) * 16u, buf + 8192 + (i * 4 + wv) * 1024);
}

DI float halfswap_max(float m) {
  auto rr = __builtin_amdgcn_permlane32_swap(__float_as_uint(m), __float_as_uint(m), false, false);
  return fmaxf(__uint_as_float(rr[0]), __uint_as_float(rr[1]));
}
DI float max16(const f32x16& s) {
  float mx = s[0];
#pragma unroll
  for (int r = 1; r < 16; ++r) mx = fmaxf(mx, s[r]);
  return halfswap_max(mx);
}
#define MFMA32(a, b, c) __builtin_amdgcn_mfma_f32_32x32x16_bf16((a), (b), (c), 0, 0, 0)

DI void exp_pack(const f32x16& s, bf16x8& p0, bf16x8& p1) {
  u32x4 a, b;
#pragma unroll
  for (int r = 0; r < 4; ++r) a[r] = pk2(__builtin_amdgcn_exp2f(s[2 * r]), __builtin_amdgcn_exp2f(s[2 * r + 1]));
#pragma unroll
  for (int r = 0; r < 4; ++r) b[r] = pk2(__builtin_amdgcn_exp2f(s[8 + 2 * r]), __builtin_amdgcn_exp2f(s[9 + 2 * r]));
  p0 = __builtin_bit_cast(bf16x8, a);
  p1 = __builtin_bit_cast(bf16x8, b);
}

#define EX2(x) __builtin_amdgcn_exp2f(x)
#define PHASE(S, PA, PB, M1, M2, M3, M4)                                              \
  do {                                                                                \
    M1; SBAR(); PA[0] = pk2(EX2(S[0]), EX2(S[1])); PA[1] = pk2(EX2(S[2]), EX2(S[3])); SBAR();  \
    M2; SBAR(); PA[2] = pk2(EX2(S[4]), EX2(S[5])); PA[3] = pk2(EX2(S[6]), EX2(S[7])); SBAR();  \
    M3; SBAR(); PB[0] = pk2(EX2(S[8]), EX2(S[9])); PB[1] = pk2(EX2(S[10]), EX2(S[11])); SBAR(); \
    M4; SBAR(); PB[2] = pk2(EX2(S[12]), EX2(S[13])); PB[3] = pk2(EX2(S[14]), EX2(S[15])); SBAR(); \
  } while (0)
#define BC8(x) __builtin_bit_cast(bf16x8, x)
DI void attn_fast(const u16* __restrict__ Kh, const u16* __restrict__ Vh, const bf16x8 (&qf)[2][2], char* lds,
                  f32x16 (&o)[2], int wv, int lane) {
  const int r32 = lane & 31, h = lane >> 5;
  const int koff0 = r32 * 64 + ((h ^ ((r32 >> 2) & 3)) << 4);
  const int koff1 = r32 * 64 + (((2 + h) ^ ((r32 >> 2) & 3)) << 4);
  const int voff = 8192 + lane * 16;
  const unsigned l0 = lds_addr(lds);
  f32x16 negm[2], sa, sb;
#pragma unroll
  for (int a = 0; a < 2; ++a)
#pragma unroll
    for (int r = 0; r < 16; ++r) { o[a][r] = 0.f; negm[a][r] = 0.f; }
  waitv<8>();
  BARRIER();
  bf16x8 kc0 = *(const bf16x8*)(lds + koff0), kc1 = *(const bf16x8*)(lds + koff1);
  {
#pragma unroll
    for (int a = 0; a < 2; ++a) {
      f32x16 s = MFMA32(kc0, qf[a][0], negm[a]);
      s = MFMA32(kc1, qf[a][1], s);
      float mx = max16(s);
#pragma unroll
      for (int r = 0; r < 16; ++r) { negm[a][r] = -mx; if (a == 0) sa[r] = s[r] - mx; }
    }
  }
  u32x4 pa0, pa1, pb0 = {0u, 0u, 0u, 0u}, pb1 = {0u, 0u, 0u, 0u};
  bf16x8 vp0 = {0, 0, 0, 0, 0, 0, 0, 0}, vp1 = {0, 0, 0, 0, 0, 0, 0, 0};
  for (int c = 0; c < 16; ++c) {
    if (c + 2 < 16) waitv<4>(); else waitv<0>();
    BARRIER();
    if (c + 3 < 16) attn_stage(Kh, Vh, c + 3, l0 + ((c + 3) & 3) * 16384, wv, lane);
    const char* buf = lds + (c & 3) * 16384;
    const char* bufn = lds + ((c + 1) & 3) * 16384;
#pragma unroll
    for (int t = 0; t < 4; ++t) {
      const char* kb = t < 3 ? buf + (t + 1) * 2048 : bufn;
      const char* vb = buf + t * 2048 + voff;
      bf16x8 kn0 = *(const bf16x8*)(kb + koff0), kn1 = *(const bf16x8*)(kb + koff1);
      bf16x8 vc0 = *(const bf16x8*)vb, vc1 = *(const bf16x8*)(vb + 1024);
      SBAR();
      PHASE(sa, pa0, pa1, sb = MFMA32(kc0, qf[1][0], negm[1]), o[1] = MFMA32(vp0, BC8(pb0), o[1]),
            sb = MFMA32(kc1, qf[1][1], sb), o[1] = MFMA32(vp1, BC8(pb1), o[1]));
      PHASE(sb, pb0, pb1, sa = MFMA32(kn0, qf[0][0], negm[0]), o[0] = MFMA32(vc0, BC8(pa0), o[0]),
            sa = MFMA32(kn1, qf[0][1], sa), o[0] = MFMA32(vc1, BC8(pa1), o[0]));
      kc0 = kn0; kc1 = kn1; vp0 = vc0; vp1 = vc1;
    }
  }
  o[1] = MFMA32(vp0, BC8(pb0), o[1]);
  o[1] = MFMA32(vp1, BC8(pb1), o[1]);
}

DI void attn_safe(const u16* __restrict__ Kh, const u16* __restrict__ Vh, const bf16x8 (&qf)[2][2], char* lds,
                  f32x16 (&o)[2], int wv, int lane) {
  const int r32 = lane & 31, h = lane >> 5;
  const int koff0 = r32 * 64 + ((h ^ ((r32 >> 2) & 3)) << 4);
  const int koff1 = r32 * 64 + (((2 + h) ^ ((r32 >> 2) & 3)) << 4);
  const int voff = 8192 + lane * 16;
  const unsigned l0 = lds_addr(lds);
  f32x16 zero;
  float m[2] = {-INFINITY, -INFINITY};
#pragma unroll
  for (int r = 0; r < 16; ++r) { o[0][r] = 0.f; o[1][r] = 0.f; zero[r] = 0.f; }
  for (int c = 0; c < 16; ++c) {
    BARRIER();
    attn_stage(Kh, Vh, c, l0, wv, lane);
    waitv<0>();
    BARRIER();
    for (int t = 0; t < 4; ++t) {
      const char* kb = lds + t * 2048;
      bf16x8 k0 = *(const bf16x8*)(kb + koff0), k1 = *(const bf16x8*)(kb + koff1);
      bf16x8 va = *(const bf16x8*)(kb + voff), vb = *(const bf16x8*)(kb + voff + 1024);
#pragma unroll
      for (int a = 0; a < 2; ++a) {
        f32x16 s = MFMA32(k0, qf[a][0], zero);
        s = MFMA32(k1, qf[a][1], s);
        float mn = fmaxf(m[a], max16(s));
        float al = __builtin_amdgcn_exp2f(m[a] - mn);
        m[a] = mn;
#pragma unroll
        for (int r = 0; r < 16; ++r) { o[a][r] *= al; s[r] -= mn; }
        bf16x8 p0, p1;
        exp_pack(s, p0, p1);
        o[a] = MFMA32(va, p0, o[a]);
        o[a] = MFMA32(vb, p1, o[a]);
      }
    }
  }
}

DI void attn_block(int bid, const u16* __restrict__ qkvp, const float* __restrict__ gate, u16* __restrict__ Ao,
                   char* lds) {
  const int tid = threadIdx.x, lane = tid & 63, r32 = lane & 31, h = lane >> 5;
  const int wv = __builtin_amdgcn_readfirstlane(tid >> 6);
  const int head = (bid & 7) * 4 + ((bid >> 3) & 3), qb = bid >> 5;
  const u16* Qh = qkvp + (size_t)head * (S_ * 32);
  const u16* Kh = qkvp + (size_t)(H_ + head) * (S_ * 32);
  const u16* Vh = qkvp + (size_t)(2 * H_ + head) * (S_ * 32);
  const int qrow0 = qb * 256 + wv * 64 + r32;
  {
    const unsigned l0 = lds_addr(lds);
    attn_stage(Kh, Vh, 0, l0, wv, lane);
    attn_stage(Kh, Vh, 1, l0 + 16384, wv, lane);
    attn_stage(Kh, Vh, 2, l0 + 32768, wv, lane);
  }
  bf16x8 qf[2][2];
#pragma unroll
  for (int a = 0; a < 2; ++a) {
    qf[a][0] = *(const bf16x8*)(Qh + (size_t)(qrow0 + a * 32) * 32 + h * 8);
    qf[a][1] = *(const bf16x8*)(Qh + (size_t)(qrow0 + a * 32) * 32 + 16 + h * 8);
  }
  float gq[2] = {gate[qrow0], gate[qrow0 + 32]};
  asm volatile("" ::"v"(qf[0][0]), "v"(qf[0][1]), "v"(qf[1][0]), "v"(qf[1][1]), "v"(gq[0]), "v"(gq[1]));
  f32x16 o[2];
  attn_fast(Kh, Vh, qf, lds, o, wv, lane);
  bool bad = false;
#pragma unroll
  for (int a = 0; a < 2; ++a)
#pragma unroll
    for (int r = 0; r < 11; ++r) bad |= (r < 8 || h == 0) && !(fabsf(o[a][r]) < 1e30f);
  if (__syncthreads_or(bad)) attn_safe(Kh, Vh, qf, lds, o, wv, lane);
#pragma unroll
  for (int a = 0; a < 2; ++a) {
    float l = __shfl(o[a][10], r32);
    float sc = gq[a] / l;
    const int q = qrow0 + a * 32;
    char* rowb = (char*)Ao + (size_t)(q >> 4) * (18 * 1024) + (q & 15) * 16;
    const int U = head * 2 + h;
    *(u32x4*)(rowb + (U >> 2) * 1024 + (U & 3) * 256) = (u32x4){pk2(o[a][0] * sc, o[a][1] * sc), pk2(o[a][2] * sc, o[a][3] * sc),
                                                                pk2(o[a][4] * sc, o[a][5] * sc), pk2(o[a][6] * sc, o[a][7] * sc)};
    const int U2 = 64 + (head >> 2);
    if (h == 0) *(unsigned*)(rowb + (U2 >> 2) * 1024 + (U2 & 3) * 256 + (head & 3) * 4) = pk2(o[a][8] * sc, o[a][9] * sc);
  }
}

constexpr int K2_STAGE = 16384 + 18432;
DI void down_block(int bid, const u16* __restrict__ Ah, const u16* __restrict__ wd, u16* __restrict__ part,
                   char* lds) {
  const int tid = threadIdx.x, lane = tid & 63;
  const int wv = __builtin_amdgcn_readfirstlane(tid >> 6);
  const int mt_blk = (bid & 7) * 2 + ((bid >> 3) & 1), rest = bid >> 4;
  const int nt_blk = rest & 3, sp = rest >> 2;
  const int m0 = mt_blk * 128, n0 = nt_blk * 144;
  f32x4 acc[9][2] = {};
  const char* pa = (const char*)Ah + ((size_t)(mt_blk * 8) * 144 + sp * 36) * 1024;
  const u16* pb = wd + (size_t)sp * 1152 * D_ + n0;
  const unsigned l0 = lds_addr(lds);
  const unsigned aof = (unsigned)(wv * 2 * 144 * 1024 + lane * 16);
  unsigned bof[5];
#pragma unroll
  for (int i = 0; i < 5; ++i) {
    int u = (i < 4 ? i * 4 + wv : 16 + (wv & 1)) * 64 + lane;
    int st = u >> 4, kblk = st / 9, nblk = st - kblk * 9;
    int rr = ((u >> 1) & 7) ^ ((kblk & 1) << 2);
    int k = (kblk >> 2) * 32 + (rr >> 2) * 16 + (kblk & 3) * 4 + (rr & 3);
    bof[i] = (unsigned)(k * D_ + nblk * 16 + (u & 1) * 8) * 2u;
  }
  auto piece = [&](int i, int kt, unsigned buf) {
    if (i < 4) glds16(pa + kt * 2048 + ((i >> 1) * 144 + (i & 1)) * 1024, aof, buf + (wv * 4 + i) * 1024);
    else if (i < 8) glds16(pb + (size_t)kt * 64 * D_, bof[i - 4], buf + 16384 + ((i - 4) * 4 + wv) * 1024);
    else if (wv < 2) glds16(pb + (size_t)kt * 64 * D_, bof[4], buf + 16384 + (16 + wv) * 1024);
  };
  const int g = lane >> 4;
  const int aoff0 = wv * 4096 + lane * 16, aoff1 = aoff0 + 1024;
  const int boff = 16384 + g * 9 * 256 + ((lane & 15) >> 2) * 32 + (lane & 3) * 8;
  const int hlo = (g & 1) << 7, hhi = hlo ^ 128;
#pragma unroll
  for (int i = 0; i < 9; ++i) piece(i, 0, l0);
  for (int kt = 0; kt < 18; ++kt) {
    waitv<0>();
    BARRIER();
    const char* cur = lds + (kt & 1) * K2_STAGE;
    const unsigned dst = l0 + ((kt + 1) & 1) * K2_STAGE;
    const bool more = kt + 1 < 18;
    bf16x8 af[2][2];
#pragma unroll
    for (int mt = 0; mt < 2; ++mt) {
      af[0][mt] = *(const bf16x8*)(cur + aoff0 + mt * 2048);
      af[1][mt] = *(const bf16x8*)(cur + aoff1 + mt * 2048);
    }
    bf16x8 bw0 = bfrag2(cur + boff + hlo, cur + boff + hhi);
    bf16x8 bw1 = bfrag2(cur + boff + 256 + hlo, cur + boff + 256 + hhi);
#pragma unroll
    for (int q = 0; q < 18; ++q) {
      bf16x8 b = (q & 1) ? bw1 : bw0;
      if (q + 2 < 18) {
        const int q2 = q + 2, o2 = ((q2 / 9) * 36 + q2 % 9) * 256;
        bf16x8 nb = bfrag2(cur + boff + o2 + hlo, cur + boff + o2 + hhi);
        if (q & 1) bw1 = nb; else bw0 = nb;
      }
      if (q < 9 && more) piece(q, kt + 1, dst);
#pragma unroll
      for (int mt = 0; mt < 2; ++mt)
        acc[q % 9][mt] = __builtin_amdgcn_mfma_f32_16x16x32_bf16(b, af[q / 9][mt], acc[q % 9][mt], 0, 0, 0);
      SBAR();
    }
  }
  u16* dst = part + ((size_t)((sp * 64 + mt_blk * 4 + wv) * 36 + nt_blk * 9) * 64 + lane) * 8;
#pragma unroll
  for (int nt = 0; nt < 9; ++nt)
    *(u32x4*)(dst + nt * 512) = (u32x4){pk2(acc[nt][0][0], acc[nt][0][1]), pk2(acc[nt][0][2], acc[nt][0][3]),
                                        pk2(acc[nt][1][0], acc[nt][1][1]), pk2(acc[nt][1][2], acc[nt][1][3])};
}

__global__ __launch_bounds__(256, 2) void k_mid(const u16* __restrict__ qkvp, const float* __restrict__ gate,
                                                u16* __restrict__ Ao, const u16* __restrict__ Ah,
                                                const u16* __restrict__ wd, u16* __restrict__ part) {
  __shared__ __attribute__((aligned(16))) char lds[2 * K2_STAGE];
  const int bid = blockIdx.x;
  if (bid < 256) attn_block(bid, qkvp, gate, Ao, lds);
  else down_block(bid - 256, Ah, wd, part, lds);
}

constexpr int K3_STAGE = 4096 + 6144;
__global__ __launch_bounds__(256) void k_out(const u16* __restrict__ Ao, const u16* __restrict__ wo,
                                             const u16* __restrict__ part, u16* __restrict__ out) {
  __shared__ __attribute__((aligned(16))) char lds[15 * K3_STAGE];
  const int tid = threadIdx.x, lane = tid & 63;
  const int wv = __builtin_amdgcn_readfirstlane(tid >> 6), wm = wv >> 1, wn = wv & 1;
  const int nt_blk = blockIdx.x % 6, mt_blk = blockIdx.x / 6;
  const int m0 = mt_blk * 64, n0 = nt_blk * 96;
  f32x4 acc[3][2] = {};
  const char* pa = (const char*)Ao + (size_t)(mt_blk * 4 + wv) * (18 * 1024);
  const u16* pb = wo + n0;
  unsigned bofA[2], bofB[2];
#pragma unroll
  for (int i = 0; i < 2; ++i) {
    int u = (i == 0 ? wv : 4 + (wv & 1)) * 64 + lane;
    int st = u >> 4, kb = st / 6, nblk = st - kb * 6;
    int j = ((u >> 1) & 7) ^ ((kb & 1) << 2);
    int col = nblk * 16 + (u & 1) * 8;
    bofA[i] = (unsigned)(((kb >> 1) * 18 + 4 * (kb & 1) + (j & 3) + 8 * (j >> 2)) * D_ + col) * 2u;
    bofB[i] = (unsigned)(((4 * kb + (j >> 1)) * 18 + 16 + (j & 1)) * D_ + col) * 2u;
  }
  auto piece = [&](int i, int kt, unsigned buf) {
    if (i == 0) glds16(pa + kt * 1024, lane * 16u, buf + wv * 1024);
    else if (i == 1 || wv < 2) {
      const u16* bb = kt < 16 ? pb + (size_t)kt * 36 * D_ : pb + (size_t)(kt - 16) * 288 * D_;
      unsigned o = kt < 16 ? bofA[i - 1] : bofB[i - 1];
      glds16(bb, o, buf + 4096 + (i == 1 ? wv : 4 + wv) * 1024);
    }
  };
  auto waitL = [&](auto n) { if (wv < 2) waitv<9 * decltype(n)::value>(); else waitv<6 * decltype(n)::value>(); };
  auto bar = [&]() { BARRIER(); };
  const int g = lane >> 4;
  const int aoff = wm * 2048 + lane * 16;
  const int boff = 4096 + (g * 6 + wn * 3) * 256 + ((lane & 15) >> 2) * 32 + (lane & 3) * 8;
  const int hlo = (g & 1) << 7, hhi = hlo ^ 128;
  auto ldA = [&](const char* st, int mt) { return *(const bf16x8*)(st + aoff + mt * 1024); };
  auto ldB = [&](const char* st, int nt) { return bfrag2(st + boff + nt * 256 + hlo, st + boff + nt * 256 + hhi); };
  auto extra = [&](int, int, unsigned) {};
  u32x4 pv[3][4];
  auto extra0 = [&]() {
#pragma unroll
    for (int nt = 0; nt < 3; ++nt)
#pragma unroll
      for (int sp = 0; sp < 4; ++sp)
        pv[nt][sp] = __builtin_nontemporal_load(
            (const u32x4*)(part + ((size_t)((sp * 64 + mt_blk * 2 + wm) * 36 + nt_blk * 6 + wn * 3 + nt) * 64 + lane) * 8));
  };
  gemm_loop<2, 3, 3, K3_STAGE, 3, 5>(6, lds, acc, ldA, ldB, piece, waitL, extra, bar, extra0);
#pragma unroll
  for (int nt = 0; nt < 3; ++nt)
#pragma unroll
    for (int mt = 0; mt < 2; ++mt) {
      size_t off = (size_t)(m0 + wm * 32 + mt * 16 + (lane & 15)) * D_ + n0 + wn * 48 + nt * 16 + 4 * (lane >> 4);
#pragma unroll
      for (int sp = 0; sp < 4; ++sp) {
        u32x2 w = {pv[nt][sp][mt * 2], pv[nt][sp][mt * 2 + 1]};
        acc[nt][mt] += (f32x4){__uint_as_float(w[0] << 16), __uint_as_float(w[0] & 0xffff0000u), __uint_as_float(w[1] << 16),
                               __uint_as_float(w[1] & 0xffff0000u)};
      }
      u32x2 v = {pk2(acc[nt][mt][0], acc[nt][mt][1]), pk2(acc[nt][mt][2], acc[nt][mt][3])};
      *(u32x2*)(out + off) = v;
    }
}

extern "C" void kernel_launch(void* const* d_in, const int* in_sizes, int n_in, void* d_out, int out_size,
                              void* d_ws, size_t ws_size, hipStream_t stream) {
  const u16* x = (const u16*)d_in[0];
  const u16* wqkv = (const u16*)d_in[1];
  const u16* wo = (const u16*)d_in[2];
  const u16* wg = (const u16*)d_in[4];
  const u16* wu = (const u16*)d_in[5];
  const u16* wd = (const u16*)d_in[6];
  const u16* gw = (const u16*)d_in[7];
  char* ws = (char*)d_ws;
  u16* qkvp = (u16*)ws;
  float* gate = (float*)(ws + (size_t)16 * 1024 * 1024);
  u16* Ao = (u16*)(ws + (size_t)17 * 1024 * 1024);
  u16* Ah = (u16*)(ws + (size_t)20 * 1024 * 1024);
  u16* part = (u16*)(ws + (size_t)48 * 1024 * 1024);
  k_proj<<<256, 512, 0, stream>>>(x, wqkv, wg, wu, gw, qkvp, gate, Ah, wd, wo);
  k_mid<<<512, 256, 0, stream>>>(qkvp, gate, Ao, Ah, wd, part);
  k_out<<<192, 256, 0, stream>>>(Ao, wo, part, (u16*)d_out);
}
```

```cpp
#include <hip/hip_runtime.h>
#include <hip/hip_bf16.h>
#include <type_traits>

typedef __attribute__((ext_vector_type(8))) short bf16x8;
typedef __attribute__((ext_vector_type(4))) short s16x4;
typedef __attribute__((ext_vector_type(4))) float f32x4;
typedef __attribute__((ext_vector_type(2))) float f32x2;
typedef __attribute__((ext_vector_type(16))) float f32x16;
typedef __attribute__((ext_vector_type(4))) unsigned u32x4;
typedef __attribute__((ext_vector_type(2))) unsigned u32x2;
typedef __attribute__((ext_vector_type(2))) __bf16 bf16x2_t;
typedef unsigned short u16;

#define DI __device__ __forceinline__
#define LDSP(T) __attribute__((address_space(3))) T

constexpr int S_ = 2048, D_ = 576, H_ = 32, DH_ = 18, F_ = 4608;

DI unsigned pk2(float lo, float hi) {
  f32x2 v = {lo, hi};
  bf16x2_t b = __builtin_convertvector(v, bf16x2_t);
  return __builtin_bit_cast(unsigned, b);
}
DI float rbf(float x) { return __uint_as_float(pk2(x, 0.f) << 16); }

DI void glds16(const void* sbase, unsigned voff, unsigned lds_base) {
  unsigned sv;
  asm volatile("s_mov_b32 %0, m0\n\ts_mov_b32 m0, %3\n\ts_nop 0\n\tglobal_load_lds_dwordx4 %1, %2\n\ts_mov_b32 m0, %0"
               : "=&s"(sv) : "v"(voff), "s"(sbase), "s"(__builtin_amdgcn_readfirstlane(lds_base)) : "memory");
}
DI unsigned lds_addr(const char* p) { return (unsigned)(size_t)(LDSP(const char)*)p; }
DI s16x4 trread(const char* p) { return __builtin_amdgcn_ds_read_tr16_b64_v4i16((LDSP(s16x4)*)p); }
template <int N> DI void waitv() { asm volatile("s_waitcnt vmcnt(%0)" ::"n"(N) : "memory"); }
#define BARRIER() do { asm volatile("s_waitcnt lgkmcnt(0)" ::: "memory"); __builtin_amdgcn_s_barrier(); asm volatile("" ::: "memory"); } while (0)

#define SBAR() __builtin_amdgcn_sched_barrier(0)
template <int NBs>
DI unsigned bseg_off(int u, int ld) {
  int st = u >> 4, kblk = st / NBs, nblk = st - kblk * NBs;
  int rr = (u >> 1) & 7, hf = u & 1;
  int k = kblk * 8 + (rr ^ ((kblk & 1) << 2));
  return (unsigned)(k * ld + nblk * 16 + hf * 8) * 2u;
}
DI unsigned aseg_off(int u, int lda) {
  int row = u >> 2, ch = (u & 3) ^ ((-(row >> 2)) & 3);
  return (unsigned)(row * lda + ch * 8) * 2u;
}
DI bf16x8 bfrag2(const char* lo, const char* hi) {
  s16x4 a = trread(lo), b = trread(hi);
  return __builtin_shufflevector(a, b, 0, 1, 2, 3, 4, 5, 6, 7);
}

template <int N> using IC = std::integral_constant<int, N>;
template <int MT, int NT, int LMAX, int STAGE, int KS, int NR, class LdA, class LdB, class Piece, class WaitL, class Extra, class Bar, class Pre>
DI void gemm_loop(int nsup, char* lds, f32x4 (&acc)[NT][MT], LdA ldA, LdB ldB, Piece piece, WaitL waitL, Extra extra, Bar bar, Pre pre) {
  static_assert(NT >= MT + 1 && NT >= LMAX, "schedule");
  constexpr int SUP = KS * STAGE;
  const unsigned l0 = lds_addr(lds);
#pragma unroll
  for (int s = 0; s < (NR - 1) * KS; ++s)
#pragma unroll
    for (int i = 0; i < LMAX; ++i) piece(i, s, l0 + s * STAGE);
  pre();
  waitL(IC<NR - 2>{});
  bar();
  bf16x8 af[MT], bw0, bw1;
#pragma unroll
  for (int mt = 0; mt < MT; ++mt) af[mt] = ldA(lds, mt);
  bw0 = ldB(lds, 0);
  bw1 = ldB(lds, 1);
  int cb = 0;
  auto body = [&](int j, auto wn_, auto more_) {
    constexpr bool more = decltype(more_)::value;
    waitL(wn_);
    bar();
    const int nb_ = cb + 1 == NR ? 0 : cb + 1, pb_ = cb == 0 ? NR - 1 : cb - 1;
    const char* base = lds + cb * SUP;
    const char* nbase = lds + nb_ * SUP;
    const unsigned dst = l0 + pb_ * SUP;
#pragma unroll
    for (int ks = 0; ks < KS; ++ks) {
      const char* cur = base + ks * STAGE;
      const char* nx = ks + 1 < KS ? base + (ks + 1) * STAGE : nbase;
      bf16x8 an[MT];
#pragma unroll
      for (int nt = 0; nt < NT; ++nt) {
        bf16x8 b = (nt & 1) ? bw1 : bw0;
        bf16x8 nb = nt + 2 < NT ? ldB(cur, nt + 2) : ldB(nx, nt + 2 - NT);
        if (nt & 1) bw1 = nb; else bw0 = nb;
        if (nt >= NT - 1 - MT && nt < NT - 1) an[nt - (NT - 1 - MT)] = ldA(nx, nt - (NT - 1 - MT));
        if (nt < LMAX && more) piece(nt, (j + NR - 1) * KS + ks, dst + ks * STAGE);
#pragma unroll
        for (int mt = 0; mt < MT; ++mt) acc[nt][mt] = __builtin_amdgcn_mfma_f32_16x16x32_bf16(b, af[mt], acc[nt][mt], 0, 0, 0);
        if (more) extra(nt, (j + NR - 1) * KS + ks, dst + ks * STAGE - l0);
        SBAR();
      }
#pragma unroll
      for (int mt = 0; mt < MT; ++mt) af[mt] = an[mt];
      if (NT & 1) { bf16x8 t = bw0; bw0 = bw1; bw1 = t; }
    }
    cb = nb_;
  };
  for (int j = 0; j < nsup - (NR - 1); ++j) body(j, IC<NR - 3>{}, IC<1>{});
#pragma unroll 1
  for (int j = nsup - (NR - 1); j < nsup; ++j) body(j, IC<0>{}, IC<0>{});
}

constexpr float QSCALE = 0.23570226039551584f * 1.4426950408889634f;
constexpr int K1_STAGE = 16384 + 9216 + 9216 + 4096;

__global__ __launch_bounds__(512) void k_proj(const u16* __restrict__ x, const u16* __restrict__ wqkv,
                                              const u16* __restrict__ wg, const u16* __restrict__ wu,
                                              const u16* __restrict__ gate_w, u16* __restrict__ qkvp,
                                              float* __restrict__ gate, u16* __restrict__ Ah,
                                              const u16* __restrict__ wd, const u16* __restrict__ wo) {
  __shared__ __attribute__((aligned(16))) char lds[4 * K1_STAGE + 1152 + 1024];
  const int tid = threadIdx.x, lane = tid & 63;
  const int wv = __builtin_amdgcn_readfirstlane(tid >> 6), wm = wv >> 1, wn = wv & 1;
  const int bid = blockIdx.x;
  const int nt_blk = (bid & 7) * 4 + ((bid >> 3) & 3), mt_blk = bid >> 5;
  const int m0 = mt_blk * 256, f0 = nt_blk * 144;
  const int coff = nt_blk == 31 ? 2 : 0;
  const int c0 = nt_blk * 54 - coff;
  char* gwl = lds + 4 * K1_STAGE;

  f32x4 acc[11][4] = {};
  const u16* xa = x + (size_t)m0 * D_;
  const unsigned aof0 = aseg_off(wv * 64 + lane, D_), aof1 = aseg_off((8 + wv) * 64 + lane, D_);
  unsigned bof[3];
  const u16* bsrc[3];
  int bld[3];
#pragma unroll
  for (int i = 0; i < 3; ++i) {
    int jj = i * 8 + wv;
    if (jj >= 22) jj -= 6;
    if (jj < 18) {
      int j = jj < 9 ? jj : jj - 9;
      bof[i] = bseg_off<9>(j * 64 + lane, F_);
      bsrc[i] = (jj < 9 ? wg : wu) + f0;
      bld[i] = F_;
    } else {
      bof[i] = bseg_off<4>(((jj - 18) & 3) * 64 + lane, 3 * D_);
      bsrc[i] = wqkv + c0;
      bld[i] = 3 * D_;
    }
  }
  const bool padlane = wv >= 2 && wv < 6 && (lane & 1) && (lane >> 4) == 3;
  auto piece = [&](int i, int kt, unsigned buf) {
    if (i == 0) glds16(xa + kt * 32, aof0, buf + wv * 1024);
    else if (i == 1) glds16(xa + kt * 32, aof1, buf + (8 + wv) * 1024);
    else if (i < 4) glds16(bsrc[i - 2] + (size_t)kt * 32 * bld[i - 2], bof[i - 2], buf + 16384 + ((i - 2) * 8 + wv) * 1024);
    else if (!padlane) glds16(bsrc[2] + (size_t)kt * 32 * bld[2], bof[2], buf + 16384 + (16 + (wv < 6 ? wv : wv - 6)) * 1024);
  };
  const int gk = lane & 31, gkb = gk >> 3;
  const int gdst = 16384 + 18432 + (gkb * 4 + 3) * 256 + ((gk & 7) ^ ((gkb & 1) << 2)) * 32 + 16;
  auto gfill = [&](int kt, unsigned off) {
    if (wv == 6 && lane < 32) {
      u32x4 v = {(unsigned)*(const u16*)(gwl + (kt * 32 + gk) * 2), 0u, 0u, 0u};
      *(u32x4*)(lds + off + gdst) = v;
    }
  };
  auto pre = [&]() {
    if (wv == 6) {
      waitv<15>();
      gfill(0, 0); gfill(1, K1_STAGE); gfill(2, 2 * K1_STAGE);
    }
  };
  if (wv == 6) {
    glds16(gate_w, lane * 16u, lds_addr(gwl));
    if (lane < 8) glds16(gate_w + 512, lane * 16u, lds_addr(gwl) + 1024);
  }
  auto waitL = [&](auto n) { waitv<5 * decltype(n)::value>(); };
  auto bar = [&]() { BARRIER(); };
  const int g = lane >> 4;
  const int bl = ((lane & 15) >> 2) * 32 + (lane & 3) * 8;
  const int aoff = (wm * 64 + (lane & 15)) * 64 + ((g ^ ((-(lane >> 2)) & 3)) << 4);
  const int bgu = 16384 + wn * 9216 + g * 9 * 256 + bl;
  const int bqq = 16384 + 18432 + (g * 4 + wn * 2) * 256 + bl;
  const int hlo = (g & 1) << 7, hhi = hlo ^ 128;
  auto ldA = [&](const char* st, int mt) { return *(const bf16x8*)(st + aoff + mt * 1024); };
  auto ldB = [&](const char* st, int nt) {
    const char* bp = st + (nt < 9 ? bgu + nt * 256 : bqq + (nt - 9) * 256);
    return bfrag2(bp + hlo, bp + hhi);
  };
  auto extra = [&](int nt, int kt, unsigned off) {
    if (nt == 5) gfill(kt, off);
    if (nt == 7 && kt >= 8 && kt < 11) {
      const unsigned i = (unsigned)(kt - 8);
      if (wv < 7) {
        unsigned o = (unsigned)bid * 20736u + (wv * 3 + i) * 1024u + lane * 16u;
        glds16(wd, o < 5308416u ? o : 0u, lds_addr(gwl) + 1152);
      } else {
        unsigned o = (unsigned)bid * 2592u + i * 1024u + lane * 16u;
        glds16(wo, o < 663552u ? o : 0u, lds_addr(gwl) + 1152);
      }
    }
  };
  gemm_loop<4, 11, 5, K1_STAGE, 1, 4>(18, lds, acc, ldA, ldB, piece, waitL, extra, bar, pre);
  BARRIER();
  int tz = threadIdx.x;
  asm volatile("" : "+v"(tz));
  const int lz = tz & 63, gz = lz >> 4, tk = lz & 15;

  float* G = (float*)(lds + 73728);
  const int hg0 = nt_blk * 3;
  {
    char* xs = lds + wv * 9216 + lz * 8;
    if (wn == 1) {
#pragma unroll
      for (int nt = 0; nt < 9; ++nt)
#pragma unroll
        for (int m2 = 0; m2 < 2; ++m2) { f32x4 t = acc[nt][m2]; acc[nt][m2] = acc[nt][2 + m2]; acc[nt][2 + m2] = t; }
    }
#pragma unroll
    for (int nt = 0; nt < 9; ++nt)
#pragma unroll
      for (int m2 = 0; m2 < 2; ++m2) {
        f32x4 v = acc[nt][2 + m2];
        *(u32x2*)(xs + (nt * 2 + m2) * 512) = (u32x2){pk2(v[0], v[1]), pk2(v[2], v[3])};
      }
    if (wn == 1 && gz == 2) {
#pragma unroll
      for (int mt = 0; mt < 4; ++mt) {
        float gv = rbf(1.f / (1.f + __expf(-rbf(acc[10][mt][0]))));
        G[wm * 64 + mt * 16 + tk] = gv;
        if (nt_blk == 0) gate[m0 + wm * 64 + mt * 16 + tk] = gv;
      }
    }
  }
  __syncthreads();
  {
    const char* xr = lds + (wv ^ 1) * 9216 + lz * 8;
    u32x2 pv[9][2];
#pragma unroll
    for (int nt = 0; nt < 9; ++nt)
#pragma unroll
      for (int m2 = 0; m2 < 2; ++m2) pv[nt][m2] = *(const u32x2*)(xr + (nt * 2 + m2) * 512);
    float og[2];
#pragma unroll
    for (int m2 = 0; m2 < 2; ++m2) og[m2] = rbf(1.f - G[wm * 64 + (wn * 2 + m2) * 16 + tk]);
    asm volatile("s_waitcnt lgkmcnt(0)" ::: "memory");
    __builtin_amdgcn_s_barrier();
#pragma unroll
    for (int nt = 0; nt < 9; ++nt)
#pragma unroll
      for (int m2 = 0; m2 < 2; ++m2) {
        f32x4 v = acc[nt][m2];
        u32x2 uv = pv[nt][m2];
        float h0 = v[0] * og[m2] * __uint_as_float(uv[0] << 16);
        float h1 = v[1] * og[m2] * __uint_as_float(uv[0] & 0xffff0000u);
        float h2 = v[2] * og[m2] * __uint_as_float(uv[1] << 16);
        float h3 = v[3] * og[m2] * __uint_as_float(uv[1] & 0xffff0000u);
        pv[nt][m2] = (u32x2){pk2(h0, h1), pk2(h2, h3)};
      }
#pragma unroll
    for (int m2 = 0; m2 < 2; ++m2) {
      char* hb = (char*)Ah + ((size_t)((mt_blk * 16 + wm * 4 + wn * 2 + m2) * 144 + ((nt_blk * 9) >> 1)) * 64 + lz) * 16;
      if ((nt_blk & 1) == 0) {
#pragma unroll
        for (int i = 0; i < 4; ++i)
          *(u32x4*)(hb + i * 1024) = (u32x4){pv[2 * i][m2][0], pv[2 * i][m2][1], pv[2 * i + 1][m2][0], pv[2 * i + 1][m2][1]};
        *(u32x2*)(hb + 4096) = pv[8][m2];
      } else {
        *(u32x2*)(hb + 8) = pv[0][m2];
#pragma unroll
        for (int i = 0; i < 4; ++i)
          *(u32x4*)(hb + (i + 1) * 1024) =
              (u32x4){pv[2 * i + 1][m2][0], pv[2 * i + 1][m2][1], pv[2 * i + 2][m2][0], pv[2 * i + 2][m2][1]};
      }
    }
  }
  {
    char* img = lds;
#pragma unroll
    for (int t2 = 0; t2 < 2; ++t2)
#pragma unroll
      for (int r = 0; r < 4; r += 2) {
        int c = wn * 32 + t2 * 16 + 4 * gz + r - coff;
        if (c >= 0 && c < 54) {
          int hl = c / 18, d = c - hl * 18;
          int which = (hg0 + hl) >> 5;
          float sc = which == 0 ? QSCALE : 1.f;
#pragma unroll
          for (int mt = 0; mt < 4; ++mt) {
            int tok = wm * 64 + mt * 16 + tk;
            unsigned v = pk2(acc[9 + t2][mt][r] * sc, acc[9 + t2][mt][r + 1] * sc);
            if (which == 2) {
              char* q = img + hl * 16384 + (tok >> 5) * 2048 + ((((tok >> 4) & 1) * 2 + ((tok >> 2) & 1)) * 32 + d) * 16 +
                        ((tok & 3) + 4 * ((tok >> 3) & 1)) * 2;
              *(u16*)q = (u16)v;
              *(u16*)(q + 16) = (u16)(v >> 16);
            } else {
              int ch = d >> 3;
              if (which == 1) ch ^= (tok >> 2) & 3;
              *(unsigned*)(img + (hl * 256 + tok) * 64 + ch * 16 + (d & 7) * 2) = v;
            }
          }
        }
      }
    for (int i = tz; i < 768; i += 512) {
      int which = (hg0 + (i >> 8)) >> 5;
      if (which == 2) {
        char* grp = img + (i >> 8) * 16384 + ((i & 255) >> 3) * 512;
        int d0 = 18 + (i & 7) * 2;
        unsigned one = d0 == 18 ? 0x3f803f80u : 0u;
        if (d0 < 32) {
          *(u32x4*)(grp + d0 * 16) = (u32x4){one, one, one, one};
          *(u32x4*)(grp + d0 * 16 + 16) = (u32x4){0u, 0u, 0u, 0u};
        }
      } else {
        int sw = which == 1 ? (i >> 2) & 3 : 0;
        char* row = img + i * 64;
        *(unsigned*)(row + (2 ^ sw) * 16 + 4) = 0u;
        *(u32x2*)(row + (2 ^ sw) * 16 + 8) = (u32x2){0u, 0u};
        *(u32x4*)(row + (3 ^ sw) * 16) = (u32x4){0u, 0u, 0u, 0u};
      }
    }
  }
  __syncthreads();
#pragma unroll
  for (int i = 0; i < 6; ++i) {
    int u = i * 512 + tz;
    int hl = u >> 10, rem = u & 1023;
    u32x4 v = *(const u32x4*)(lds + u * 16);
    *(u32x4*)(qkvp + ((size_t)(nt_blk * 3 + hl) * S_ + m0) * 32 + rem * 8) = v;
  }
}

DI void attn_stage(const u16* __restrict__ Kh, const u16* __restrict__ Vh, int c, unsigned buf, int wv, int lane) {
#pragma unroll
  for (int i = 0; i < 2; ++i) glds16(Kh + (size_t)c * 4096, ((i * 4 + wv) * 64 + lane) * 16u, buf + (i * 4 + wv) * 1024);
#pragma unroll
  for (int i = 0; i < 2; ++i)
    glds16(Vh + (size_t)c * 4096, ((i * 4 + wv) * 64 + lane) * 16u, buf + 8192 + (i * 4 + wv) * 1024);
}

DI float halfswap_max(float m) {
  auto rr = __builtin_amdgcn_permlane32_swap(__float_as_uint(m), __float_as_uint(m), false, false);
  return fmaxf(__uint_as_float(rr[0]), __uint_as_float(rr[1]));
}
DI float max16(const f32x16& s) {
  float mx = s[0];
#pragma unroll
  for (int r = 1; r < 16; ++r) mx = fmaxf(mx, s[r]);
  return halfswap_max(mx);
}
#define MFMA32(a, b, c) __builtin_amdgcn_mfma_f32_32x32x16_bf16((a), (b), (c), 0, 0, 0)

DI void exp_pack(const f32x16& s, bf16x8& p0, bf16x8& p1) {
  u32x4 a, b;
#pragma unroll
  for (int r = 0; r < 4; ++r) a[r] = pk2(__builtin_amdgcn_exp2f(s[2 * r]), __builtin_amdgcn_exp2f(s[2 * r + 1]));
#pragma unroll
  for (int r = 0; r < 4; ++r) b[r] = pk2(__builtin_amdgcn_exp2f(s[8 + 2 * r]), __builtin_amdgcn_exp2f(s[9 + 2 * r]));
  p0 = __builtin_bit_cast(bf16x8, a);
  p1 = __builtin_bit_cast(bf16x8, b);
}

#define EX2(x) __builtin_amdgcn_exp2f(x)
#define PHASE(S, PA, PB, M1, M2, M3, M4)                                              \
  do {                                                                                \
    M1; SBAR(); PA[0] = pk2(EX2(S[0]), EX2(S[1])); PA[1] = pk2(EX2(S[2]), EX2(S[3])); SBAR();  \
    M2; SBAR(); PA[2] = pk2(EX2(S[4]), EX2(S[5])); PA[3] = pk2(EX2(S[6]), EX2(S[7])); SBAR();  \
    M3; SBAR(); PB[0] = pk2(EX2(S[8]), EX2(S[9])); PB[1] = pk2(EX2(S[10]), EX2(S[11])); SBAR(); \
    M4; SBAR(); PB[2] = pk2(EX2(S[12]), EX2(S[13])); PB[3] = pk2(EX2(S[14]), EX2(S[15])); SBAR(); \
  } while (0)
#define BC8(x) __builtin_bit_cast(bf16x8, x)
DI void attn_fast(const u16* __restrict__ Kh, const u16* __restrict__ Vh, const bf16x8 (&qf)[2][2], char* lds,
                  f32x16 (&o)[2], int wv, int lane) {
  const int r32 = lane & 31, h = lane >> 5;
  const int koff0 = r32 * 64 + ((h ^ ((r32 >> 2) & 3)) << 4);
  const int koff1 = r32 * 64 + (((2 + h) ^ ((r32 >> 2) & 3)) << 4);
  const int voff = 8192 + lane * 16;
  const unsigned l0 = lds_addr(lds);
  f32x16 negm[2], sa, sb;
#pragma unroll
  for (int a = 0; a < 2; ++a)
#pragma unroll
    for (int r = 0; r < 16; ++r) { o[a][r] = 0.f; negm[a][r] = 0.f; }
  waitv<8>();
  BARRIER();
  bf16x8 kc0 = *(const bf16x8*)(lds + koff0), kc1 = *(const bf16x8*)(lds + koff1);
  {
#pragma unroll
    for (int a = 0; a < 2; ++a) {
      f32x16 s = MFMA32(kc0, qf[a][0], negm[a]);
      s = MFMA32(kc1, qf[a][1], s);
      float mx = max16(s);
#pragma unroll
      for (int r = 0; r < 16; ++r) { negm[a][r] = -mx; if (a == 0) sa[r] = s[r] - mx; }
    }
  }
  u32x4 pa0, pa1, pb0 = {0u, 0u, 0u, 0u}, pb1 = {0u, 0u, 0u, 0u};
  bf16x8 vp0 = {0, 0, 0, 0, 0, 0, 0, 0}, vp1 = {0, 0, 0, 0, 0, 0, 0, 0};
  for (int c = 0; c < 16; ++c) {
    if (c + 2 < 16) waitv<4>(); else waitv<0>();
    BARRIER();
    if (c + 3 < 16) attn_stage(Kh, Vh, c + 3, l0 + ((c + 3) & 3) * 16384, wv, lane);
    const char* buf = lds + (c & 3) * 16384;
    const char* bufn = lds + ((c + 1) & 3) * 16384;
#pragma unroll
    for (int t = 0; t < 4; ++t) {
      const char* kb = t < 3 ? buf + (t + 1) * 2048 : bufn;
      const char* vb = buf + t * 2048 + voff;
      bf16x8 kn0 = *(const bf16x8*)(kb + koff0), kn1 = *(const bf16x8*)(kb + koff1);
      bf16x8 vc0 = *(const bf16x8*)vb, vc1 = *(const bf16x8*)(vb + 1024);
      SBAR();
      PHASE(sa, pa0, pa1, sb = MFMA32(kc0, qf[1][0], negm[1]), o[1] = MFMA32(vp0, BC8(pb0), o[1]),
            sb = MFMA32(kc1, qf[1][1], sb), o[1] = MFMA32(vp1, BC8(pb1), o[1]));
      PHASE(sb, pb0, pb1, sa = MFMA32(kn0, qf[0][0], negm[0]), o[0] = MFMA32(vc0, BC8(pa0), o[0]),
            sa = MFMA32(kn1, qf[0][1], sa), o[0] = MFMA32(vc1, BC8(pa1), o[0]));
      kc0 = kn0; kc1 = kn1; vp0 = vc0; vp1 = vc1;
    }
  }
  o[1] = MFMA32(vp0, BC8(pb0), o[1]);
  o[1] = MFMA32(vp1, BC8(pb1), o[1]);
}

DI void attn_safe(const u16* __restrict__ Kh, const u16* __restrict__ Vh, const bf16x8 (&qf)[2][2], char* lds,
                  f32x16 (&o)[2], int wv, int lane) {
  const int r32 = lane & 31, h = lane >> 5;
  const int koff0 = r32 * 64 + ((h ^ ((r32 >> 2) & 3)) << 4);
  const int koff1 = r32 * 64 + (((2 + h) ^ ((r32 >> 2) & 3)) << 4);
  const int voff = 8192 + lane * 16;
  const unsigned l0 = lds_addr(lds);
  f32x16 zero;
  float m[2] = {-INFINITY, -INFINITY};
#pragma unroll
  for (int r = 0; r < 16; ++r) { o[0][r] = 0.f; o[1][r] = 0.f; zero[r] = 0.f; }
  for (int c = 0; c < 16; ++c) {
    BARRIER();
    attn_stage(Kh, Vh, c, l0, wv, lane);
    waitv<0>();
    BARRIER();
    for (int t = 0; t < 4; ++t) {
      const char* kb = lds + t * 2048;
      bf16x8 k0 = *(const bf16x8*)(kb + koff0), k1 = *(const bf16x8*)(kb + koff1);
      bf16x8 va = *(const bf16x8*)(kb + voff), vb = *(const bf16x8*)(kb + voff + 1024);
#pragma unroll
      for (int a = 0; a < 2; ++a) {
        f32x16 s = MFMA32(k0, qf[a][0], zero);
        s = MFMA32(k1, qf[a][1], s);
        float mn = fmaxf(m[a], max16(s));
        float al = __builtin_amdgcn_exp2f(m[a] - mn);
        m[a] = mn;
#pragma unroll
        for (int r = 0; r < 16; ++r) { o[a][r] *= al; s[r] -= mn; }
        bf16x8 p0, p1;
        exp_pack(s, p0, p1);
        o[a] = MFMA32(va, p0, o[a]);
        o[a] = MFMA32(vb, p1, o[a]);
      }
    }
  }
}

DI void attn_block(int bid, const u16* __restrict__ qkvp, const float* __restrict__ gate, u16* __restrict__ Ao,
                   char* lds) {
  const int tid = threadIdx.x, lane = tid & 63, r32 = lane & 31, h = lane >> 5;
  const int wv = __builtin_amdgcn_readfirstlane(tid >> 6);
  const int head = (bid & 7) * 4 + ((bid >> 3) & 3), qb = bid >> 5;
  const u16* Qh = qkvp + (size_t)head * (S_ * 32);
  const u16* Kh = qkvp + (size_t)(H_ + head) * (S_ * 32);
  const u16* Vh = qkvp + (size_t)(2 * H_ + head) * (S_ * 32);
  const int qrow0 = qb * 256 + wv * 64 + r32;
  {
    const unsigned l0 = lds_addr(lds);
    attn_stage(Kh, Vh, 0, l0, wv, lane);
    attn_stage(Kh, Vh, 1, l0 + 16384, wv, lane);
    attn_stage(Kh, Vh, 2, l0 + 32768, wv, lane);
  }
  bf16x8 qf[2][2];
#pragma unroll
  for (int a = 0; a < 2; ++a) {
    qf[a][0] = *(const bf16x8*)(Qh + (size_t)(qrow0 + a * 32) * 32 + h * 8);
    qf[a][1] = *(const bf16x8*)(Qh + (size_t)(qrow0 + a * 32) * 32 + 16 + h * 8);
  }
  float gq[2] = {gate[qrow0], gate[qrow0 + 32]};
  asm volatile("" ::"v"(qf[0][0]), "v"(qf[0][1]), "v"(qf[1][0]), "v"(qf[1][1]), "v"(gq[0]), "v"(gq[1]));
  f32x16 o[2];
  attn_fast(Kh, Vh, qf, lds, o, wv, lane);
  bool bad = false;
#pragma unroll
  for (int a = 0; a < 2; ++a)
#pragma unroll
    for (int r = 0; r < 11; ++r) bad |= (r < 8 || h == 0) && !(fabsf(o[a][r]) < 1e30f);
  if (__syncthreads_or(bad)) attn_safe(Kh, Vh, qf, lds, o, wv, lane);
#pragma unroll
  for (int a = 0; a < 2; ++a) {
    float l = __shfl(o[a][10], r32);
    float sc = gq[a] / l;
    const int q = qrow0 + a * 32;
    char* rowb = (char*)Ao + (size_t)(q >> 4) * (18 * 1024) + (q & 15) * 16;
    const int U = head * 2 + h;
    *(u32x4*)(rowb + (U >> 2) * 1024 + (U & 3) * 256) = (u32x4){pk2(o[a][0] * sc, o[a][1] * sc), pk2(o[a][2] * sc, o[a][3] * sc),
                                                                pk2(o[a][4] * sc, o[a][5] * sc), pk2(o[a][6] * sc, o[a][7] * sc)};
    const int U2 = 64 + (head >> 2);
    if (h == 0) *(unsigned*)(rowb + (U2 >> 2) * 1024 + (U2 & 3) * 256 + (head & 3) * 4) = pk2(o[a][8] * sc, o[a][9] * sc);
  }
}

constexpr int K2_STAGE = 16384 + 18432;
DI void down_block(int bid, const u16* __restrict__ Ah, const u16* __restrict__ wd, u16* __restrict__ part,
                   char* lds) {
  const int tid = threadIdx.x, lane = tid & 63;
  const int wv = __builtin_amdgcn_readfirstlane(tid >> 6);
  const int mt_blk = (bid & 7) * 2 + ((bid >> 3) & 1), rest = bid >> 4;
  const int nt_blk = rest & 3, sp = rest >> 2;
  const int m0 = mt_blk * 128, n0 = nt_blk * 144;
  f32x4 acc[9][2] = {};
  const char* pa = (const char*)Ah + ((size_t)(mt_blk * 8) * 144 + sp * 36) * 1024;
  const u16* pb = wd + (size_t)sp * 1152 * D_ + n0;
  const unsigned l0 = lds_addr(lds);
  const unsigned aof = (unsigned)(wv * 2 * 144 * 1024 + lane * 16);
  unsigned bof[5];
#pragma unroll
  for (int i = 0; i < 5; ++i) {
    int u = (i < 4 ? i * 4 + wv : 16 + (wv & 1)) * 64 + lane;
    int st = u >> 4, kblk = st / 9, nblk = st - kblk * 9;
    int rr = ((u >> 1) & 7) ^ ((kblk & 1) << 2);
    int k = (kblk >> 2) * 32 + (rr >> 2) * 16 + (kblk & 3) * 4 + (rr & 3);
    bof[i] = (unsigned)(k * D_ + nblk * 16 + (u & 1) * 8) * 2u;
  }
  auto piece = [&](int i, int kt, unsigned buf) {
    if (i < 4) glds16(pa + kt * 2048 + ((i >> 1) * 144 + (i & 1)) * 1024, aof, buf + (wv * 4 + i) * 1024);
    else if (i < 8) glds16(pb + (size_t)kt * 64 * D_, bof[i - 4], buf + 16384 + ((i - 4) * 4 + wv) * 1024);
    else if (wv < 2) glds16(pb + (size_t)kt * 64 * D_, bof[4], buf + 16384 + (16 + wv) * 1024);
  };
  const int g = lane >> 4;
  const int aoff0 = wv * 4096 + lane * 16, aoff1 = aoff0 + 1024;
  const int boff = 16384 + g * 9 * 256 + ((lane & 15) >> 2) * 32 + (lane & 3) * 8;
  const int hlo = (g & 1) << 7, hhi = hlo ^ 128;
#pragma unroll
  for (int i = 0; i < 9; ++i) piece(i, 0, l0);
  for (int kt = 0; kt < 18; ++kt) {
    waitv<0>();
    BARRIER();
    const char* cur = lds + (kt & 1) * K2_STAGE;
    const unsigned dst = l0 + ((kt + 1) & 1) * K2_STAGE;
    const bool more = kt + 1 < 18;
    bf16x8 af[2][2];
#pragma unroll
    for (int mt = 0; mt < 2; ++mt) {
      af[0][mt] = *(const bf16x8*)(cur + aoff0 + mt * 2048);
      af[1][mt] = *(const bf16x8*)(cur + aoff1 + mt * 2048);
    }
    bf16x8 bw0 = bfrag2(cur + boff + hlo, cur + boff + hhi);
    bf16x8 bw1 = bfrag2(cur + boff + 256 + hlo, cur + boff + 256 + hhi);
#pragma unroll
    for (int q = 0; q < 18; ++q) {
      bf16x8 b = (q & 1) ? bw1 : bw0;
      if (q + 2 < 18) {
        const int q2 = q + 2, o2 = ((q2 / 9) * 36 + q2 % 9) * 256;
        bf16x8 nb = bfrag2(cur + boff + o2 + hlo, cur + boff + o2 + hhi);
        if (q & 1) bw1 = nb; else bw0 = nb;
      }
      if (q < 9 && more) piece(q, kt + 1, dst);
#pragma unroll
      for (int mt = 0; mt < 2; ++mt)
        acc[q % 9][mt] = __builtin_amdgcn_mfma_f32_16x16x32_bf16(b, af[q / 9][mt], acc[q % 9][mt], 0, 0, 0);
      SBAR();
    }
  }
  u16* dst = part + ((size_t)((sp * 64 + mt_blk * 4 + wv) * 36 + nt_blk * 9) * 64 + lane) * 8;
#pragma unroll
  for (int nt = 0; nt < 9; ++nt)
    *(u32x4*)(dst + nt * 512) = (u32x4){pk2(acc[nt][0][0], acc[nt][0][1]), pk2(acc[nt][0][2], acc[nt][0][3]),
                                        pk2(acc[nt][1][0], acc[nt][1][1]), pk2(acc[nt][1][2], acc[nt][1][3])};
}

__global__ __launch_bounds__(256, 2) void k_mid(const u16* __restrict__ qkvp, const float* __restrict__ gate,
                                                u16* __restrict__ Ao, const u16* __restrict__ Ah,
                                                const u16* __restrict__ wd, u16* __restrict__ part) {
  __shared__ __attribute__((aligned(16))) char lds[2 * K2_STAGE];
  const int bid = blockIdx.x;
  if (bid < 256) attn_block(bid, qkvp, gate, Ao, lds);
  else down_block(bid - 256, Ah, wd, part, lds);
}

constexpr int K3_STAGE = 4096 + 6144;
__global__ __launch_bounds__(256) void k_out(const u16* __restrict__ Ao, const u16* __restrict__ wo,
                                             const u16* __restrict__ part, u16* __restrict__ out) {
  __shared__ __attribute__((aligned(16))) char lds[15 * K3_STAGE];
  const int tid = threadIdx.x, lane = tid & 63;
  const int wv = __builtin_amdgcn_readfirstlane(tid >> 6), wm = wv >> 1, wn = wv & 1;
  const int nt_blk = blockIdx.x % 6, mt_blk = blockIdx.x / 6;
  const int m0 = mt_blk * 64, n0 = nt_blk * 96;
  f32x4 acc[3][2] = {};
  const char* pa = (const char*)Ao + (size_t)(mt_blk * 4 + wv) * (18 * 1024);
  const u16* pb = wo + n0;
  unsigned bofA[2], bofB[2];
#pragma unroll
  for (int i = 0; i < 2; ++i) {
    int u = (i == 0 ? wv : 4 + (wv & 1)) * 64 + lane;
    int st = u >> 4, kb = st / 6, nblk = st - kb * 6;
    int j = ((u >> 1) & 7) ^ ((kb & 1) << 2);
    int col = nblk * 16 + (u & 1) * 8;
    bofA[i] = (unsigned)(((kb >> 1) * 18 + 4 * (kb & 1) + (j & 3) + 8 * (j >> 2)) * D_ + col) * 2u;
    bofB[i] = (unsigned)(((4 * kb + (j >> 1)) * 18 + 16 + (j & 1)) * D_ + col) * 2u;
  }
  auto piece = [&](int i, int kt, unsigned buf) {
    if (i == 0) glds16(pa + kt * 1024, lane * 16u, buf + wv * 1024);
    else if (i == 1 || wv < 2) {
      const u16* bb = kt < 16 ? pb + (size_t)kt * 36 * D_ : pb + (size_t)(kt - 16) * 288 * D_;
      unsigned o = kt < 16 ? bofA[i - 1] : bofB[i - 1];
      glds16(bb, o, buf + 4096 + (i == 1 ? wv : 4 + wv) * 1024);
    }
  };
  auto waitL = [&](auto n) { if (wv < 2) waitv<9 * decltype(n)::value>(); else waitv<6 * decltype(n)::value>(); };
  auto bar = [&]() { BARRIER(); };
  const int g = lane >> 4;
  const int aoff = wm * 2048 + lane * 16;
  const int boff = 4096 + (g * 6 + wn * 3) * 256 + ((lane & 15) >> 2) * 32 + (lane & 3) * 8;
  const int hlo = (g & 1) << 7, hhi = hlo ^ 128;
  auto ldA = [&](const char* st, int mt) { return *(const bf16x8*)(st + aoff + mt * 1024); };
  auto ldB = [&](const char* st, int nt) { return bfrag2(st + boff + nt * 256 + hlo, st + boff + nt * 256 + hhi); };
  auto extra = [&](int, int, unsigned) {};
  u32x4 pv[3][4];
  auto extra0 = [&]() {
#pragma unroll
    for (int nt = 0; nt < 3; ++nt)
#pragma unroll
      for (int sp = 0; sp < 4; ++sp)
        pv[nt][sp] = __builtin_nontemporal_load(
            (const u32x4*)(part + ((size_t)((sp * 64 + mt_blk * 2 + wm) * 36 + nt_blk * 6 + wn * 3 + nt) * 64 + lane) * 8));
  };
  gemm_loop<2, 3, 3, K3_STAGE, 3, 5>(6, lds, acc, ldA, ldB, piece, waitL, extra, bar, extra0);
#pragma unroll
  for (int nt = 0; nt < 3; ++nt)
#pragma unroll
    for (int mt = 0; mt < 2; ++mt) {
      size_t off = (size_t)(m0 + wm * 32 + mt * 16 + (lane & 15)) * D_ + n0 + wn * 48 + nt * 16 + 4 * (lane >> 4);
#pragma unroll
      for (int sp = 0; sp < 4; ++sp) {
        u32x2 w = {pv[nt][sp][mt * 2], pv[nt][sp][mt * 2 + 1]};
        acc[nt][mt] += (f32x4){__uint_as_float(w[0] << 16), __uint_as_float(w[0] & 0xffff0000u), __uint_as_float(w[1] << 16),
                               __uint_as_float(w[1] & 0xffff0000u)};
      }
      u32x2 v = {pk2(acc[nt][mt][0], acc[nt][mt][1]), pk2(acc[nt][mt][2], acc[nt][mt][3])};
      *(u32x2*)(out + off) = v;
    }
}

extern "C" void kernel_launch(void* const* d_in, const int* in_sizes, int n_in, void* d_out, int out_size,
                              void* d_ws, size_t ws_size, hipStream_t stream) {
  const u16* x = (const u16*)d_in[0];
  const u16* wqkv = (const u16*)d_in[1];
  const u16* wo = (const u16*)d_in[2];
  const u16* wg = (const u16*)d_in[4];
  const u16* wu = (const u16*)d_in[5];
  const u16* wd = (const u16*)d_in[6];
  const u16* gw = (const u16*)d_in[7];
  char* ws = (char*)d_ws;
  u16* qkvp = (u16*)ws;
  float* gate = (float*)(ws + (size_t)16 * 1024 * 1024);
  u16* Ao = (u16*)(ws + (size_t)17 * 1024 * 1024);
  u16* Ah = (u16*)(ws + (size_t)20 * 1024 * 1024);
  u16* part = (u16*)(ws + (size_t)48 * 1024 * 1024);
  k_proj<<<256, 512, 0, stream>>>(x, wqkv, wg, wu, gw, qkvp, gate, Ah, wd, wo);
  k_mid<<<512, 256, 0, stream>>>(qkvp, gate, Ao, Ah, wd, part);
  k_out<<<192, 256, 0, stream>>>(Ao, wo, part, (u16*)d_out);
}
```
